```python
import torch

def _softmax(x, axis=-1):
    return torch.softmax(x, dim=axis)
def _gelu(x):
    return torch.nn.functional.gelu(x, approximate="tanh")
def _silu(x):
    return torch.nn.functional.silu(x)
def _sigmoid(x):
    return torch.sigmoid(x)
def _layernorm(x, axis=-1, eps=1e-5):
    m = x.mean(dim=axis, keepdim=True)
    v = x.var(dim=axis, keepdim=True, unbiased=False)
    return (x - m) / torch.sqrt(v + eps)
def _scatter_add(tgt, idx, src, axis=0):
    return tgt.clone().index_add_(axis, idx, src)
def _segment_sum(src, seg, n):
    out = torch.zeros((n,) + src.shape[1:], dtype=src.dtype)
    return out.index_add_(0, seg, src)
def _pad(x, pw):
    flat = tuple(v for pair in reversed(pw) for v in pair)
    return torch.nn.functional.pad(x, flat)

kernel_name = 'interleave_layers__sliding_block_cached__mixer_token_mlp__decode'

def setup_inputs():

    _w = (lambda a: (lambda t: t.to(torch.bfloat16) if t.is_floating_point() else t)(torch.as_tensor(a)))
    def _A_setup():
        B, Sq, D = 4, 1, 512
        Hq, Hkv, Sk, Dh = 32, 8, 16384, 128
        assert Hq % Hkv == 0, (Hq, Hkv)
        return {
            "x": _w((lambda *s: torch.randn(*s, dtype=torch.bfloat16) * 0.05)(B, Sq, D)),
            "k_cache": _w((lambda *s: torch.randn(*s, dtype=torch.bfloat16) * 0.8)(B, Hkv, Sk, Dh)),
            "v_cache": _w((lambda *s: torch.randn(*s, dtype=torch.bfloat16) * 1.6)(B, Hkv, Sk, Dh)),
            "wq": _w((lambda *s: torch.randn(*s, dtype=torch.bfloat16) * 0.8)(D, Hq * Dh) * (D ** -0.5)),
            "wo": _w((lambda *s: torch.randn(*s, dtype=torch.bfloat16) * 1.6)(Hq * Dh, D) * ((Hq * Dh) ** -0.5)),
        }
    _A_in = _A_setup()

    def _B_setup():
        B, S, D, F = 4, 1, 512, 1792
        return {
            "x": _w((lambda *s: torch.randn(*s, dtype=torch.bfloat16) * 0.05)(B, S, D)),
            "w1": _w((lambda *s: torch.randn(*s, dtype=torch.bfloat16) * 0.8)(S, F) * (S ** -0.5)),
            "b1": _w((lambda *s: torch.randn(*s, dtype=torch.bfloat16) * 0.2)(F) * 0.02),
            "w2": _w((lambda *s: torch.randn(*s, dtype=torch.bfloat16) * 0.2)(F, S) * (F ** -0.5)),
            "b2": _w((lambda *s: torch.randn(*s, dtype=torch.bfloat16) * 0.2)(S) * 0.02),
        }
    _B_in = _B_setup()

    return {
        "A_x": _A_in["x"],
        "A_k_cache": _A_in["k_cache"],
        "A_v_cache": _A_in["v_cache"],
        "A_wq": _A_in["wq"],
        "A_wo": _A_in["wo"],
        "B_x": _B_in["x"],
        "B_w1": _B_in["w1"],
        "B_b1": _B_in["b1"],
        "B_w2": _B_in["w2"],
        "B_b2": _B_in["b2"],
    }

def reference(A_x, A_k_cache, A_v_cache, A_wq, A_wo, B_x, B_w1, B_b1, B_w2, B_b2):
    def _A_ref(x, k_cache, v_cache, wq, wo):
        B, Sq, D = x.shape
        Hq, Dh = 32, 128
        Hkv, Sk = k_cache.shape[1], k_cache.shape[2]
        W = min(4096, max(256, Sk // 4))
        rep = Hq // Hkv
        q = (lambda x, *d: x.permute(*d))((x @ wq).reshape(B, Sq, Hq, Dh), 0, 2, 1, 3)
        k = (lambda x, n, axis: torch.repeat_interleave(x, n, dim=axis))(k_cache[:, :, Sk - W:, :], rep, axis=1)
        v = (lambda x, n, axis: torch.repeat_interleave(x, n, dim=axis))(v_cache[:, :, Sk - W:, :], rep, axis=1)
        scores = torch.einsum("bhqd,bhkd->bhqk", q, k) * (Dh ** -0.5)
        p = _softmax(scores, axis=-1)
        o = torch.einsum("bhqk,bhkd->bhqd", p, v)
        return (lambda x, *d: x.permute(*d))(o, 0, 2, 1, 3).reshape(B, Sq, Hq * Dh) @ wo

    def _B_ref(x, w1, b1, w2, b2):
        B, S, D = x.shape
        h = _layernorm(x, axis=-1)
        u = torch.einsum("bsd,sf->bdf", h, w1) + b1
        u = _gelu(u)
        y = torch.einsum("bdf,fs->bds", u, w2) + b2
        return (lambda x, *d: x.permute(*d))(y, 0, 2, 1)

    x = A_x
    x = x + _A_ref(_layernorm(x, axis=-1), A_k_cache, A_v_cache, A_wq, A_wo)
    x = x + _B_ref(_layernorm(x, axis=-1), B_w1, B_b1, B_w2, B_b2)
    x = x + _A_ref(_layernorm(x, axis=-1), A_k_cache, A_v_cache, A_wq, A_wo)
    x = x + _B_ref(_layernorm(x, axis=-1), B_w1, B_b1, B_w2, B_b2)
    return x
```

```cpp
#include <hip/hip_runtime.h>
#include <hip/hip_bf16.h>
#include <stdint.h>

typedef unsigned short u16;
__device__ __forceinline__ float bf2f(u16 b){ return __uint_as_float(((unsigned)b)<<16); }
__device__ __forceinline__ u16 f2bf(float f){ unsigned u=__float_as_uint(f); u += 0x7FFFu + ((u>>16)&1u); return (u16)(u>>16); }

constexpr int B=4,D=512,HQ=32,HKV=8,SK=16384,DH=128,W=4096,F=1792;

__device__ float block_sum(float v, float* red){
  for(int o=32;o>0;o>>=1) v+=__shfl_xor(v,o);
  int w=threadIdx.x>>6;
  __syncthreads();
  if((threadIdx.x&63)==0) red[w]=v;
  __syncthreads();
  float s=0; for(int i=0;i<(int)(blockDim.x>>6);++i) s+=red[i];
  return s;
}

__global__ void k_init(const u16* x, float* xf){ int i=blockIdx.x*blockDim.x+threadIdx.x; if(i<B*D) xf[i]=bf2f(x[i]); }

__global__ void k_q(const float* xf, const u16* wq, float* q){
  __shared__ float h[D]; __shared__ float red[8]; __shared__ float part[4][64];
  int b=blockIdx.y; int t=threadIdx.x;
  float a0=xf[b*D+t], a1=xf[b*D+t+256];
  float m=block_sum(a0+a1,red)/D;
  float v=block_sum((a0-m)*(a0-m)+(a1-m)*(a1-m),red)/D;
  float r=1.0f/sqrtf(v+1e-5f);
  h[t]=(a0-m)*r; h[t+256]=(a1-m)*r;
  __syncthreads();
  int c=blockIdx.x*64+(t&63); int kq=t>>6;
  float acc=0;
  for(int k=kq*128;k<kq*128+128;++k) acc+=h[k]*bf2f(wq[(size_t)k*4096+c]);
  part[kq][t&63]=acc; __syncthreads();
  if(t<64) q[b*4096+c]=part[0][t]+part[1][t]+part[2][t]+part[3][t];
}
__global__ void k_attn(const float* q, const u16* K, const u16* V, float* po, float* pm, float* pl){
  __shared__ float qs[4][128]; __shared__ float sc[4][512]; __shared__ float red[8];
  int s=blockIdx.x,h=blockIdx.y,b=blockIdx.z,t=threadIdx.x;
  for(int i=t;i<512;i+=256) qs[i>>7][i&127]=q[b*4096+h*512+i];
  __syncthreads();
  const u16* Kp=K+((size_t)(b*HKV+h)*SK+(SK-W)+s*512)*DH;
  const u16* Vp=V+((size_t)(b*HKV+h)*SK+(SK-W)+s*512)*DH;
  for(int j=t;j<512;j+=256){
    float a[4]={0,0,0,0};
    for(int d=0;d<128;++d){ float kv=bf2f(Kp[(size_t)j*DH+d]); for(int g=0;g<4;++g) a[g]+=kv*qs[g][d]; }
    for(int g=0;g<4;++g) sc[g][j]=a[g]*0.08838834764831845f;
  }
  __syncthreads();
  float mx[4],l[4];
  for(int g=0;g<4;++g){
    float m=fmaxf(sc[g][t],sc[g][t+256]);
    for(int o=32;o>0;o>>=1) m=fmaxf(m,__shfl_xor(m,o));
    __syncthreads(); if((t&63)==0) red[t>>6]=m; __syncthreads();
    m=fmaxf(fmaxf(red[0],red[1]),fmaxf(red[2],red[3]));
    float e0=__expf(sc[g][t]-m), e1=__expf(sc[g][t+256]-m);
    __syncthreads();
    sc[g][t]=e0; sc[g][t+256]=e1;
    l[g]=block_sum(e0+e1,red); mx[g]=m;
  }
  __syncthreads();
  for(int i=t;i<512;i+=256){ int g=i>>7,d=i&127; float acc=0; for(int j=0;j<512;++j) acc+=sc[g][j]*bf2f(Vp[(size_t)j*DH+d]);
    po[(((size_t)b*HKV+h)*8+s)*512+i]=acc; }
  if(t<4){ pm[((b*HKV+h)*8+s)*4+t]=mx[t]; pl[((b*HKV+h)*8+s)*4+t]=l[t]; }
}
__global__ void k_comb(const float* po,const float* pm,const float* pl,float* o){
  int h=blockIdx.x,b=blockIdx.y,t=threadIdx.x;
  for(int i=t;i<512;i+=256){ int g=i>>7; float M=-1e30f; for(int s=0;s<8;++s) M=fmaxf(M,pm[((b*HKV+h)*8+s)*4+g]);
    float L=0,acc=0; for(int s=0;s<8;++s){ float w=__expf(pm[((b*HKV+h)*8+s)*4+g]-M); L+=w*pl[((b*HKV+h)*8+s)*4+g]; acc+=w*po[(((size_t)b*HKV+h)*8+s)*512+i]; }
    o[b*4096+h*512+i]=acc/L; }
}
__global__ void k_wo(const float* o,const u16* wo,float* xf){
  __shared__ float part[16][64];
  int b=blockIdx.y,t=threadIdx.x,c=blockIdx.x*64+(t&63),kq=t>>6;
  float acc=0; for(int k=kq*256;k<kq*256+256;++k) acc+=o[b*4096+k]*bf2f(wo[(size_t)k*512+c]);
  part[kq][t&63]=acc; __syncthreads();
  if(t<64){ float s=0; for(int i=0;i<16;++i) s+=part[i][t]; xf[b*D+c]+=s; }
}
__global__ void k_mix(const float* xin,float* xout,u16* out,const u16* w1,const u16* b1,const u16* w2,const u16* b2){
  __shared__ float red[8];
  int b=blockIdx.y,t=threadIdx.x;
  float a0=xin[b*D+t], a1=xin[b*D+t+256];
  float m=block_sum(a0+a1,red)/D;
  float v=block_sum((a0-m)*(a0-m)+(a1-m)*(a1-m),red)/D;
  float r=1.0f/sqrtf(v+1e-5f);
  float h0=(a0-m)*r,h1=(a1-m)*r;
  float m2=block_sum(h0+h1,red)/D;
  float v2=block_sum((h0-m2)*(h0-m2)+(h1-m2)*(h1-m2),red)/D;
  float r2=1.0f/sqrtf(v2+1e-5f);
  for(int j=0;j<8;++j){
    int c=blockIdx.x*8+j; float xv=xin[b*D+c]; float h=((xv-m)*r-m2)*r2;
    float acc=0;
    for(int f=t;f<F;f+=256){ float u=h*bf2f(w1[f])+bf2f(b1[f]); float z=0.7978845608028654f*(u+0.044715f*u*u*u); float g=0.5f*u*(1.0f+tanhf(z)); acc+=g*bf2f(w2[f]); }
    acc=block_sum(acc,red);
    if(t==0){ float y=xv+acc+bf2f(b2[0]); if(xout) xout[b*D+c]=y; if(out) out[b*D+c]=f2bf(y); }
  }
}

extern "C" void kernel_launch(void* const* d_in, const int* in_sizes, int n_in, void* d_out, int out_size, void* d_ws, size_t ws_size, hipStream_t stream){
  const u16* x=(const u16*)d_in[0]; const u16* K=(const u16*)d_in[1]; const u16* V=(const u16*)d_in[2];
  const u16* wq=(const u16*)d_in[3]; const u16* wo=(const u16*)d_in[4];
  const u16* w1=(const u16*)d_in[6]; const u16* b1=(const u16*)d_in[7]; const u16* w2=(const u16*)d_in[8]; const u16* b2=(const u16*)d_in[9];
  float* ws=(float*)d_ws;
  float* xa=ws; float* xb=ws+2048; float* q=ws+4096; float* o=q+16384; float* po=o+16384; float* pm=po+4*8*8*512; float* pl=pm+1024;
  k_init<<<8,256,0,stream>>>(x,xa);
  for(int layer=0;layer<2;++layer){
    float* xc = layer==0? xa: xb;
    k_q<<<dim3(64,4),256,0,stream>>>(xc,wq,q);
    k_attn<<<dim3(8,8,4),256,0,stream>>>(q,K,V,po,pm,pl);
    k_comb<<<dim3(8,4),256,0,stream>>>(po,pm,pl,o);
    k_wo<<<dim3(8,4),1024,0,stream>>>(o,wo,xc);
    k_mix<<<dim3(64,4),256,0,stream>>>(xc, layer==0? xb:nullptr, layer==1?(u16*)d_out:nullptr, w1,b1,w2,b2);
  }
}
```

```cpp
#include <hip/hip_runtime.h>

typedef unsigned int u32;
typedef unsigned short u16;
typedef unsigned long long u64;
typedef u32 v4u __attribute__((ext_vector_type(4)));
typedef u32 v2u __attribute__((ext_vector_type(2)));
typedef float f4 __attribute__((ext_vector_type(4)));
typedef float f2 __attribute__((ext_vector_type(2)));
typedef short s8v __attribute__((ext_vector_type(8)));
typedef short s4v __attribute__((ext_vector_type(4)));
typedef __bf16 bf2v __attribute__((ext_vector_type(2)));
#define LAS __attribute__((address_space(3)))
#define GAS __attribute__((address_space(1)))
#define DI __device__ __forceinline__

#define STAMPS 0
#define MAPMODE 0
#define DLY_B 6
#define DLY_CD 8
#define DLY_E 8
#define DLY_A 5
#define STW 0
#define WD 6
#define CODE_KB 0

constexpr int SK = 16384, WIN = 4096;
constexpr float EPS = 1e-5f;
constexpr float SCL = 0.08838834764831845f * 1.4426950408889634f;
constexpr float GK0 = -2.0f * 0.7978845608028654f * 1.4426950408889634f;
constexpr float GK1 = GK0 * 0.044715f;

constexpr int WS_EP = 0;
constexpr int WS_QB = 4096;
constexpr int WS_OB = WS_QB + 2 * 4 * 8 * 2 * 2048;
constexpr int WS_OS = WS_OB + 2 * 4 * 8 * 8 * 2048;
constexpr int WS_PB = WS_OS + 2 * 4 * 8 * 8 * 64;
constexpr int WS_XB = WS_PB + 2 * 4 * 8 * 2048;
constexpr int WS_END = WS_XB + 4 * 2048;
constexpr int WS_STAMP = 4 << 20;

constexpr int L_KV = 0;
constexpr int L_PAR = 131072;
constexpr int PSTR = 1088;
constexpr int L_PL = L_PAR + 11264;
constexpr int L_HB = L_PL + 4 * PSTR;
constexpr int L_ZERO = L_HB + 1024;
constexpr int QSTR = 288;
constexpr int L_QL = L_ZERO + 64;
constexpr int L_XL = L_QL + 4 * QSTR;
constexpr int L_RED = L_XL + 2048;
constexpr int L_ST = L_RED + 5120;
constexpr int L_MB = L_ST + 64;
constexpr int L_LB = L_MB + 128;
constexpr int L_R2 = L_LB + 512;
constexpr int L_STAMP = L_R2 + 128;
constexpr int L_END = L_STAMP + 1024;
static_assert(L_END <= 163840, "LDS");

struct Params {
  const u16* x; const unsigned char* K; const unsigned char* V; const unsigned char* wq; const unsigned char* wo;
  const u16* w1; const u16* b1; const u16* w2; const u16* b2; u16* out; unsigned char* ws;
};

template <int CTRL> DI float dpp(float x) {
  return __builtin_bit_cast(float, __builtin_amdgcn_update_dpp(0, __builtin_bit_cast(int, x), CTRL, 0xf, 0xf, true));
}
template <int CTRL, int RM> DI float dppm(float x) {
  return __builtin_bit_cast(float, __builtin_amdgcn_update_dpp(0, __builtin_bit_cast(int, x), CTRL, RM, 0xf, false));
}
constexpr int XOR1 = 0xB1, XOR2 = 0x4E, HMIR = 0x141, MIR = 0x140, ROR4 = 0x124, ROR8 = 0x128, BC15 = 0x142, BC31 = 0x143;
constexpr int SHL4 = 0x104, SHL8 = 0x108, SHL12 = 0x10C;
DI float asf(u32 x) { return __builtin_bit_cast(float, x); }
DI u32 asu(float x) { return __builtin_bit_cast(u32, x); }
DI u32 cvtpk(float lo, float hi) { f2 f = {lo, hi}; return __builtin_bit_cast(u32, __builtin_convertvector(f, bf2v)); }
DI f4 mfma(v4u a, v4u b, f4 c) {
  return __builtin_amdgcn_mfma_f32_16x16x32_bf16(__builtin_bit_cast(s8v, a), __builtin_bit_cast(s8v, b), c, 0, 0, 0);
}
DI float xrow_max(float x) {
  auto s = __builtin_amdgcn_permlane16_swap(asu(x), asu(x), false, false);
  x = fmaxf(asf(s[0]), asf(s[1]));
  auto t = __builtin_amdgcn_permlane32_swap(asu(x), asu(x), false, false);
  return fmaxf(asf(t[0]), asf(t[1]));
}
DI float xrow_sum(float x) {
  auto s = __builtin_amdgcn_permlane16_swap(asu(x), asu(x), false, false);
  x = asf(s[0]) + asf(s[1]);
  auto t = __builtin_amdgcn_permlane32_swap(asu(x), asu(x), false, false);
  return asf(t[0]) + asf(t[1]);
}
#define BAR() asm volatile("s_waitcnt lgkmcnt(0)\n\ts_barrier" ::: "memory")
#define CBAR() asm volatile("" ::: "memory")
#define SB() __builtin_amdgcn_sched_barrier(0)
#define WAITV(n) asm volatile("s_waitcnt vmcnt(" #n ")" ::: "memory")

#define STAMP(i)
#define STAMPR(i)
#define STAMPV(i, v)
#define STAMPW(i)

constexpr u32 TMASK = 0x3ffu;
DI u32 tagf(float v, u32 tag) { return ((asu(v) + 0x200u) & ~TMASK) | tag; }
DI float untag(u32 v) { return asf(v & ~TMASK); }

DI u32 ld32s(__amdgpu_buffer_rsrc_t rs, int off) { return __builtin_amdgcn_raw_buffer_load_b32(rs, off, 0, 16); }
DI v4u ld128s(__amdgpu_buffer_rsrc_t rs, int off) { return __builtin_amdgcn_raw_buffer_load_b128(rs, off, 0, 16); }
DI void st128s(v4u v, __amdgpu_buffer_rsrc_t rs, int off) { __builtin_amdgcn_raw_buffer_store_b128(v, rs, off, 0, 16); }

DI void ln_stats(float x, LAS unsigned char* L, int w, int lane, float& mean, float& var) {
  const float c = asf(__builtin_amdgcn_readfirstlane(asu(x)));
  const float d = x - c;
  float s1 = d, s2 = d * d;
  s1 += dpp<XOR1>(s1); s2 += dpp<XOR1>(s2);
  s1 += dpp<XOR2>(s1); s2 += dpp<XOR2>(s2);
  s1 += dpp<HMIR>(s1); s2 += dpp<HMIR>(s2);
  s1 += dpp<MIR>(s1); s2 += dpp<MIR>(s2);
  s1 += dppm<BC15, 0xa>(s1); s2 += dppm<BC15, 0xa>(s2);
  s1 += dppm<BC31, 0xc>(s1); s2 += dppm<BC31, 0xc>(s2);
  if (lane == 63) {
    f2 v = {c + s1 * (1.0f / 64), s2 - s1 * s1 * (1.0f / 64)};
    *(LAS f2*)(L + L_ST + w * 8) = v;
  }
  BAR();
  const f2 v = *(const LAS f2*)(L + L_ST + (lane & 7) * 8);
  float m = v.x;
  m += dpp<XOR1>(m); m += dpp<XOR2>(m); m += dpp<HMIR>(m);
  mean = m * 0.125f;
  const float dv = v.x - mean;
  float t = v.y + 64.0f * dv * dv;
  t += dpp<XOR1>(t); t += dpp<XOR2>(t); t += dpp<HMIR>(t);
  var = t * (1.0f / 512);
}

DI void transpose8(const v4u (&raw)[8], v4u (&fr)[8]) {
#pragma unroll
  for (int i = 0; i < 8; ++i) {
    const u32 sel = (i & 1) ? 0x07060302u : 0x05040100u;
#pragma unroll
    for (int m = 0; m < 4; ++m) fr[i][m] = __builtin_amdgcn_perm(raw[2 * m + 1][i >> 1], raw[2 * m][i >> 1], sel);
  }
}

DI void q_gemv(const v4u (&wf)[8], LAS unsigned char* L, int tid, int w, int lane, int kh, __amdgpu_buffer_rsrc_t rs, int qoff, u32 tag) {
  const int g = lane >> 4, r = lane & 15;
  const int real = L_HB + (256 * kh + 32 * w + 8 * g) * 2;
  f4 acc = {0.f, 0.f, 0.f, 0.f};
#pragma unroll
  for (int i = 0; i < 8; ++i) {
    const v4u a = *(const LAS v4u*)(L + ((r == i) ? real : L_ZERO));
    acc = mfma(a, wf[i], acc);
  }
  if (lane < 32) *(LAS f4*)(L + L_RED + w * 576 + (8 * r + 4 * g) * 4) = acc;
  BAR();
  if (tid < 128) {
    const int n4 = tid >> 2, wp = tid & 3;
    f4 v = *(const LAS f4*)(L + L_RED + wp * 576 + n4 * 16) + *(const LAS f4*)(L + L_RED + (wp + 4) * 576 + n4 * 16);
#pragma unroll
    for (int e = 0; e < 4; ++e) { v[e] += dpp<XOR1>(v[e]); v[e] += dpp<XOR2>(v[e]); }
    if (wp == 0) { v4u o = {tagf(v[0], tag), tagf(v[1], tag), tagf(v[2], tag), tagf(v[3], tag)}; st128s(o, rs, qoff + n4 * 16); }
  }
}

DI float attn_scores(float qv, const v4u (&kf)[16], LAS unsigned char* L, int tid, int w, int lane) {
  const int g = lane >> 4, c = lane & 15, h = c & 3, s4 = c >> 2;
  *(LAS u16*)(L + L_QL + (tid >> 7) * QSTR + (tid & 127) * 2) = (u16)(cvtpk(qv, qv) & 0xffffu);
  BAR();
  v4u qf[4];
#pragma unroll
  for (int st = 0; st < 4; ++st) qf[st] = *(const LAS v4u*)(L + L_QL + h * QSTR + st * 64 + g * 16);
  f4 d[4];
#pragma unroll
  for (int t = 0; t < 4; ++t) {
    d[t] = f4{0.f, 0.f, 0.f, 0.f};
#pragma unroll
    for (int st = 0; st < 4; ++st) d[t] = mfma(kf[t * 4 + st], qf[st], d[t]);
  }
  float sc[4];
#pragma unroll
  for (int i = 0; i < 4; ++i) {
    const float a = (s4 & 1) ? d[1][i] : d[0][i], bq = (s4 & 1) ? d[3][i] : d[2][i];
    sc[i] = (s4 & 2) ? bq : a;
  }
  float m = fmaxf(fmaxf(sc[0], sc[1]), fmaxf(sc[2], sc[3]));
  m = fmaxf(m, dpp<ROR4>(m)); m = fmaxf(m, dpp<ROR8>(m));
  m = xrow_max(m);
  if (lane < 4) *(LAS float*)(L + L_MB + (lane * 8 + w) * 4) = m;
  BAR();
  const f4 m0 = *(const LAS f4*)(L + L_MB + h * 32), m1 = *(const LAS f4*)(L + L_MB + h * 32 + 16);
  float M = fmaxf(fmaxf(fmaxf(m0[0], m0[1]), fmaxf(m0[2], m0[3])), fmaxf(fmaxf(m1[0], m1[1]), fmaxf(m1[2], m1[3])));
  M = asf(asu(M * SCL) & ~TMASK);
  float p[4];
#pragma unroll
  for (int i = 0; i < 4; ++i) p[i] = __builtin_amdgcn_exp2f(fmaf(sc[i], SCL, -M));
  v2u pk = {cvtpk(p[0], p[1]), cvtpk(p[2], p[3])};
  *(LAS v2u*)(L + L_PL + s4 * PSTR + ((2 * (w & 1) + (g >> 1)) * 16 + (w >> 1) * 4 + h) * 16 + (g & 1) * 8) = pk;
  float l = (p[0] + p[1]) + (p[2] + p[3]);
  l += dpp<ROR4>(l); l += dpp<ROR8>(l);
  if (c < 4) *(LAS float*)(L + L_LB + (w * 16 + g * 4 + c) * 4) = l;
  return M;
}

DI void pv_chunk(int t, f4 (&acc)[4], LAS unsigned char* L, int vbase, int lane) {
  const v4u pf = *(const LAS v4u*)(L + L_PL + t * PSTR + lane * 16);
#pragma unroll
  for (int u = 0; u < 4; ++u) {
    const s4v lo = __builtin_amdgcn_ds_read_tr16_b64_v4i16((LAS s4v*)(L + vbase + u * 32768 + t * 4096));
    const s4v hi = __builtin_amdgcn_ds_read_tr16_b64_v4i16((LAS s4v*)(L + vbase + u * 32768 + t * 4096 + 1024));
    const v2u l2 = __builtin_bit_cast(v2u, lo), h2 = __builtin_bit_cast(v2u, hi);
    const v4u a = {l2[0], l2[1], h2[0], h2[1]};
    acc[u] = mfma(a, pf, acc[u]);
  }
}

DI void attn_publish(const f4 (&acc)[4], float M, LAS unsigned char* L, int w, int lane, __amdgpu_buffer_rsrc_t rs, int ooff, int soff, u32 tag, u32 T0) {
  const int g = lane >> 4, c = lane & 15;
  v4u o;
#pragma unroll
  for (int e = 0; e < 4; ++e) {
    float v = acc[0][e];
    v += dpp<SHL4>(acc[1][e]); v += dpp<SHL8>(acc[2][e]); v += dpp<SHL12>(acc[3][e]);
    o[e] = tagf(v, tag);
  }
  if (c < 4) st128s(o, rs, ooff + (c * 128 + 16 * w + 4 * g) * 4);
  if (w == 0) {
    float l = *(const LAS float*)(L + L_LB + lane * 4) + *(const LAS float*)(L + L_LB + (lane + 64) * 4);
    l += dpp<ROR4>(l); l += dpp<ROR8>(l);
    l = xrow_sum(l);
    if (lane < 4) { v4u sv = {asu(M) | tag, tagf(l, tag), T0, ~T0}; st128s(sv, rs, soff + lane * 16); }
  }
}

DI void o_gemv(const v4u (&wf)[8], LAS unsigned char* L, int tid, int w, int lane, __amdgpu_buffer_rsrc_t rs, int obase, int sbase, int poff, u32 tag, u32 T0, int& budget, int sb) {
  const int g = lane >> 4, r = lane & 15;
  u32 o[8]; v4u sv;
  const int oo = obase + (64 * w + lane) * 4, so = sbase + (lane & 7) * 64 + (w >> 1) * 16;
  for (;;) {
    CBAR();
#pragma unroll
    for (int i = 0; i < 8; ++i) o[i] = ld32s(rs, oo + i * 2048);
    sv = ld128s(rs, so);
    u32 d = (sv[0] ^ tag) | (sv[1] ^ tag);
#pragma unroll
    for (int i = 0; i < 8; ++i) d |= o[i] ^ tag;
    d = (d & TMASK) | (sv[2] ^ T0) | (sv[3] ^ ~T0);
    if (__all(d == 0) || --budget <= 0) break;
  }
  STAMP(sb); STAMPV(sb + 1, budget);
  const float ms = untag(sv[0]), ls = untag(sv[1]);
  float M = ms;
  M = fmaxf(M, dpp<XOR1>(M)); M = fmaxf(M, dpp<XOR2>(M)); M = fmaxf(M, dpp<HMIR>(M));
  const float f = __builtin_amdgcn_exp2f(ms - M);
  float Ls = f * ls;
  Ls += dpp<XOR1>(Ls); Ls += dpp<XOR2>(Ls); Ls += dpp<HMIR>(Ls);
  const float cs = f * __builtin_amdgcn_rcpf(Ls);
  float ov = 0.f;
#pragma unroll
  for (int i = 0; i < 8; ++i) ov = fmaf(asf(__builtin_amdgcn_readlane(asu(cs), i)), untag(o[i]), ov);
  *(LAS u16*)(L + L_HB + (64 * w + lane) * 2) = (u16)(cvtpk(ov, ov) & 0xffffu);
  CBAR();
  const int real = L_HB + (64 * w + 32 * (r >> 3) + 8 * g) * 2;
  f4 acc = {0.f, 0.f, 0.f, 0.f};
#pragma unroll
  for (int i = 0; i < 8; ++i) {
    const v4u a = *(const LAS v4u*)(L + (((r & 7) == i) ? real : L_ZERO));
    acc = mfma(a, wf[i], acc);
  }
  if ((g >> 1) == (r >> 3)) *(LAS f4*)(L + L_RED + (2 * w + (g >> 1)) * 320 + (8 * (r & 7) + 4 * (g & 1)) * 4) = acc;
  BAR();
  if (tid < 64) {
    const int n4 = tid >> 2, wp = tid & 3;
    f4 v = (*(const LAS f4*)(L + L_RED + wp * 320 + n4 * 16) + *(const LAS f4*)(L + L_RED + (wp + 4) * 320 + n4 * 16)) +
           (*(const LAS f4*)(L + L_RED + (wp + 8) * 320 + n4 * 16) + *(const LAS f4*)(L + L_RED + (wp + 12) * 320 + n4 * 16));
#pragma unroll
    for (int e = 0; e < 4; ++e) { v[e] += dpp<XOR1>(v[e]); v[e] += dpp<XOR2>(v[e]); }
    if (wp == 0) { v4u ov4 = {tagf(v[0], tag), tagf(v[1], tag), tagf(v[2], tag), tagf(v[3], tag)}; st128s(ov4, rs, poff + n4 * 16); }
  }
}

DI f4 mixer(float& xres, LAS unsigned char* L, int tid, int w, int lane, int j, __amdgpu_buffer_rsrc_t rs, int pbase, u32 tag, float b2, int& budget, int sb) {
  float pw1[7], pb1[7], pw2[7];
  const int fl = tid & 255, q = tid >> 8;
#pragma unroll
  for (int i = 0; i < 7; ++i) {
    pw1[i] = asf((u32) * (const LAS u16*)(L + L_PAR + (fl + 256 * i) * 2) << 16);
    pb1[i] = asf((u32) * (const LAS u16*)(L + L_PAR + 3584 + (fl + 256 * i) * 2) << 16);
    pw2[i] = asf((u32) * (const LAS u16*)(L + L_PAR + 7168 + (fl + 256 * i) * 2) << 16);
  }
  SB();
  if (DLY_CD) __builtin_amdgcn_s_sleep(DLY_CD);
  u32 p[8];
  const int po = pbase + tid * 4;
  for (;;) {
    CBAR();
#pragma unroll
    for (int i = 0; i < 8; ++i) p[i] = ld32s(rs, po + i * 2048);
    u32 d = 0;
#pragma unroll
    for (int i = 0; i < 8; ++i) d |= p[i] ^ tag;
    if (__all((d & TMASK) == 0) || --budget <= 0) break;
  }
  STAMP(sb); STAMPV(sb + 1, budget);
  const float x1 = xres + (((untag(p[0]) + untag(p[1])) + (untag(p[2]) + untag(p[3]))) + ((untag(p[4]) + untag(p[5])) + (untag(p[6]) + untag(p[7]))));
  xres = x1;
  *(LAS float*)(L + L_XL + tid * 4) = x1;
  float mean, var;
  ln_stats(x1, L, w, lane, mean, var);
  const float r1 = __builtin_amdgcn_rsqf(var + EPS);
  const float r2 = __builtin_amdgcn_rsqf(var * r1 * r1 + EPS);
  const float rr = r1 * r2;
  STAMP(sb + 2); if (sb == 22) STAMPW(88);
  const f4 xv = *(const LAS f4*)(L + L_XL + (8 * j + 4 * q) * 4);
  const f2 hA = {(xv[0] - mean) * rr, (xv[1] - mean) * rr}, hB = {(xv[2] - mean) * rr, (xv[3] - mean) * rr};
  f2 aA = {0.f, 0.f}, aB = {0.f, 0.f};
#pragma unroll
  for (int i = 0; i < 7; ++i) {
    const f2 w1 = {pw1[i], pw1[i]}, b1 = {pb1[i], pb1[i]}, w2 = {pw2[i], pw2[i]};
    const f2 k0 = {GK0, GK0}, k1 = {GK1, GK1}, one = {1.f, 1.f};
    {
      const f2 u = hA * w1 + b1, z = u * (u * u * k1 + k0);
      const f2 e = {__builtin_amdgcn_exp2f(z[0]), __builtin_amdgcn_exp2f(z[1])}, dd = e + one;
      const f2 rc = {__builtin_amdgcn_rcpf(dd[0]), __builtin_amdgcn_rcpf(dd[1])};
      aA += (u * rc) * w2;
    }
    {
      const f2 u = hB * w1 + b1, z = u * (u * u * k1 + k0);
      const f2 e = {__builtin_amdgcn_exp2f(z[0]), __builtin_amdgcn_exp2f(z[1])}, dd = e + one;
      const f2 rc = {__builtin_amdgcn_rcpf(dd[0]), __builtin_amdgcn_rcpf(dd[1])};
      aB += (u * rc) * w2;
    }
  }
  float t0 = (lane & 1) ? aA[1] : aA[0], t1 = (lane & 1) ? aA[0] : aA[1];
  t0 += dpp<XOR1>(t1);
  float t2 = (lane & 1) ? aB[1] : aB[0], t3 = (lane & 1) ? aB[0] : aB[1];
  t2 += dpp<XOR1>(t3);
  float s0 = (lane & 2) ? t2 : t0, s1 = (lane & 2) ? t0 : t2;
  s0 += dpp<XOR2>(s1);
  s0 += dpp<ROR4>(s0); s0 += dpp<ROR8>(s0);
  s0 = xrow_sum(s0);
  STAMP(sb + 3); if (sb == 22) STAMPW(96);
  if (lane < 4) *(LAS float*)(L + L_R2 + (w * 4 + lane) * 4) = s0;
  BAR();
  f4 res = {0.f, 0.f, 0.f, 0.f};
  if (w == 0) {
    const int qq = lane & 1;
    const f4 a = (*(const LAS f4*)(L + L_R2 + (qq * 4 + 0) * 16) + *(const LAS f4*)(L + L_R2 + (qq * 4 + 1) * 16)) +
                 (*(const LAS f4*)(L + L_R2 + (qq * 4 + 2) * 16) + *(const LAS f4*)(L + L_R2 + (qq * 4 + 3) * 16));
    const f4 xr = *(const LAS f4*)(L + L_XL + (8 * j + 4 * qq) * 4);
    res = xr + a + f4{b2, b2, b2, b2};
  }
  return res;
}

__global__ void __launch_bounds__(512, 2) fused_decode_kernel(Params p) {
  __shared__ __attribute__((aligned(1024))) unsigned char smem[L_END];
  LAS unsigned char* L = (LAS unsigned char*)smem;
  const int tid = threadIdx.x, lane = tid & 63, w = __builtin_amdgcn_readfirstlane(tid >> 6);
  const int bid = blockIdx.x;
  int b, kvh, s;
  if (MAPMODE == 0) { kvh = bid & 7; b = bid >> 6; s = (bid >> 3) & 7; }
  else { const int x = bid & 7, i = (bid >> 3) + 32 * (x & 1); b = x >> 1; kvh = i >> 3; s = i & 7; }
  const int j = kvh * 8 + s;
  const int g = lane >> 4, c = lane & 15;
  STAMPR(36); STAMP(0);

  const size_t slab = ((size_t)(b * 8 + kvh) * SK + (SK - WIN) + s * 512) * 256;
  const __amdgpu_buffer_rsrc_t rK = __builtin_amdgcn_make_buffer_rsrc((void*)(p.K + slab), 0, 512 * 256, 0x00020000);
  const __amdgpu_buffer_rsrc_t rV = __builtin_amdgcn_make_buffer_rsrc((void*)(p.V + slab), 0, 512 * 256, 0x00020000);
  const __amdgpu_buffer_rsrc_t rQ = __builtin_amdgcn_make_buffer_rsrc((void*)p.wq, 0, 512 * 4096 * 2, 0x00020000);
  const __amdgpu_buffer_rsrc_t rO = __builtin_amdgcn_make_buffer_rsrc((void*)p.wo, 0, 512 * 4096 * 2, 0x00020000);
  const __amdgpu_buffer_rsrc_t rs = __builtin_amdgcn_make_buffer_rsrc((void*)p.ws, 0, WS_END, 0x00020000);
  const __amdgpu_buffer_rsrc_t rX = __builtin_amdgcn_make_buffer_rsrc((void*)p.x, 0, 4096, 0x00020000);
  const u32 ldsb = (u32)(size_t)L;

  u32 ev, xraw, b2raw;
  asm volatile("s_nop 4\n\tbuffer_load_dword %0, %1, %2, 0 offen sc1" : "=v"(ev) : "v"(WS_EP + b * 256), "s"(rs) : "memory");
  asm volatile("buffer_load_ushort %0, %1, %2, 0 offen" : "=v"(xraw) : "v"((b * 512 + tid) * 2), "s"(rX) : "memory");
  asm volatile("global_load_ushort %0, %1, %2" : "=v"(b2raw) : "v"(0), "s"(p.b2) : "memory");
  const int rot = __builtin_amdgcn_readfirstlane((int)(((u32)(bid * 8 + w) * 2654435761u) >> 28));
  const int cg = s >> 1, kh = s & 1;
  const int voq = ((256 * kh + 32 * w + 8 * g) * 4096 + kvh * 512 + 128 * cg + 8 * c) * 2;
  const int voo = ((kvh * 512 + 64 * w + 32 * (c >> 3) + 8 * g) * 512 + 64 * s + 8 * (c & 7)) * 2;
  int vok[4], vov[2];
#pragma unroll
  for (int i = 0; i < 4; ++i) vok[i] = (64 * w + g) * 256 + ((c ^ (4 * i + g)) * 16);
#pragma unroll
  for (int i = 0; i < 2; ++i) vov[i] = (64 * w + g) * 256 + ((((c >> 1) ^ (g | (i << 2))) * 32) + (c & 1) * 16);
  v4u wqr[8], wor[8], kf[16];
#define LDQ(jj) asm volatile("buffer_load_dwordx4 %0, %1, %2, %3 offen nt" : "=v"(wqr[jj]) : "v"(voq), "s"(rQ), "s"((jj) * 8192) : "memory")
#define LDO(jj) asm volatile("buffer_load_dwordx4 %0, %1, %2, %3 offen nt" : "=v"(wor[jj]) : "v"(voo), "s"(rO), "s"((jj) * 1024) : "memory")
#define LDK(i) asm volatile("s_mov_b32 m0, %0\n\ts_nop 0\n\tbuffer_load_dwordx4 %1, %2, %3 offen nt lds" ::"s"(ldsb + L_KV + w * 16384 + (i) * 1024), "v"(vok[(i) & 3]), "s"(rK), "s"((((i) + rot) & 15) * 1024) : "memory")
#define LDV(i) asm volatile("s_mov_b32 m0, %0\n\ts_nop 0\n\tbuffer_load_dwordx4 %1, %2, %3 offen nt lds" ::"s"(ldsb + L_KV + w * 16384 + (i) * 1024), "v"(vov[((i) >> 1) & 1]), "s"(rV), "s"((((i) + rot) & 15) * 1024) : "memory")
#define G() asm volatile("s_waitcnt vmcnt(%0)" ::"n"(WD - 1) : "memory")
#define EV(n, i) asm volatile("s_waitcnt vmcnt(%0)" ::"n"((n) - (i) - 1) : "memory")
#define KFRAG(t)                                                                                                     \
  _Pragma("unroll") for (int st = 0; st < 4; ++st)                                                                   \
      kf[(t) * 4 + st] = *(const LAS v4u*)(L + L_KV + w * 16384 + (16 * (t) + c) * 256 + (((4 * st + g) ^ c) * 16)); \
  asm volatile("" : "+v"(kf[(t) * 4]), "+v"(kf[(t) * 4 + 1]), "+v"(kf[(t) * 4 + 2]), "+v"(kf[(t) * 4 + 3])::"memory")
  LDQ(0); LDK(0); LDQ(1); LDK(1); LDQ(2); LDK(2);
  if (tid < 16) *(LAS u32*)(L + L_ZERO + tid * 4) = 0u;
  G(); LDQ(3); G(); LDK(3); G(); LDQ(4); G(); LDK(4);
  G(); LDQ(5); G(); LDK(5); G(); LDQ(6); G(); LDK(6);
  G(); LDQ(7); G(); LDK(7);
  EV(16, 7); STAMPW(48); KFRAG(0);
  G(); LDK(8); G(); LDK(9); G(); LDK(10); G(); LDK(11); G(); LDK(12);
  EV(21, 14);
  asm volatile("" : "+v"(wqr[0]), "+v"(wqr[1]), "+v"(wqr[2]), "+v"(wqr[3]), "+v"(wqr[4]), "+v"(wqr[5]), "+v"(wqr[6]), "+v"(wqr[7]), "+v"(ev), "+v"(xraw), "+v"(b2raw)::"memory");
  STAMP(1); STAMPW(40);
  const u32 T0 = __builtin_amdgcn_readfirstlane(ev) + 1u;
  const u32 tag = T0 & TMASK;
  v4u wqf[8];
  transpose8(wqr, wqf);
  float xres = asf(xraw << 16);
  {
    float mean, var;
    ln_stats(xres, L, w, lane, mean, var);
    const float hv = (xres - mean) * __builtin_amdgcn_rsqf(var + EPS);
    *(LAS u16*)(L + L_HB + tid * 2) = (u16)(cvtpk(hv, hv) & 0xffffu);
    BAR();
    q_gemv(wqf, L, tid, w, lane, kh, rs, WS_QB + ((b * 8 + kvh) * 2 + kh) * 2048 + cg * 512, tag);
  }
  STAMP(2);
  G(); LDK(13);
  EV(22, 15); KFRAG(1);
  G(); LDK(14); G(); LDK(15);
  {
#pragma unroll
    for (int k = 0; k < 2; ++k) {
      const int piece = k == 0 ? w : 8 + (w % 3);
      int gr = piece * 64 + lane; gr = gr < 672 ? gr : 671;
      const int arr = gr / 224, e = gr - arr * 224;
      const unsigned char* src = (const unsigned char*)(arr == 0 ? p.w1 : arr == 1 ? p.b1 : p.w2) + e * 16;
      G();
      asm volatile("s_mov_b32 m0, %0\n\ts_nop 0\n\tglobal_load_lds_dwordx4 %1, off" ::"s"(ldsb + L_PAR + piece * 1024), "v"(src) : "memory");
    }
  }
  EV(26, 19); KFRAG(2);
  G(); LDV(0); G(); LDV(1);
  STAMP(3);
  u32 q0, q1;
  const int qo = WS_QB + (b * 8 + kvh) * 4096 + tid * 4;
  G(); asm volatile("buffer_load_dword %0, %1, %2, 0 offen sc1" : "=v"(q0) : "v"(qo), "s"(rs) : "memory");
  asm volatile("buffer_load_dword %0, %1, %2, 0 offen offset:2048 sc1" : "=v"(q1) : "v"(qo), "s"(rs) : "memory");
  EV(30, 23); KFRAG(3);
  STAMP(4); STAMPW(56);
  G(); LDV(2); G(); LDV(3); G(); LDO(0);
  G(); LDV(4); G(); LDV(5); G(); LDO(1);
  EV(36, 29);
  asm volatile("" : "+v"(q0), "+v"(q1)::"memory");
  STAMP(5); STAMPW(64);
  int budget = 100000;
  if (!__all((((q0 ^ tag) | (q1 ^ tag)) & TMASK) == 0)) {
    _Pragma("nounroll") do {
      asm volatile("buffer_load_dword %0, %2, %3, 0 offen sc1\n\tbuffer_load_dword %1, %2, %3, 0 offen offset:2048 sc1\n\ts_waitcnt vmcnt(0)" : "=&v"(q0), "=&v"(q1) : "v"(qo), "s"(rs) : "memory");
    } while (!__all((((q0 ^ tag) | (q1 ^ tag)) & TMASK) == 0) && --budget > 0);
  }
  const int vbase = L_KV + (g >> 1) * 16384 + (8 * (g & 1) + (c >> 2)) * 256 + ((w ^ ((c >> 2) | ((g & 1) << 2))) * 32) + (c & 3) * 8;
  f4 acc[4];
  float M = attn_scores(untag(q0) + untag(q1), kf, L, tid, w, lane);
  STAMP(6);
  G(); LDV(6); G(); LDV(7); G(); LDO(2);
  G(); LDV(8); G(); LDV(9); G(); LDO(3);
  G(); LDV(10); G(); LDV(11); G(); LDO(4);
  G(); LDV(12); G(); LDV(13); G(); LDO(5);
  G(); LDV(14); G(); LDV(15); G(); LDO(6); G(); LDO(7);
  EV(52, 43);
  BAR();
#pragma unroll
  for (int u = 0; u < 4; ++u) acc[u] = f4{0.f, 0.f, 0.f, 0.f};
  pv_chunk(0, acc, L, vbase, lane);
  pv_chunk(1, acc, L, vbase, lane);
  pv_chunk(2, acc, L, vbase, lane);
  SB();
  EV(52, 49);
  STAMP(7); STAMPW(72);
  BAR();
  pv_chunk(3, acc, L, vbase, lane);
  attn_publish(acc, M, L, w, lane, rs, WS_OB + ((b * 8 + kvh) * 8 + s) * 2048, WS_OS + ((b * 8 + kvh) * 8 + s) * 64, tag, T0);
  STAMP(8);
  if (w == 0) asm volatile("s_waitcnt vmcnt(2)" ::: "memory"); else asm volatile("s_waitcnt vmcnt(1)" ::: "memory");
  asm volatile("" : "+v"(wor[0]), "+v"(wor[1]), "+v"(wor[2]), "+v"(wor[3]), "+v"(wor[4]), "+v"(wor[5]), "+v"(wor[6]), "+v"(wor[7])::"memory");
  STAMP(9); STAMPW(80);
  v4u wof[8];
  transpose8(wor, wof);
  const float b2 = asf(b2raw << 16);

  if (DLY_B) __builtin_amdgcn_s_sleep(DLY_B);
  o_gemv(wof, L, tid, w, lane, rs, WS_OB + (b * 8 + kvh) * 8 * 2048, WS_OS + (b * 8 + kvh) * 8 * 64, WS_PB + (b * 8 + kvh) * 2048 + s * 256, tag, T0, budget, 20);
  STAMP(10);
  {
    const f4 r = mixer(xres, L, tid, w, lane, j, rs, WS_PB + b * 8 * 2048, tag, b2, budget, 22);
    if (tid < 2) { v4u o = {tagf(r[0], tag), tagf(r[1], tag), tagf(r[2], tag), tagf(r[3], tag)}; st128s(o, rs, WS_XB + b * 2048 + j * 32 + tid * 16); }
  }
  STAMP(11);
  if (DLY_E) __builtin_amdgcn_s_sleep(DLY_E);
  {
    u32 xv;
    for (;;) { CBAR(); xv = ld32s(rs, WS_XB + b * 2048 + tid * 4); if (__all(((xv ^ tag) & TMASK) == 0) || --budget <= 0) break; }
    STAMP(26); STAMPV(27, budget);
    xres = untag(xv);
    float mean, var;
    ln_stats(xres, L, w, lane, mean, var);
    const float hv = (xres - mean) * __builtin_amdgcn_rsqf(var + EPS);
    *(LAS u16*)(L + L_HB + tid * 2) = (u16)(cvtpk(hv, hv) & 0xffffu);
    BAR();
    STAMP(12);
    q_gemv(wqf, L, tid, w, lane, kh, rs, WS_QB + 4 * 8 * 4096 + ((b * 8 + kvh) * 2 + kh) * 2048 + cg * 512, tag);
  }
  STAMP(13);
  if (DLY_A) __builtin_amdgcn_s_sleep(DLY_A);
  {
    const int qo2 = qo + 4 * 8 * 4096;
    for (;;) { CBAR(); q0 = ld32s(rs, qo2); q1 = ld32s(rs, qo2 + 2048); if (__all((((q0 ^ tag) | (q1 ^ tag)) & TMASK) == 0) || --budget <= 0) break; }
    STAMP(14); STAMPV(18, budget);
    M = attn_scores(untag(q0) + untag(q1), kf, L, tid, w, lane);
    BAR();
#pragma unroll
    for (int u = 0; u < 4; ++u) acc[u] = f4{0.f, 0.f, 0.f, 0.f};
    pv_chunk(0, acc, L, vbase, lane);
    pv_chunk(1, acc, L, vbase, lane);
    pv_chunk(2, acc, L, vbase, lane);
    pv_chunk(3, acc, L, vbase, lane);
    attn_publish(acc, M, L, w, lane, rs, WS_OB + (1 << 19) + ((b * 8 + kvh) * 8 + s) * 2048, WS_OS + 16384 + ((b * 8 + kvh) * 8 + s) * 64, tag, T0);
  }
  STAMP(15);
  if (DLY_B) __builtin_amdgcn_s_sleep(DLY_B);
  o_gemv(wof, L, tid, w, lane, rs, WS_OB + (1 << 19) + (b * 8 + kvh) * 8 * 2048, WS_OS + 16384 + (b * 8 + kvh) * 8 * 64, WS_PB + 65536 + (b * 8 + kvh) * 2048 + s * 256, tag, T0, budget, 28);
  STAMP(16);
  {
    const f4 r = mixer(xres, L, tid, w, lane, j, rs, WS_PB + 65536 + b * 8 * 2048, tag, b2, budget, 30);
    if (w == 0) {
      const u32 lo = cvtpk(r[0], r[1]), hi = cvtpk(r[2], r[3]);
      if (lane < 2) { v2u o = {lo, hi}; *(GAS v2u*)((GAS unsigned char*)p.out + (b * 512 + 8 * j + 4 * lane) * 2) = o; }
      if (lane == 0 && j == 0) __builtin_amdgcn_raw_buffer_store_b32(T0, rs, WS_EP + b * 256, 0, 16);
    }
  }
  STAMP(17); STAMPR(37);
}

extern "C" void kernel_launch(void* const* d_in, const int* in_sizes, int n_in, void* d_out, int out_size, void* d_ws, size_t ws_size, hipStream_t stream) {
  Params p;
  p.x = (const u16*)d_in[0]; p.K = (const unsigned char*)d_in[1]; p.V = (const unsigned char*)d_in[2];
  p.wq = (const unsigned char*)d_in[3]; p.wo = (const unsigned char*)d_in[4];
  p.w1 = (const u16*)d_in[6]; p.b1 = (const u16*)d_in[7]; p.w2 = (const u16*)d_in[8]; p.b2 = (const u16*)d_in[9];
  p.out = (u16*)d_out; p.ws = (unsigned char*)d_ws;
  hipLaunchKernelGGL(fused_decode_kernel, dim3(256), dim3(512), 0, stream, p);
}
```

```cpp
#include <hip/hip_runtime.h>

typedef unsigned int u32;
typedef unsigned short u16;
typedef unsigned long long u64;
typedef u32 v4u __attribute__((ext_vector_type(4)));
typedef u32 v2u __attribute__((ext_vector_type(2)));
typedef float f4 __attribute__((ext_vector_type(4)));
typedef float f2 __attribute__((ext_vector_type(2)));
typedef short s8v __attribute__((ext_vector_type(8)));
typedef short s4v __attribute__((ext_vector_type(4)));
typedef __bf16 bf2v __attribute__((ext_vector_type(2)));
#define LAS __attribute__((address_space(3)))
#define GAS __attribute__((address_space(1)))
#define DI __device__ __forceinline__

#define STAMPS 0
#define MAPMODE 0
#define DLY_B 6
#define DLY_CD 8
#define DLY_E 8
#define DLY_A 5
#define STW 0
#define KVPOL " nt"
#define WPOL " nt"
#define WD 6
#define CODE_KB 0

constexpr int SK = 16384, WIN = 4096;
constexpr float EPS = 1e-5f;
constexpr float SCL = 0.08838834764831845f * 1.4426950408889634f;
constexpr float GK0 = -2.0f * 0.7978845608028654f * 1.4426950408889634f;
constexpr float GK1 = GK0 * 0.044715f;

constexpr int WS_EP = 0;
constexpr int WS_QB = 4096;
constexpr int WS_OB = WS_QB + 2 * 4 * 8 * 2 * 2048;
constexpr int WS_OS = WS_OB + 2 * 4 * 8 * 8 * 2048;
constexpr int WS_PB = WS_OS + 2 * 4 * 8 * 8 * 64;
constexpr int WS_XB = WS_PB + 2 * 4 * 8 * 2048;
constexpr int WS_END = WS_XB + 4 * 2048;
constexpr int WS_STAMP = 4 << 20;

constexpr int L_KV = 0;
constexpr int L_PAR = 131072;
constexpr int PSTR = 1088;
constexpr int L_PL = L_PAR + 11264;
constexpr int L_HB = L_PL + 4 * PSTR;
constexpr int L_ZERO = L_HB + 1024;
constexpr int QSTR = 288;
constexpr int L_QL = L_ZERO + 64;
constexpr int L_XL = L_QL + 4 * QSTR;
constexpr int L_RED = L_XL + 2048;
constexpr int L_ST = L_RED + 5120;
constexpr int L_MB = L_ST + 64;
constexpr int L_LB = L_MB + 128;
constexpr int L_R2 = L_LB + 512;
constexpr int L_STAMP = L_R2 + 128;
constexpr int L_END = L_STAMP + 1024;
static_assert(L_END <= 163840, "LDS");

struct Params {
  const u16* x; const unsigned char* K; const unsigned char* V; const unsigned char* wq; const unsigned char* wo;
  const u16* w1; const u16* b1; const u16* w2; const u16* b2; u16* out; unsigned char* ws;
};

template <int CTRL> DI float dpp(float x) {
  return __builtin_bit_cast(float, __builtin_amdgcn_update_dpp(0, __builtin_bit_cast(int, x), CTRL, 0xf, 0xf, true));
}
template <int CTRL, int RM> DI float dppm(float x) {
  return __builtin_bit_cast(float, __builtin_amdgcn_update_dpp(0, __builtin_bit_cast(int, x), CTRL, RM, 0xf, false));
}
constexpr int XOR1 = 0xB1, XOR2 = 0x4E, HMIR = 0x141, MIR = 0x140, ROR4 = 0x124, ROR8 = 0x128, BC15 = 0x142, BC31 = 0x143;
constexpr int SHL4 = 0x104, SHL8 = 0x108, SHL12 = 0x10C;
DI float asf(u32 x) { return __builtin_bit_cast(float, x); }
DI u32 asu(float x) { return __builtin_bit_cast(u32, x); }
DI u32 cvtpk(float lo, float hi) { f2 f = {lo, hi}; return __builtin_bit_cast(u32, __builtin_convertvector(f, bf2v)); }
DI f4 mfma(v4u a, v4u b, f4 c) {
  return __builtin_amdgcn_mfma_f32_16x16x32_bf16(__builtin_bit_cast(s8v, a), __builtin_bit_cast(s8v, b), c, 0, 0, 0);
}
DI float xrow_max(float x) {
  auto s = __builtin_amdgcn_permlane16_swap(asu(x), asu(x), false, false);
  x = fmaxf(asf(s[0]), asf(s[1]));
  auto t = __builtin_amdgcn_permlane32_swap(asu(x), asu(x), false, false);
  return fmaxf(asf(t[0]), asf(t[1]));
}
DI float xrow_sum(float x) {
  auto s = __builtin_amdgcn_permlane16_swap(asu(x), asu(x), false, false);
  x = asf(s[0]) + asf(s[1]);
  auto t = __builtin_amdgcn_permlane32_swap(asu(x), asu(x), false, false);
  return asf(t[0]) + asf(t[1]);
}
#define BAR() asm volatile("s_waitcnt lgkmcnt(0)\n\ts_barrier" ::: "memory")
#define CBAR() asm volatile("" ::: "memory")
#define SB() __builtin_amdgcn_sched_barrier(0)
#define WAITV(n) asm volatile("s_waitcnt vmcnt(" #n ")" ::: "memory")

#define STAMP(i)
#define STAMPR(i)
#define STAMPV(i, v)
#define STAMPW(i)

constexpr u32 TMASK = 0x3ffu;
DI u32 tagf(float v, u32 tag) { return ((asu(v) + 0x200u) & ~TMASK) | tag; }
DI float untag(u32 v) { return asf(v & ~TMASK); }

DI u32 ld32s(__amdgpu_buffer_rsrc_t rs, int off) { return __builtin_amdgcn_raw_buffer_load_b32(rs, off, 0, 16); }
DI v4u ld128s(__amdgpu_buffer_rsrc_t rs, int off) { return __builtin_amdgcn_raw_buffer_load_b128(rs, off, 0, 16); }
DI void st128s(v4u v, __amdgpu_buffer_rsrc_t rs, int off) { __builtin_amdgcn_raw_buffer_store_b128(v, rs, off, 0, 16); }

DI void ln_stats(float x, LAS unsigned char* L, int w, int lane, float& mean, float& var) {
  const float c = asf(__builtin_amdgcn_readfirstlane(asu(x)));
  const float d = x - c;
  float s1 = d, s2 = d * d;
  s1 += dpp<XOR1>(s1); s2 += dpp<XOR1>(s2);
  s1 += dpp<XOR2>(s1); s2 += dpp<XOR2>(s2);
  s1 += dpp<HMIR>(s1); s2 += dpp<HMIR>(s2);
  s1 += dpp<MIR>(s1); s2 += dpp<MIR>(s2);
  s1 += dppm<BC15, 0xa>(s1); s2 += dppm<BC15, 0xa>(s2);
  s1 += dppm<BC31, 0xc>(s1); s2 += dppm<BC31, 0xc>(s2);
  if (lane == 63) {
    f2 v = {c + s1 * (1.0f / 64), s2 - s1 * s1 * (1.0f / 64)};
    *(LAS f2*)(L + L_ST + w * 8) = v;
  }
  BAR();
  const f2 v = *(const LAS f2*)(L + L_ST + (lane & 7) * 8);
  float m = v.x;
  m += dpp<XOR1>(m); m += dpp<XOR2>(m); m += dpp<HMIR>(m);
  mean = m * 0.125f;
  const float dv = v.x - mean;
  float t = v.y + 64.0f * dv * dv;
  t += dpp<XOR1>(t); t += dpp<XOR2>(t); t += dpp<HMIR>(t);
  var = t * (1.0f / 512);
}

DI void transpose8(const v4u (&raw)[8], v4u (&fr)[8]) {
#pragma unroll
  for (int i = 0; i < 8; ++i) {
    const u32 sel = (i & 1) ? 0x07060302u : 0x05040100u;
#pragma unroll
    for (int m = 0; m < 4; ++m) fr[i][m] = __builtin_amdgcn_perm(raw[2 * m + 1][i >> 1], raw[2 * m][i >> 1], sel);
  }
}

DI void q_gemv(const v4u (&wf)[8], LAS unsigned char* L, int tid, int w, int lane, int kh, __amdgpu_buffer_rsrc_t rs, int qoff, u32 tag) {
  const int g = lane >> 4, r = lane & 15;
  const int real = L_HB + (256 * kh + 32 * w + 8 * g) * 2;
  f4 acc = {0.f, 0.f, 0.f, 0.f};
#pragma unroll
  for (int i = 0; i < 8; ++i) {
    const v4u a = *(const LAS v4u*)(L + ((r == i) ? real : L_ZERO));
    acc = mfma(a, wf[i], acc);
  }
  if (lane < 32) *(LAS f4*)(L + L_RED + w * 576 + (8 * r + 4 * g) * 4) = acc;
  BAR();
  if (tid < 128) {
    const int n4 = tid >> 2, wp = tid & 3;
    f4 v = *(const LAS f4*)(L + L_RED + wp * 576 + n4 * 16) + *(const LAS f4*)(L + L_RED + (wp + 4) * 576 + n4 * 16);
#pragma unroll
    for (int e = 0; e < 4; ++e) { v[e] += dpp<XOR1>(v[e]); v[e] += dpp<XOR2>(v[e]); }
    if (wp == 0) { v4u o = {tagf(v[0], tag), tagf(v[1], tag), tagf(v[2], tag), tagf(v[3], tag)}; st128s(o, rs, qoff + n4 * 16); }
  }
}

DI float attn_scores(float qv, const v4u (&kf)[16], LAS unsigned char* L, int tid, int w, int lane) {
  const int g = lane >> 4, c = lane & 15, h = c & 3, s4 = c >> 2;
  *(LAS u16*)(L + L_QL + (tid >> 7) * QSTR + (tid & 127) * 2) = (u16)(cvtpk(qv, qv) & 0xffffu);
  BAR();
  v4u qf[4];
#pragma unroll
  for (int st = 0; st < 4; ++st) qf[st] = *(const LAS v4u*)(L + L_QL + h * QSTR + st * 64 + g * 16);
  f4 d[4];
#pragma unroll
  for (int t = 0; t < 4; ++t) {
    d[t] = f4{0.f, 0.f, 0.f, 0.f};
#pragma unroll
    for (int st = 0; st < 4; ++st) d[t] = mfma(kf[t * 4 + st], qf[st], d[t]);
  }
  float sc[4];
#pragma unroll
  for (int i = 0; i < 4; ++i) {
    const float a = (s4 & 1) ? d[1][i] : d[0][i], bq = (s4 & 1) ? d[3][i] : d[2][i];
    sc[i] = (s4 & 2) ? bq : a;
  }
  float m = fmaxf(fmaxf(sc[0], sc[1]), fmaxf(sc[2], sc[3]));
  m = fmaxf(m, dpp<ROR4>(m)); m = fmaxf(m, dpp<ROR8>(m));
  m = xrow_max(m);
  if (lane < 4) *(LAS float*)(L + L_MB + (lane * 8 + w) * 4) = m;
  BAR();
  const f4 m0 = *(const LAS f4*)(L + L_MB + h * 32), m1 = *(const LAS f4*)(L + L_MB + h * 32 + 16);
  float M = fmaxf(fmaxf(fmaxf(m0[0], m0[1]), fmaxf(m0[2], m0[3])), fmaxf(fmaxf(m1[0], m1[1]), fmaxf(m1[2], m1[3])));
  M = asf(asu(M * SCL) & ~TMASK);
  float p[4];
#pragma unroll
  for (int i = 0; i < 4; ++i) p[i] = __builtin_amdgcn_exp2f(fmaf(sc[i], SCL, -M));
  v2u pk = {cvtpk(p[0], p[1]), cvtpk(p[2], p[3])};
  *(LAS v2u*)(L + L_PL + s4 * PSTR + ((2 * (w & 1) + (g >> 1)) * 16 + (w >> 1) * 4 + h) * 16 + (g & 1) * 8) = pk;
  float l = (p[0] + p[1]) + (p[2] + p[3]);
  l += dpp<ROR4>(l); l += dpp<ROR8>(l);
  if (c < 4) *(LAS float*)(L + L_LB + (w * 16 + g * 4 + c) * 4) = l;
  return M;
}

DI void pv_chunk(int t, f4 (&acc)[4], LAS unsigned char* L, int vbase, int lane) {
  const v4u pf = *(const LAS v4u*)(L + L_PL + t * PSTR + lane * 16);
#pragma unroll
  for (int u = 0; u < 4; ++u) {
    const s4v lo = __builtin_amdgcn_ds_read_tr16_b64_v4i16((LAS s4v*)(L + vbase + u * 32768 + t * 4096));
    const s4v hi = __builtin_amdgcn_ds_read_tr16_b64_v4i16((LAS s4v*)(L + vbase + u * 32768 + t * 4096 + 1024));
    const v2u l2 = __builtin_bit_cast(v2u, lo), h2 = __builtin_bit_cast(v2u, hi);
    const v4u a = {l2[0], l2[1], h2[0], h2[1]};
    acc[u] = mfma(a, pf, acc[u]);
  }
}

DI void attn_publish(const f4 (&acc)[4], float M, LAS unsigned char* L, int w, int lane, __amdgpu_buffer_rsrc_t rs, int ooff, int soff, u32 tag, u32 T0) {
  const int g = lane >> 4, c = lane & 15;
  v4u o;
#pragma unroll
  for (int e = 0; e < 4; ++e) {
    float v = acc[0][e];
    v += dpp<SHL4>(acc[1][e]); v += dpp<SHL8>(acc[2][e]); v += dpp<SHL12>(acc[3][e]);
    o[e] = tagf(v, tag);
  }
  if (c < 4) st128s(o, rs, ooff + (c * 128 + 16 * w + 4 * g) * 4);
  if (w == 0) {
    float l = *(const LAS float*)(L + L_LB + lane * 4) + *(const LAS float*)(L + L_LB + (lane + 64) * 4);
    l += dpp<ROR4>(l); l += dpp<ROR8>(l);
    l = xrow_sum(l);
    if (lane < 4) { v4u sv = {asu(M) | tag, tagf(l, tag), T0, ~T0}; st128s(sv, rs, soff + lane * 16); }
  }
}

DI void o_gemv(const v4u (&wf)[8], LAS unsigned char* L, int tid, int w, int lane, __amdgpu_buffer_rsrc_t rs, int obase, int sbase, int poff, u32 tag, u32 T0, int& budget, int sb) {
  const int g = lane >> 4, r = lane & 15;
  u32 o[8]; v4u sv;
  const int oo = obase + (64 * w + lane) * 4, so = sbase + (lane & 7) * 64 + (w >> 1) * 16;
  for (;;) {
    CBAR();
#pragma unroll
    for (int i = 0; i < 8; ++i) o[i] = ld32s(rs, oo + i * 2048);
    sv = ld128s(rs, so);
    u32 d = (sv[0] ^ tag) | (sv[1] ^ tag);
#pragma unroll
    for (int i = 0; i < 8; ++i) d |= o[i] ^ tag;
    d = (d & TMASK) | (sv[2] ^ T0) | (sv[3] ^ ~T0);
    if (__all(d == 0) || --budget <= 0) break;
  }
  STAMP(sb); STAMPV(sb + 1, budget);
  const float ms = untag(sv[0]), ls = untag(sv[1]);
  float M = ms;
  M = fmaxf(M, dpp<XOR1>(M)); M = fmaxf(M, dpp<XOR2>(M)); M = fmaxf(M, dpp<HMIR>(M));
  const float f = __builtin_amdgcn_exp2f(ms - M);
  float Ls = f * ls;
  Ls += dpp<XOR1>(Ls); Ls += dpp<XOR2>(Ls); Ls += dpp<HMIR>(Ls);
  const float cs = f * __builtin_amdgcn_rcpf(Ls);
  float ov = 0.f;
#pragma unroll
  for (int i = 0; i < 8; ++i) ov = fmaf(asf(__builtin_amdgcn_readlane(asu(cs), i)), untag(o[i]), ov);
  *(LAS u16*)(L + L_HB + (64 * w + lane) * 2) = (u16)(cvtpk(ov, ov) & 0xffffu);
  CBAR();
  const int real = L_HB + (64 * w + 32 * (r >> 3) + 8 * g) * 2;
  f4 acc = {0.f, 0.f, 0.f, 0.f};
#pragma unroll
  for (int i = 0; i < 8; ++i) {
    const v4u a = *(const LAS v4u*)(L + (((r & 7) == i) ? real : L_ZERO));
    acc = mfma(a, wf[i], acc);
  }
  if ((g >> 1) == (r >> 3)) *(LAS f4*)(L + L_RED + (2 * w + (g >> 1)) * 320 + (8 * (r & 7) + 4 * (g & 1)) * 4) = acc;
  BAR();
  if (tid < 64) {
    const int n4 = tid >> 2, wp = tid & 3;
    f4 v = (*(const LAS f4*)(L + L_RED + wp * 320 + n4 * 16) + *(const LAS f4*)(L + L_RED + (wp + 4) * 320 + n4 * 16)) +
           (*(const LAS f4*)(L + L_RED + (wp + 8) * 320 + n4 * 16) + *(const LAS f4*)(L + L_RED + (wp + 12) * 320 + n4 * 16));
#pragma unroll
    for (int e = 0; e < 4; ++e) { v[e] += dpp<XOR1>(v[e]); v[e] += dpp<XOR2>(v[e]); }
    if (wp == 0) { v4u ov4 = {tagf(v[0], tag), tagf(v[1], tag), tagf(v[2], tag), tagf(v[3], tag)}; st128s(ov4, rs, poff + n4 * 16); }
  }
}

DI f4 mixer(float& xres, LAS unsigned char* L, int tid, int w, int lane, int j, __amdgpu_buffer_rsrc_t rs, int pbase, u32 tag, float b2, int& budget, int sb) {
  float pw1[7], pb1[7], pw2[7];
  const int fl = tid & 255, q = tid >> 8;
#pragma unroll
  for (int i = 0; i < 7; ++i) {
    pw1[i] = asf((u32) * (const LAS u16*)(L + L_PAR + (fl + 256 * i) * 2) << 16);
    pb1[i] = asf((u32) * (const LAS u16*)(L + L_PAR + 3584 + (fl + 256 * i) * 2) << 16);
    pw2[i] = asf((u32) * (const LAS u16*)(L + L_PAR + 7168 + (fl + 256 * i) * 2) << 16);
  }
  SB();
  if (DLY_CD) __builtin_amdgcn_s_sleep(DLY_CD);
  u32 p[8];
  const int po = pbase + tid * 4;
  for (;;) {
    CBAR();
#pragma unroll
    for (int i = 0; i < 8; ++i) p[i] = ld32s(rs, po + i * 2048);
    u32 d = 0;
#pragma unroll
    for (int i = 0; i < 8; ++i) d |= p[i] ^ tag;
    if (__all((d & TMASK) == 0) || --budget <= 0) break;
  }
  STAMP(sb); STAMPV(sb + 1, budget);
  const float x1 = xres + (((untag(p[0]) + untag(p[1])) + (untag(p[2]) + untag(p[3]))) + ((untag(p[4]) + untag(p[5])) + (untag(p[6]) + untag(p[7]))));
  xres = x1;
  *(LAS float*)(L + L_XL + tid * 4) = x1;
  float mean, var;
  ln_stats(x1, L, w, lane, mean, var);
  const float r1 = __builtin_amdgcn_rsqf(var + EPS);
  const float r2 = __builtin_amdgcn_rsqf(var * r1 * r1 + EPS);
  const float rr = r1 * r2;
  STAMP(sb + 2); if (sb == 22) STAMPW(88);
  const f4 xv = *(const LAS f4*)(L + L_XL + (8 * j + 4 * q) * 4);
  const f2 hA = {(xv[0] - mean) * rr, (xv[1] - mean) * rr}, hB = {(xv[2] - mean) * rr, (xv[3] - mean) * rr};
  f2 aA = {0.f, 0.f}, aB = {0.f, 0.f};
#pragma unroll
  for (int i = 0; i < 7; ++i) {
    const f2 w1 = {pw1[i], pw1[i]}, b1 = {pb1[i], pb1[i]}, w2 = {pw2[i], pw2[i]};
    const f2 k0 = {GK0, GK0}, k1 = {GK1, GK1}, one = {1.f, 1.f};
    {
      const f2 u = hA * w1 + b1, z = u * (u * u * k1 + k0);
      const f2 e = {__builtin_amdgcn_exp2f(z[0]), __builtin_amdgcn_exp2f(z[1])}, dd = e + one;
      const f2 rc = {__builtin_amdgcn_rcpf(dd[0]), __builtin_amdgcn_rcpf(dd[1])};
      aA += (u * rc) * w2;
    }
    {
      const f2 u = hB * w1 + b1, z = u * (u * u * k1 + k0);
      const f2 e = {__builtin_amdgcn_exp2f(z[0]), __builtin_amdgcn_exp2f(z[1])}, dd = e + one;
      const f2 rc = {__builtin_amdgcn_rcpf(dd[0]), __builtin_amdgcn_rcpf(dd[1])};
      aB += (u * rc) * w2;
    }
  }
  float t0 = (lane & 1) ? aA[1] : aA[0], t1 = (lane & 1) ? aA[0] : aA[1];
  t0 += dpp<XOR1>(t1);
  float t2 = (lane & 1) ? aB[1] : aB[0], t3 = (lane & 1) ? aB[0] : aB[1];
  t2 += dpp<XOR1>(t3);
  float s0 = (lane & 2) ? t2 : t0, s1 = (lane & 2) ? t0 : t2;
  s0 += dpp<XOR2>(s1);
  s0 += dpp<ROR4>(s0); s0 += dpp<ROR8>(s0);
  s0 = xrow_sum(s0);
  STAMP(sb + 3); if (sb == 22) STAMPW(96);
  if (lane < 4) *(LAS float*)(L + L_R2 + (w * 4 + lane) * 4) = s0;
  BAR();
  f4 res = {0.f, 0.f, 0.f, 0.f};
  if (w == 0) {
    const int qq = lane & 1;
    const f4 a = (*(const LAS f4*)(L + L_R2 + (qq * 4 + 0) * 16) + *(const LAS f4*)(L + L_R2 + (qq * 4 + 1) * 16)) +
                 (*(const LAS f4*)(L + L_R2 + (qq * 4 + 2) * 16) + *(const LAS f4*)(L + L_R2 + (qq * 4 + 3) * 16));
    const f4 xr = *(const LAS f4*)(L + L_XL + (8 * j + 4 * qq) * 4);
    res = xr + a + f4{b2, b2, b2, b2};
  }
  return res;
}

__global__ void __launch_bounds__(512, 2) fused_decode_kernel(Params p) {
  __shared__ __attribute__((aligned(1024))) unsigned char smem[L_END];
  LAS unsigned char* L = (LAS unsigned char*)smem;
  const int tid = threadIdx.x, lane = tid & 63, w = __builtin_amdgcn_readfirstlane(tid >> 6);
  const int bid = blockIdx.x;
  int b, kvh, s;
  if (MAPMODE == 0) { kvh = bid & 7; b = bid >> 6; s = (bid >> 3) & 7; }
  else { const int x = bid & 7, i = (bid >> 3) + 32 * (x & 1); b = x >> 1; kvh = i >> 3; s = i & 7; }
  const int j = kvh * 8 + s;
  const int g = lane >> 4, c = lane & 15;
  STAMPR(36); STAMP(0);

  const size_t slab = ((size_t)(b * 8 + kvh) * SK + (SK - WIN) + s * 512) * 256;
  const __amdgpu_buffer_rsrc_t rK = __builtin_amdgcn_make_buffer_rsrc((void*)(p.K + slab), 0, 512 * 256, 0x00020000);
  const __amdgpu_buffer_rsrc_t rV = __builtin_amdgcn_make_buffer_rsrc((void*)(p.V + slab), 0, 512 * 256, 0x00020000);
  const __amdgpu_buffer_rsrc_t rQ = __builtin_amdgcn_make_buffer_rsrc((void*)p.wq, 0, 512 * 4096 * 2, 0x00020000);
  const __amdgpu_buffer_rsrc_t rO = __builtin_amdgcn_make_buffer_rsrc((void*)p.wo, 0, 512 * 4096 * 2, 0x00020000);
  const __amdgpu_buffer_rsrc_t rs = __builtin_amdgcn_make_buffer_rsrc((void*)p.ws, 0, WS_END, 0x00020000);
  const __amdgpu_buffer_rsrc_t rX = __builtin_amdgcn_make_buffer_rsrc((void*)p.x, 0, 4096, 0x00020000);
  const u32 ldsb = (u32)(size_t)L;

  v4u dummy = {0u, 0u, 0u, 0u};
  u32 ev, xraw, b2raw;
  asm volatile("s_nop 4\n\tbuffer_load_dword %0, %1, %2, 0 offen sc1" : "=v"(ev) : "v"(WS_EP + b * 256), "s"(rs) : "memory");
  asm volatile("buffer_load_ushort %0, %1, %2, 0 offen" : "=v"(xraw) : "v"((b * 512 + tid) * 2), "s"(rX) : "memory");
  asm volatile("global_load_ushort %0, %1, %2" : "=v"(b2raw) : "v"(0), "s"(p.b2) : "memory");
  const int rot = __builtin_amdgcn_readfirstlane((int)(((u32)(bid * 8 + w) * 2654435761u) >> 28));
  const int cg = s >> 1, kh = s & 1;
  const int voq = ((256 * kh + 32 * w + 8 * g) * 4096 + kvh * 512 + 128 * cg + 8 * c) * 2;
  const int voo = ((kvh * 512 + 64 * w + 32 * (c >> 3) + 8 * g) * 512 + 64 * s + 8 * (c & 7)) * 2;
  int vok[4], vov[2];
#pragma unroll
  for (int i = 0; i < 4; ++i) vok[i] = (64 * w + g) * 256 + ((c ^ (4 * i + g)) * 16);
#pragma unroll
  for (int i = 0; i < 2; ++i) vov[i] = (64 * w + g) * 256 + ((((c >> 1) ^ (g | (i << 2))) * 32) + (c & 1) * 16);
  v4u wqr[8], wor[8], kf[16];
#define LDQ(jj) asm volatile("buffer_load_dwordx4 %0, %1, %2, %3 offen" WPOL : "=v"(wqr[jj]) : "v"(voq), "s"(rQ), "s"((jj) * 8192) : "memory")
#define LDO(jj) asm volatile("buffer_load_dwordx4 %0, %1, %2, %3 offen" WPOL : "=v"(wor[jj]) : "v"(voo), "s"(rO), "s"((jj) * 1024) : "memory")
#define LDK(i) asm volatile("s_mov_b32 m0, %0\n\ts_nop 0\n\tbuffer_load_dwordx4 %1, %2, %3 offen" KVPOL " lds" ::"s"(ldsb + L_KV + w * 16384 + (i) * 1024), "v"(vok[(i) & 3]), "s"(rK), "s"((((i) + rot) & 15) * 1024) : "memory")
#define LDV(i) asm volatile("s_mov_b32 m0, %0\n\ts_nop 0\n\tbuffer_load_dwordx4 %1, %2, %3 offen" KVPOL " lds" ::"s"(ldsb + L_KV + w * 16384 + (i) * 1024), "v"(vov[((i) >> 1) & 1]), "s"(rV), "s"((((i) + rot) & 15) * 1024) : "memory")
#define G() asm volatile("s_waitcnt vmcnt(%0)" ::"n"(WD - 1) : "memory")
#define EV(n, i) asm volatile("s_waitcnt vmcnt(%0)" ::"n"((n) - (i) - 1) : "memory")
#define KFRAG(t)                                                                                                     \
  _Pragma("unroll") for (int st = 0; st < 4; ++st)                                                                   \
      kf[(t) * 4 + st] = *(const LAS v4u*)(L + L_KV + w * 16384 + (16 * (t) + c) * 256 + (((4 * st + g) ^ c) * 16)); \
  asm volatile("" : "+v"(kf[(t) * 4]), "+v"(kf[(t) * 4 + 1]), "+v"(kf[(t) * 4 + 2]), "+v"(kf[(t) * 4 + 3])::"memory")
  LDQ(0); LDK(0); LDQ(1); LDK(1); LDQ(2); LDK(2);
  if (tid < 16) *(LAS u32*)(L + L_ZERO + tid * 4) = 0u;
  G(); LDQ(3); G(); LDK(3); G(); LDQ(4); G(); LDK(4);
  G(); LDQ(5); G(); LDK(5); G(); LDQ(6); G(); LDK(6);
  G(); LDQ(7); G(); LDK(7);
  EV(16, 7); STAMPW(48); KFRAG(0);
  G(); LDK(8); G(); LDK(9); G(); LDK(10); G(); LDK(11); G(); LDK(12);
  EV(21, 14);
  asm volatile("" : "+v"(wqr[0]), "+v"(wqr[1]), "+v"(wqr[2]), "+v"(wqr[3]), "+v"(wqr[4]), "+v"(wqr[5]), "+v"(wqr[6]), "+v"(wqr[7]), "+v"(ev), "+v"(xraw), "+v"(b2raw), "+v"(dummy)::"memory");
  STAMP(1); STAMPW(40);
  const u32 T0 = __builtin_amdgcn_readfirstlane(ev) + 1u;
  const u32 tag = T0 & TMASK;
  v4u wqf[8];
  transpose8(wqr, wqf);
  float xres = asf(xraw << 16);
  {
    float mean, var;
    ln_stats(xres, L, w, lane, mean, var);
    const float hv = (xres - mean) * __builtin_amdgcn_rsqf(var + EPS);
    *(LAS u16*)(L + L_HB + tid * 2) = (u16)(cvtpk(hv, hv) & 0xffffu);
    BAR();
    q_gemv(wqf, L, tid, w, lane, kh, rs, WS_QB + ((b * 8 + kvh) * 2 + kh) * 2048 + cg * 512, tag);
  }
  STAMP(2);
  G(); LDK(13);
  EV(22, 15); KFRAG(1);
  G(); LDK(14); G(); LDK(15);
  {
#pragma unroll
    for (int k = 0; k < 2; ++k) {
      const int piece = k == 0 ? w : 8 + (w % 3);
      int gr = piece * 64 + lane; gr = gr < 672 ? gr : 671;
      const int arr = gr / 224, e = gr - arr * 224;
      const unsigned char* src = (const unsigned char*)(arr == 0 ? p.w1 : arr == 1 ? p.b1 : p.w2) + e * 16;
      G();
      asm volatile("s_mov_b32 m0, %0\n\ts_nop 0\n\tglobal_load_lds_dwordx4 %1, off" ::"s"(ldsb + L_PAR + piece * 1024), "v"(src) : "memory");
    }
  }
  EV(26, 19); KFRAG(2);
  G(); LDV(0); G(); LDV(1);
  STAMP(3);
  u32 q0, q1;
  const int qo = WS_QB + (b * 8 + kvh) * 4096 + tid * 4;
  G(); asm volatile("buffer_load_dword %0, %1, %2, 0 offen sc1" : "=v"(q0) : "v"(qo), "s"(rs) : "memory");
  asm volatile("buffer_load_dword %0, %1, %2, 0 offen offset:2048 sc1" : "=v"(q1) : "v"(qo), "s"(rs) : "memory");
  EV(30, 23); KFRAG(3);
  STAMP(4); STAMPW(56);
  G(); LDV(2); G(); LDV(3); G(); LDO(0);
  G(); LDV(4); G(); LDV(5); G(); LDO(1);
  EV(36, 29);
  asm volatile("" : "+v"(q0), "+v"(q1)::"memory");
  STAMP(5); STAMPW(64);
  int budget = 100000;
  if (!__all((((q0 ^ tag) | (q1 ^ tag)) & TMASK) == 0)) {
    _Pragma("nounroll") do {
      asm volatile("buffer_load_dword %0, %2, %3, 0 offen sc1\n\tbuffer_load_dword %1, %2, %3, 0 offen offset:2048 sc1\n\ts_waitcnt vmcnt(0)" : "=&v"(q0), "=&v"(q1) : "v"(qo), "s"(rs) : "memory");
    } while (!__all((((q0 ^ tag) | (q1 ^ tag)) & TMASK) == 0) && --budget > 0);
  }
  const int vbase = L_KV + (g >> 1) * 16384 + (8 * (g & 1) + (c >> 2)) * 256 + ((w ^ ((c >> 2) | ((g & 1) << 2))) * 32) + (c & 3) * 8;
  f4 acc[4];
  float M = attn_scores(untag(q0) + untag(q1), kf, L, tid, w, lane);
  STAMP(6);
  G(); LDV(6); G(); LDV(7); G(); LDO(2);
  G(); LDV(8); G(); LDV(9); G(); LDO(3);
  G(); LDV(10); G(); LDV(11); G(); LDO(4);
  G(); LDV(12); G(); LDV(13); G(); LDO(5);
  G(); LDV(14); G(); LDV(15); G(); LDO(6); G(); LDO(7);
  EV(52, 43);
  BAR();
#pragma unroll
  for (int u = 0; u < 4; ++u) acc[u] = f4{0.f, 0.f, 0.f, 0.f};
  pv_chunk(0, acc, L, vbase, lane);
  pv_chunk(1, acc, L, vbase, lane);
  pv_chunk(2, acc, L, vbase, lane);
  SB();
  EV(52, 49);
  STAMP(7); STAMPW(72);
  BAR();
  pv_chunk(3, acc, L, vbase, lane);
  attn_publish(acc, M, L, w, lane, rs, WS_OB + ((b * 8 + kvh) * 8 + s) * 2048, WS_OS + ((b * 8 + kvh) * 8 + s) * 64, tag, T0);
  STAMP(8);
  if (w == 0) asm volatile("s_waitcnt vmcnt(2)" ::: "memory"); else asm volatile("s_waitcnt vmcnt(1)" ::: "memory");
  asm volatile("" : "+v"(wor[0]), "+v"(wor[1]), "+v"(wor[2]), "+v"(wor[3]), "+v"(wor[4]), "+v"(wor[5]), "+v"(wor[6]), "+v"(wor[7])::"memory");
  STAMP(9); STAMPW(80);
  v4u wof[8];
  transpose8(wor, wof);
  const float b2 = asf(b2raw << 16);

  if (DLY_B) __builtin_amdgcn_s_sleep(DLY_B);
  o_gemv(wof, L, tid, w, lane, rs, WS_OB + (b * 8 + kvh) * 8 * 2048, WS_OS + (b * 8 + kvh) * 8 * 64, WS_PB + (b * 8 + kvh) * 2048 + s * 256, tag, T0, budget, 20);
  STAMP(10);
  {
    const f4 r = mixer(xres, L, tid, w, lane, j, rs, WS_PB + b * 8 * 2048, tag, b2, budget, 22);
    if (tid < 2) { v4u o = {tagf(r[0], tag), tagf(r[1], tag), tagf(r[2], tag), tagf(r[3], tag)}; st128s(o, rs, WS_XB + b * 2048 + j * 32 + tid * 16); }
  }
  STAMP(11);
  if (DLY_E) __builtin_amdgcn_s_sleep(DLY_E);
  {
    u32 xv;
    for (;;) { CBAR(); xv = ld32s(rs, WS_XB + b * 2048 + tid * 4); if (__all(((xv ^ tag) & TMASK) == 0) || --budget <= 0) break; }
    STAMP(26); STAMPV(27, budget);
    xres = untag(xv);
    float mean, var;
    ln_stats(xres, L, w, lane, mean, var);
    const float hv = (xres - mean) * __builtin_amdgcn_rsqf(var + EPS);
    *(LAS u16*)(L + L_HB + tid * 2) = (u16)(cvtpk(hv, hv) & 0xffffu);
    BAR();
    STAMP(12);
    q_gemv(wqf, L, tid, w, lane, kh, rs, WS_QB + 4 * 8 * 4096 + ((b * 8 + kvh) * 2 + kh) * 2048 + cg * 512, tag);
  }
  STAMP(13);
  if (DLY_A) __builtin_amdgcn_s_sleep(DLY_A);
  {
    const int qo2 = qo + 4 * 8 * 4096;
    for (;;) { CBAR(); q0 = ld32s(rs, qo2); q1 = ld32s(rs, qo2 + 2048); if (__all((((q0 ^ tag) | (q1 ^ tag)) & TMASK) == 0) || --budget <= 0) break; }
    STAMP(14); STAMPV(18, budget);
    M = attn_scores(untag(q0) + untag(q1), kf, L, tid, w, lane);
    BAR();
#pragma unroll
    for (int u = 0; u < 4; ++u) acc[u] = f4{0.f, 0.f, 0.f, 0.f};
    pv_chunk(0, acc, L, vbase, lane);
    pv_chunk(1, acc, L, vbase, lane);
    pv_chunk(2, acc, L, vbase, lane);
    pv_chunk(3, acc, L, vbase, lane);
    attn_publish(acc, M, L, w, lane, rs, WS_OB + (1 << 19) + ((b * 8 + kvh) * 8 + s) * 2048, WS_OS + 16384 + ((b * 8 + kvh) * 8 + s) * 64, tag, T0);
  }
  STAMP(15);
  if (DLY_B) __builtin_amdgcn_s_sleep(DLY_B);
  o_gemv(wof, L, tid, w, lane, rs, WS_OB + (1 << 19) + (b * 8 + kvh) * 8 * 2048, WS_OS + 16384 + (b * 8 + kvh) * 8 * 64, WS_PB + 65536 + (b * 8 + kvh) * 2048 + s * 256, tag, T0, budget, 28);
  STAMP(16);
  {
    const f4 r = mixer(xres, L, tid, w, lane, j, rs, WS_PB + 65536 + b * 8 * 2048, tag, b2, budget, 30);
    if (w == 0) {
      const u32 lo = cvtpk(r[0], r[1]), hi = cvtpk(r[2], r[3]);
      if (lane < 2) { v2u o = {lo, hi}; *(GAS v2u*)((GAS unsigned char*)p.out + (b * 512 + 8 * j + 4 * lane) * 2) = o; }
      if (lane == 0 && j == 0) __builtin_amdgcn_raw_buffer_store_b32(T0, rs, WS_EP + b * 256, 0, 16);
    }
  }
  STAMP(17); STAMPR(37);
}

extern "C" void kernel_launch(void* const* d_in, const int* in_sizes, int n_in, void* d_out, int out_size, void* d_ws, size_t ws_size, hipStream_t stream) {
  Params p;
  p.x = (const u16*)d_in[0]; p.K = (const unsigned char*)d_in[1]; p.V = (const unsigned char*)d_in[2];
  p.wq = (const unsigned char*)d_in[3]; p.wo = (const unsigned char*)d_in[4];
  p.w1 = (const u16*)d_in[6]; p.b1 = (const u16*)d_in[7]; p.w2 = (const u16*)d_in[8]; p.b2 = (const u16*)d_in[9];
  p.out = (u16*)d_out; p.ws = (unsigned char*)d_ws;
  hipLaunchKernelGGL(fused_decode_kernel, dim3(256), dim3(512), 0, stream, p);
}
```

```cpp
#include <hip/hip_runtime.h>

typedef unsigned int u32;
typedef unsigned short u16;
typedef unsigned long long u64;
typedef u32 v4u __attribute__((ext_vector_type(4)));
typedef u32 v2u __attribute__((ext_vector_type(2)));
typedef float f4 __attribute__((ext_vector_type(4)));
typedef float f2 __attribute__((ext_vector_type(2)));
typedef short s8v __attribute__((ext_vector_type(8)));
typedef short s4v __attribute__((ext_vector_type(4)));
typedef __bf16 bf2v __attribute__((ext_vector_type(2)));
#define LAS __attribute__((address_space(3)))
#define GAS __attribute__((address_space(1)))
#define DI __device__ __forceinline__

#define STAMPS 0
#define MAPMODE 0
#define DLY_B 4
#define DLY_CD 12
#define DLY_E 11
#define DLY_A 5
#define STW 0
#define KVPOL " nt"
#define WPOL ""
#define PRETOUCH 2
#define SROT 0
#define WO_EARLY 8
#define OROT 4
#define PROBE 0
#define WD 5
#define CODE_KB 22

constexpr int SK = 16384, WIN = 4096;
constexpr float EPS = 1e-5f;
constexpr float SCL = 0.08838834764831845f * 1.4426950408889634f;
constexpr float GK0 = -2.0f * 0.7978845608028654f * 1.4426950408889634f;
constexpr float GK1 = GK0 * 0.044715f;

constexpr int WS_EP = 0;
constexpr int WS_QB = 4096;
constexpr int WS_OB = WS_QB + 2 * 4 * 8 * 2 * 2048;
constexpr int WS_OS = WS_OB + 2 * 4 * 8 * 8 * 2048;
constexpr int WS_PB = WS_OS + 2 * 4 * 8 * 8 * 128;
constexpr int WS_XB = WS_PB + 2 * 4 * 8 * 2048;
constexpr int WS_END = WS_XB + 4 * 2048;
constexpr int WS_STAMP = 4 << 20;

constexpr int L_KV = 0;
constexpr int L_PAR = 131072;
constexpr int PSTR = 1088;
constexpr int L_PL = L_PAR + 11264;
constexpr int L_HB = L_PL + 4 * PSTR;
constexpr int L_ZERO = L_HB + 1024;
constexpr int QSTR = 288;
constexpr int L_QL = L_ZERO + 64;
constexpr int L_XL = L_QL + 4 * QSTR;
constexpr int L_RED = L_XL + 2048;
constexpr int L_ST = L_RED + 5120;
constexpr int L_MB = L_ST + 64;
constexpr int L_LB = L_MB + 128;
constexpr int L_R2 = L_LB + 512;
constexpr int L_STAMP = L_R2 + 128;
constexpr int L_END = L_STAMP + 1024 + 64;
static_assert(L_END <= 163840, "LDS");

struct Params {
  const u16* x; const unsigned char* K; const unsigned char* V; const unsigned char* wq; const unsigned char* wo;
  const u16* w1; const u16* b1; const u16* w2; const u16* b2; u16* out; unsigned char* ws;
};

template <int CTRL> DI float dpp(float x) {
  return __builtin_bit_cast(float, __builtin_amdgcn_update_dpp(0, __builtin_bit_cast(int, x), CTRL, 0xf, 0xf, true));
}
template <int CTRL, int RM> DI float dppm(float x) {
  return __builtin_bit_cast(float, __builtin_amdgcn_update_dpp(0, __builtin_bit_cast(int, x), CTRL, RM, 0xf, false));
}
constexpr int XOR1 = 0xB1, XOR2 = 0x4E, HMIR = 0x141, MIR = 0x140, ROR4 = 0x124, ROR8 = 0x128, BC15 = 0x142, BC31 = 0x143;
constexpr int SHL4 = 0x104, SHL8 = 0x108, SHL12 = 0x10C;
DI float asf(u32 x) { return __builtin_bit_cast(float, x); }
DI u32 asu(float x) { return __builtin_bit_cast(u32, x); }
DI u32 cvtpk(float lo, float hi) { f2 f = {lo, hi}; return __builtin_bit_cast(u32, __builtin_convertvector(f, bf2v)); }
DI f4 mfma(v4u a, v4u b, f4 c) {
  return __builtin_amdgcn_mfma_f32_16x16x32_bf16(__builtin_bit_cast(s8v, a), __builtin_bit_cast(s8v, b), c, 0, 0, 0);
}
DI float fmx(float a, float b) { return __builtin_elementwise_maximum(a, b); }
DI float xrow_max(float x) {
  auto s = __builtin_amdgcn_permlane16_swap(asu(x), asu(x), false, false);
  x = fmx(asf(s[0]), asf(s[1]));
  auto t = __builtin_amdgcn_permlane32_swap(asu(x), asu(x), false, false);
  return fmx(asf(t[0]), asf(t[1]));
}
DI u32 xo(u32 a, u32 t, u32 acc) { return __builtin_amdgcn_bitop3_b32(a, t, acc, 0xBE); }
#define PIN1(a) asm volatile("" : "+v"(a))
#define PIN2(a, b) asm volatile("" : "+v"(a), "+v"(b))
#define PIN4(a, b, c, d) asm volatile("" : "+v"(a), "+v"(b), "+v"(c), "+v"(d))
#define PIN8(x) asm volatile("" : "+v"(x[0]), "+v"(x[1]), "+v"(x[2]), "+v"(x[3]), "+v"(x[4]), "+v"(x[5]), "+v"(x[6]), "+v"(x[7]))
DI float xrow_sum(float x) {
  auto s = __builtin_amdgcn_permlane16_swap(asu(x), asu(x), false, false);
  x = asf(s[0]) + asf(s[1]);
  auto t = __builtin_amdgcn_permlane32_swap(asu(x), asu(x), false, false);
  return asf(t[0]) + asf(t[1]);
}
#define BAR() asm volatile("s_waitcnt lgkmcnt(0)\n\ts_barrier" ::: "memory")
#define CBAR() asm volatile("" ::: "memory")
#define SB() __builtin_amdgcn_sched_barrier(0)
#define WAITV(n) asm volatile("s_waitcnt vmcnt(" #n ")" ::: "memory")

#define STAMP(i) asm volatile("; MARK " #i)
#define STAMPR(i)
#define STAMPV(i, v)
#define STAMPW(i)

constexpr u32 TMASK = 0x3ffu;
DI u32 tagf(float v, u32 tag) { return __builtin_amdgcn_bitop3_b32(asu(v) + 0x200u, TMASK, tag, 0xBA); }
DI float untag(u32 v) { return asf(v & ~TMASK); }

DI u32 ld32s(__amdgpu_buffer_rsrc_t rs, int off) { return __builtin_amdgcn_raw_buffer_load_b32(rs, off, 0, 16); }
DI v4u ld128s(__amdgpu_buffer_rsrc_t rs, int off) { return __builtin_amdgcn_raw_buffer_load_b128(rs, off, 0, 16); }
DI void st128s(v4u v, __amdgpu_buffer_rsrc_t rs, int off) { __builtin_amdgcn_raw_buffer_store_b128(v, rs, off, 0, 16); }

DI void ln_stats(float x, LAS unsigned char* L, int w, int lane, float& mean, float& var) {
  const float c = asf(__builtin_amdgcn_readfirstlane(asu(x)));
  const float d = x - c;
  float s1 = d, s2 = d * d;
  s1 += dpp<XOR1>(s1); s2 += dpp<XOR1>(s2);
  s1 += dpp<XOR2>(s1); s2 += dpp<XOR2>(s2);
  s1 += dpp<HMIR>(s1); s2 += dpp<HMIR>(s2);
  s1 += dpp<MIR>(s1); s2 += dpp<MIR>(s2);
  s1 += dppm<BC15, 0xa>(s1); s2 += dppm<BC15, 0xa>(s2);
  s1 += dppm<BC31, 0xc>(s1); s2 += dppm<BC31, 0xc>(s2);
  const f2 pv = {fmaf(s1, 1.0f / 64, c), fmaf(s1 * s1, -1.0f / 64, s2)};
  if (lane == 63) *(LAS f2*)(L + L_ST + w * 8) = pv;
  BAR();
  const f2 v = *(const LAS f2*)(L + L_ST + (lane & 7) * 8);
  float m = v.x;
  m += dpp<XOR1>(m); m += dpp<XOR2>(m); m += dpp<HMIR>(m);
  mean = m * 0.125f;
  const float dv = v.x - mean;
  float t = fmaf(64.0f * dv, dv, v.y);
  t += dpp<XOR1>(t); t += dpp<XOR2>(t); t += dpp<HMIR>(t);
  var = t * (1.0f / 512);
}

DI void transpose8(const v4u (&raw)[8], v4u (&fr)[8]) {
#pragma unroll
  for (int i = 0; i < 8; ++i) {
    const u32 sel = (i & 1) ? 0x07060302u : 0x05040100u;
#pragma unroll
    for (int m = 0; m < 4; ++m) fr[i][m] = __builtin_amdgcn_perm(raw[2 * m + 1][i >> 1], raw[2 * m][i >> 1], sel);
  }
}

DI v4u q_operand(LAS unsigned char* L, int xaddr, float mean, float var) {
  const f4 a = *(const LAS f4*)(L + xaddr), b = *(const LAS f4*)(L + xaddr + 16);
  const float rs_ = __builtin_amdgcn_rsqf(var + EPS), mr = -mean * rs_;
  return v4u{cvtpk(fmaf(a[0], rs_, mr), fmaf(a[1], rs_, mr)), cvtpk(fmaf(a[2], rs_, mr), fmaf(a[3], rs_, mr)),
             cvtpk(fmaf(b[0], rs_, mr), fmaf(b[1], rs_, mr)), cvtpk(fmaf(b[2], rs_, mr), fmaf(b[3], rs_, mr))};
}

DI void q_gemv(const v4u (&wf)[8], v4u a, LAS unsigned char* L, int tid, int lane, int wraddr, int rdaddr, __amdgpu_buffer_rsrc_t rs, int qoff, u32 tag) {
  f4 d[8];
#pragma unroll
  for (int i = 0; i < 8; ++i) d[i] = mfma(a, wf[i], f4{0.f, 0.f, 0.f, 0.f});
  f4 v;
#pragma unroll
  for (int m = 0; m < 4; ++m) v[m] = (lane & 16) ? d[4 + m][0] : d[m][0];
  if (lane < 32) *(LAS f4*)(L + wraddr) = v;
  BAR();
  if (tid < 128) {
    f4 r = *(const LAS f4*)(L + rdaddr) + *(const LAS f4*)(L + rdaddr + 4 * 576);
#pragma unroll
    for (int e = 0; e < 4; ++e) { float t = r[e]; t += dpp<XOR1>(t); t += dpp<XOR2>(t); r[e] = t; }
    if ((tid & 3) == 0) { v4u o = {tagf(r[0], tag), tagf(r[1], tag), tagf(r[2], tag), tagf(r[3], tag)}; st128s(o, rs, qoff); }
  }
}

DI float attn_scores(float qv, const v4u (&kf)[16], LAS unsigned char* L, int qwr, int qrd, int mwr, int mrd, int pwr, int lwr, int lane) {
  *(LAS u16*)(L + qwr) = (u16)(cvtpk(qv, qv) & 0xffffu);
  BAR();
  v4u qf[4];
#pragma unroll
  for (int st = 0; st < 4; ++st) qf[st] = *(const LAS v4u*)(L + qrd + st * 64);
  SB();
  f4 d[4];
#pragma unroll
  for (int st = 0; st < 4; ++st)
#pragma unroll
    for (int t = 0; t < 4; ++t) d[t] = mfma(kf[t * 4 + st], qf[st], st == 0 ? f4{0.f, 0.f, 0.f, 0.f} : d[t]);
  float sc[4];
#pragma unroll
  for (int i = 0; i < 4; ++i) {
    const float a = (lane & 4) ? d[1][i] : d[0][i], bq = (lane & 4) ? d[3][i] : d[2][i];
    sc[i] = (lane & 8) ? bq : a;
  }
  float m = fmx(fmx(sc[0], sc[1]), fmx(sc[2], sc[3]));
  m = fmx(m, dpp<ROR4>(m)); m = fmx(m, dpp<ROR8>(m));
  m = xrow_max(m);
  if (lane < 4) *(LAS float*)(L + mwr) = m;
  BAR();
  const f4 m0 = *(const LAS f4*)(L + mrd), m1 = *(const LAS f4*)(L + mrd + 16);
  float M = fmx(fmx(fmx(m0[0], m0[1]), fmx(m0[2], m0[3])), fmx(fmx(m1[0], m1[1]), fmx(m1[2], m1[3])));
  M = asf(asu(M * SCL) & ~TMASK);
  float p[4];
#pragma unroll
  for (int i = 0; i < 4; ++i) p[i] = __builtin_amdgcn_exp2f(fmaf(sc[i], SCL, -M));
  v2u pk = {cvtpk(p[0], p[1]), cvtpk(p[2], p[3])};
  *(LAS v2u*)(L + pwr) = pk;
  float l = (p[0] + p[1]) + (p[2] + p[3]);
  l += dpp<ROR4>(l); l += dpp<ROR8>(l);
  if ((lane & 12) == 0) *(LAS float*)(L + lwr) = l;
  return M;
}

DI v4u v_frag(int t, int u, LAS unsigned char* L, int vbase) {
  const s4v lo = __builtin_amdgcn_ds_read_tr16_b64_v4i16((LAS s4v*)(L + vbase + u * 32768 + t * 4096));
  const s4v hi = __builtin_amdgcn_ds_read_tr16_b64_v4i16((LAS s4v*)(L + vbase + u * 32768 + t * 4096 + 1024));
  const v2u l2 = __builtin_bit_cast(v2u, lo), h2 = __builtin_bit_cast(v2u, hi);
  return v4u{l2[0], l2[1], h2[0], h2[1]};
}
DI void pv_chunk(int t, f4 (&acc)[4], LAS unsigned char* L, int vbase, int lane) {
  const v4u pf = *(const LAS v4u*)(L + L_PL + t * PSTR + lane * 16);
#pragma unroll
  for (int u = 0; u < 4; ++u) acc[u] = mfma(v_frag(t, u, L, vbase), pf, acc[u]);
}

DI void attn_publish_stats(float M, LAS unsigned char* L, int w, int lane, __amdgpu_buffer_rsrc_t rs, int saddr, u32 tag, u32 T0) {
  if (w == 7) {
    float l = *(const LAS float*)(L + L_LB + lane * 4) + *(const LAS float*)(L + L_LB + (lane + 64) * 4);
    l += dpp<ROR4>(l); l += dpp<ROR8>(l);
    l = xrow_sum(l);
    if (lane < 8) { v4u sv = {asu(M) | tag, tagf(l, tag), T0, T0}; st128s(sv, rs, saddr); }
  }
}
DI void attn_publish(const f4 (&acc)[4], int lane, __amdgpu_buffer_rsrc_t rs, int oaddr, u32 T0) {
  float v[4];
#pragma unroll
  for (int e = 0; e < 4; ++e) {
    float t = acc[0][e];
    t += dpp<SHL4>(acc[1][e]); t += dpp<SHL8>(acc[2][e]); t += dpp<SHL12>(acc[3][e]);
    v[e] = t;
  }
  const u32 p01 = cvtpk(v[0], v[1]), p23 = cvtpk(v[2], v[3]);
  const v4u o = {__builtin_amdgcn_perm(p01, T0, 0x05040100u), __builtin_amdgcn_perm(p01, T0, 0x07060100u),
                 __builtin_amdgcn_perm(p23, T0, 0x05040100u), __builtin_amdgcn_perm(p23, T0, 0x07060100u)};
  if ((lane & 12) == 0) st128s(o, rs, oaddr);
}

DI bool o_check(const v4u& x0, const v4u& x1, const v4u& sv, u32 T0) {
  u32 d = xo(x0[0], T0, 0u);
  d = xo(x0[1], T0, d); d = xo(x0[2], T0, d); d = xo(x0[3], T0, d);
  d = xo(x1[0], T0, d); d = xo(x1[1], T0, d); d = xo(x1[2], T0, d); d = xo(x1[3], T0, d);
  d = xo(sv[0], T0, d); d = xo(sv[1], T0, d);
  d = xo(sv[3], T0, xo(sv[2], T0, d & TMASK));
  return __all(d == 0);
}
template <bool FIRST>
DI void o_gemv(v4u (&wf)[8], v4u (&wraw)[8], LAS unsigned char* L, int tid, int lane, __amdgpu_buffer_rsrc_t rs, int oo, int so, int pb, int xw, int xr, int wraddr, int rdaddr, int poff, u32 tag, u32 T0, int& budget, int w, int sb) {
  v4u x0, x1, sv = {T0, T0, T0, T0};
  CBAR();
  x0 = ld128s(rs, oo); x1 = ld128s(rs, oo + 512);
  if (lane < 8) sv = ld128s(rs, so);
  if (FIRST) {
    asm volatile("s_waitcnt vmcnt(3)" : "+v"(wraw[0]), "+v"(wraw[1]), "+v"(wraw[2]), "+v"(wraw[3]), "+v"(wraw[4]), "+v"(wraw[5]), "+v"(wraw[6]), "+v"(wraw[7])::"memory");
    STAMP(9); STAMPW(80);
    transpose8(wraw, wf);
    PIN8(wf);
  }
  while (!o_check(x0, x1, sv, T0) && --budget > 0) {
    CBAR();
    x0 = ld128s(rs, oo); x1 = ld128s(rs, oo + 512);
    if (lane < 8) sv = ld128s(rs, so);
  }
  STAMP(sb); STAMPV(sb + 1, budget); if (sb == 28) STAMPW(104);
  const v4u pk = {__builtin_amdgcn_perm(x0[1], x0[0], 0x07060302u), __builtin_amdgcn_perm(x0[3], x0[2], 0x07060302u),
                  __builtin_amdgcn_perm(x1[1], x1[0], 0x07060302u), __builtin_amdgcn_perm(x1[3], x1[2], 0x07060302u)};
  *(LAS v4u*)(L + xw) = pk;
  CBAR();
  const v4u a = *(const LAS v4u*)(L + xr);
  const float ms = untag(sv[0]), ls = untag(sv[1]);
  float M = ms;
  M = fmx(M, dpp<XOR1>(M)); M = fmx(M, dpp<XOR2>(M)); M = fmx(M, dpp<HMIR>(M));
  const float f = __builtin_amdgcn_exp2f(ms - M);
  float Ls = f * ls;
  Ls += dpp<XOR1>(Ls); Ls += dpp<XOR2>(Ls); Ls += dpp<HMIR>(Ls);
  const float cs = f * __builtin_amdgcn_rcpf(Ls);
  f4 d[8];
#pragma unroll
  for (int i = 0; i < 8; ++i) d[i] = mfma(a, wf[i], f4{0.f, 0.f, 0.f, 0.f});
  float cc[8];
#pragma unroll
  for (int i = 0; i < 8; ++i) cc[i] = asf(__builtin_amdgcn_readlane(asu(cs), i));
  const bool odd = lane & 16;
  const f2 c01 = {odd ? cc[4] : cc[0], odd ? cc[5] : cc[1]}, c23 = {odd ? cc[6] : cc[2], odd ? cc[7] : cc[3]};
  float e[8];
#pragma unroll
  for (int i = 0; i < 8; ++i) { const f2 t = f2{d[i][2], d[i][3]} * c23 + f2{d[i][0], d[i][1]} * c01; e[i] = t[0] + t[1]; }
  f4 v;
#pragma unroll
  for (int m = 0; m < 4; ++m) { auto t = __builtin_amdgcn_permlane16_swap(asu(e[m]), asu(e[m + 4]), false, false); v[m] = asf(t[0]) + asf(t[1]); }
  if ((lane >> 5) == ((lane >> 3) & 1)) *(LAS f4*)(L + wraddr) = v;
  BAR();
  if (tid < 64) {
    f4 r = (*(const LAS f4*)(L + rdaddr) + *(const LAS f4*)(L + rdaddr + 4 * 320)) + (*(const LAS f4*)(L + rdaddr + 8 * 320) + *(const LAS f4*)(L + rdaddr + 12 * 320));
#pragma unroll
    for (int k = 0; k < 4; ++k) { float t = r[k]; t += dpp<XOR1>(t); t += dpp<XOR2>(t); r[k] = t; }
    if ((tid & 3) == 0) { v4u ov4 = {tagf(r[0], tag), tagf(r[1], tag), tagf(r[2], tag), tagf(r[3], tag)}; st128s(ov4, rs, poff); }
  }
}

DI f4 mixer(float& xres, LAS unsigned char* L, int tid, int w, int lane, int j, __amdgpu_buffer_rsrc_t rs, int pbase, u32 tag, float b2, int& budget, int sb) {
  float pw1[7], pb1[7], pw2[7];
  const int fl = tid & 255, q = tid >> 8;
#pragma unroll
  for (int i = 0; i < 7; ++i) {
    pw1[i] = asf((u32) * (const LAS u16*)(L + L_PAR + (fl + 256 * i) * 2) << 16);
    pb1[i] = asf((u32) * (const LAS u16*)(L + L_PAR + 3584 + (fl + 256 * i) * 2) << 16);
    pw2[i] = asf((u32) * (const LAS u16*)(L + L_PAR + 7168 + (fl + 256 * i) * 2) << 16);
  }
#pragma unroll
  for (int i = 0; i < 7; ++i) asm volatile("" : "+v"(pw1[i]), "+v"(pb1[i]), "+v"(pw2[i]));
  int po = pbase + tid * 4, xa = L_XL + (8 * j + 4 * q) * 4, xw = L_XL + tid * 4;
  asm volatile("" : "+v"(po), "+v"(xa), "+v"(xw));
  if (DLY_CD) __builtin_amdgcn_s_sleep(DLY_CD);
  u32 p[8];
  for (;;) {
    CBAR();
#pragma unroll
    for (int i = 0; i < 8; ++i) p[i] = ld32s(rs, po + i * 2048) ^ tag;
    const u32 d = (p[0] | p[1] | p[2]) | (p[3] | p[4] | p[5]) | (p[6] | p[7]);
    if (__all((d & TMASK) == 0) || --budget <= 0) break;
  }
  STAMP(sb); STAMPV(sb + 1, budget); if (sb == 30) STAMPW(112);
  const float x1 = xres + (((asf(p[0]) + asf(p[1])) + (asf(p[2]) + asf(p[3]))) + ((asf(p[4]) + asf(p[5])) + (asf(p[6]) + asf(p[7]))));
  xres = x1;
  *(LAS float*)(L + xw) = x1;
  float mean, var;
  ln_stats(x1, L, w, lane, mean, var);
  const f4 xv = *(const LAS f4*)(L + xa);
  const float r1 = __builtin_amdgcn_rsqf(var + EPS);
  const float r2 = __builtin_amdgcn_rsqf(var * r1 * r1 + EPS);
  const float rr = r1 * r2, mr = -mean * rr;
  STAMP(sb + 2); if (sb == 22) STAMPW(88);
  const f2 hA = {fmaf(xv[0], rr, mr), fmaf(xv[1], rr, mr)}, hB = {fmaf(xv[2], rr, mr), fmaf(xv[3], rr, mr)};
  f2 aA = {0.f, 0.f}, aB = {0.f, 0.f};
#pragma unroll
  for (int i = 0; i < 7; ++i) {
    const f2 w1 = {pw1[i], pw1[i]}, b1 = {pb1[i], pb1[i]}, w2 = {pw2[i], pw2[i]};
    const f2 k0 = {GK0, GK0}, k1 = {GK1, GK1}, one = {1.f, 1.f};
    {
      const f2 u = hA * w1 + b1, z = u * (u * u * k1 + k0);
      const f2 e = {__builtin_amdgcn_exp2f(z[0]), __builtin_amdgcn_exp2f(z[1])}, dd = e + one;
      const f2 rc = {__builtin_amdgcn_rcpf(dd[0]), __builtin_amdgcn_rcpf(dd[1])};
      aA += (u * rc) * w2;
    }
    {
      const f2 u = hB * w1 + b1, z = u * (u * u * k1 + k0);
      const f2 e = {__builtin_amdgcn_exp2f(z[0]), __builtin_amdgcn_exp2f(z[1])}, dd = e + one;
      const f2 rc = {__builtin_amdgcn_rcpf(dd[0]), __builtin_amdgcn_rcpf(dd[1])};
      aB += (u * rc) * w2;
    }
  }
  float t0 = (lane & 1) ? aA[1] : aA[0], t1 = (lane & 1) ? aA[0] : aA[1];
  t0 += dpp<XOR1>(t1);
  float t2 = (lane & 1) ? aB[1] : aB[0], t3 = (lane & 1) ? aB[0] : aB[1];
  t2 += dpp<XOR1>(t3);
  float s0 = (lane & 2) ? t2 : t0, s1 = (lane & 2) ? t0 : t2;
  s0 += dpp<XOR2>(s1);
  s0 += dpp<ROR4>(s0); s0 += dpp<ROR8>(s0);
  s0 = xrow_sum(s0);
  STAMP(sb + 3); if (sb == 22) STAMPW(96);
  if (lane < 4) *(LAS float*)(L + L_R2 + (w * 4 + lane) * 4) = s0;
  BAR();
  f4 res = {0.f, 0.f, 0.f, 0.f};
  if (w == 0) {
    const int qq = lane & 1;
    const f4 a = (*(const LAS f4*)(L + L_R2 + (qq * 4 + 0) * 16) + *(const LAS f4*)(L + L_R2 + (qq * 4 + 1) * 16)) +
                 (*(const LAS f4*)(L + L_R2 + (qq * 4 + 2) * 16) + *(const LAS f4*)(L + L_R2 + (qq * 4 + 3) * 16));
    const f4 xr = *(const LAS f4*)(L + L_XL + (8 * j + 4 * qq) * 4);
    res = xr + a + f4{b2, b2, b2, b2};
  }
  return res;
}

__global__ void __launch_bounds__(512, 2) fused_decode_kernel(Params p) {
  __shared__ __attribute__((aligned(1024))) unsigned char smem[L_END];
  LAS unsigned char* L = (LAS unsigned char*)smem;
  const int tid = threadIdx.x, lane = tid & 63, w = __builtin_amdgcn_readfirstlane(tid >> 6);
  const int bid = blockIdx.x;
  int b, kvh, s;
  if (MAPMODE == 0) { kvh = bid & 7; b = bid >> 6; s = ((bid >> 3) + SROT) & 7; }
  else { const int x = bid & 7, i = (bid >> 3) + 32 * (x & 1); b = x >> 1; kvh = i >> 3; s = i & 7; }
  const int j = kvh * 8 + s;
  const int g = lane >> 4, c = lane & 15;
  STAMPR(36); STAMP(0);

  const size_t slab = ((size_t)(b * 8 + kvh) * SK + (SK - WIN) + s * 512) * 256;
  const __amdgpu_buffer_rsrc_t rK = __builtin_amdgcn_make_buffer_rsrc((void*)(p.K + slab), 0, 512 * 256, 0x00020000);
  const __amdgpu_buffer_rsrc_t rV = __builtin_amdgcn_make_buffer_rsrc((void*)(p.V + slab), 0, 512 * 256, 0x00020000);
  const __amdgpu_buffer_rsrc_t rQ = __builtin_amdgcn_make_buffer_rsrc((void*)p.wq, 0, 512 * 4096 * 2, 0x00020000);
  const __amdgpu_buffer_rsrc_t rO = __builtin_amdgcn_make_buffer_rsrc((void*)p.wo, 0, 512 * 4096 * 2, 0x00020000);
  const __amdgpu_buffer_rsrc_t rs = __builtin_amdgcn_make_buffer_rsrc((void*)p.ws, 0, WS_END, 0x00020000);
  const __amdgpu_buffer_rsrc_t rX = __builtin_amdgcn_make_buffer_rsrc((void*)p.x, 0, 4096, 0x00020000);
  const u32 ldsb = (u32)(size_t)L;

  v4u dummy = {0u, 0u, 0u, 0u};
  {
    const int piece = (bid >> 3) * 8 + w;
    if (piece < CODE_KB) {
      u64 pc; asm volatile("s_getpc_b64 %0" : "=s"(pc));
      const u64 a = (pc & ~(u64)1023) + (u64)piece * 1024 + lane * 16;
      asm volatile("global_load_dwordx4 %0, %1, off" : "+v"(dummy) : "v"(a) : "memory");
    }
  }
  u32 ev, xraw, b2raw, dummy2 = 0;
  asm volatile("s_nop 4\n\tbuffer_load_dword %0, %1, %2, 0 offen sc1" : "=v"(ev) : "v"(WS_EP + b * 256), "s"(rs) : "memory");
  asm volatile("buffer_load_ushort %0, %1, %2, 0 offen" : "=v"(xraw) : "v"((b * 512 + tid) * 2), "s"(rX) : "memory");
  asm volatile("global_load_ushort %0, %1, %2" : "=v"(b2raw) : "v"(0), "s"(p.b2) : "memory");
  if (w == 7) {
    const int me = (b * 8 + kvh) * 8 + s;
    int a_ = lane < 16 ? WS_OB + me * 2048 + lane * 128
           : lane < 32 ? WS_OB + (1 << 19) + me * 2048 + (lane - 16) * 128
           : lane < 34 ? WS_OS + (lane - 32) * 32768 + me * 128
           : lane < 42 ? WS_QB + ((lane - 34) >> 2) * (4 * 8 * 4096) + ((b * 8 + kvh) * 2 + (s & 1)) * 2048 + (s >> 1) * 512 + ((lane - 34) & 3) * 128
           : lane < 46 ? WS_PB + ((lane - 42) >> 1) * 65536 + (b * 8 + kvh) * 2048 + ((s + OROT) & 7) * 256 + ((lane - 42) & 1) * 128
           : WS_XB + b * 2048 + j * 32;
    asm volatile("buffer_load_dword %0, %1, %2, 0 offen" : "+v"(dummy2) : "v"(a_), "s"(rs) : "memory");
  }
  const int rot = __builtin_amdgcn_readfirstlane((int)(((u32)(bid * 8 + w) * 2654435761u) >> 28));
  const int cg = s >> 1, kh = s & 1;
  const int voq = ((256 * kh + 32 * w + 8 * g) * 4096 + kvh * 512 + 128 * cg + 8 * c) * 2;
  const int so_ = (s + OROT) & 7;
  const int voo = ((kvh * 512 + 64 * w + 16 * (c >> 3) + 4 * g) * 512 + 64 * so_ + 8 * (c & 7)) * 2;
  int vok[4], vov[2];
#pragma unroll
  for (int i = 0; i < 4; ++i) vok[i] = (64 * w + g) * 256 + ((c ^ (4 * i + g)) * 16);
#pragma unroll
  for (int i = 0; i < 2; ++i) vov[i] = (64 * w + g) * 256 + ((((c >> 1) ^ (g | (i << 2))) * 32) + (c & 1) * 16);
  v4u wqr[8], wor[8], kf[16];
#define LDQ(jj) asm volatile("buffer_load_dwordx4 %0, %1, %2, %3 offen" WPOL : "=v"(wqr[jj]) : "v"(voq), "s"(rQ), "s"((jj) * 8192) : "memory")
#define LDO(jj) asm volatile("buffer_load_dwordx4 %0, %1, %2, %3 offen" WPOL : "=v"(wor[jj]) : "v"(voo), "s"(rO), "s"((32 * ((jj) >> 2) + ((jj) & 3)) * 1024) : "memory")
#define LDK(i) asm volatile("s_mov_b32 m0, %0\n\ts_nop 0\n\tbuffer_load_dwordx4 %1, %2, %3 offen" KVPOL " lds" ::"s"(ldsb + L_KV + w * 16384 + (i) * 1024), "v"(vok[(i) & 3]), "s"(rK), "s"((((i) + rot) & 15) * 1024) : "memory")
#define LDV(i) asm volatile("s_mov_b32 m0, %0\n\ts_nop 0\n\tbuffer_load_dwordx4 %1, %2, %3 offen" KVPOL " lds" ::"s"(ldsb + L_KV + w * 16384 + (i) * 1024), "v"(vov[((i) >> 1) & 1]), "s"(rV), "s"((((i) + rot) & 15) * 1024) : "memory")
#define G() asm volatile("s_waitcnt vmcnt(%0)" ::"n"(WD - 1) : "memory")
#define EV(n, i) asm volatile("s_waitcnt vmcnt(%0)" ::"n"((n) - (i) - 1) : "memory")
#define KFRAG(t)                                                                                                     \
  _Pragma("unroll") for (int st = 0; st < 4; ++st)                                                                   \
      kf[(t) * 4 + st] = *(const LAS v4u*)(L + L_KV + w * 16384 + (16 * (t) + c) * 256 + (((4 * st + g) ^ c) * 16)); \
  asm volatile("" : "+v"(kf[(t) * 4]), "+v"(kf[(t) * 4 + 1]), "+v"(kf[(t) * 4 + 2]), "+v"(kf[(t) * 4 + 3])::"memory")
  LDQ(0); LDK(0); LDQ(1); LDK(1); LDQ(2); LDK(2);
  G(); LDQ(3); G(); LDK(3);
  asm volatile("" : "+v"(ev), "+v"(xraw), "+v"(b2raw), "+v"(dummy), "+v"(dummy2)::"memory");
  const u32 T0 = __builtin_amdgcn_readfirstlane(ev) + 1u;
  const u32 tag = T0 & TMASK;
  float xres = asf(xraw << 16);
  const int xqa = L_XL + (256 * kh + 32 * w + 8 * g) * 4;
  v4u qa;
  {
    *(LAS float*)(L + L_XL + tid * 4) = xres;
    float mean, var;
    ln_stats(xres, L, w, lane, mean, var);
    qa = q_operand(L, xqa, mean, var);
    PIN1(qa);
  }
  G(); LDQ(4); G(); LDK(4); G(); LDQ(5); G(); LDK(5);
  G(); LDQ(6); G(); LDK(6); G(); LDQ(7); G(); LDK(7);
  EV(16, 7); STAMPW(48); KFRAG(0);
  G(); LDK(8); G(); LDK(9); G(); LDK(10); G(); LDK(11); G(); LDK(12);
  EV(21, 14);
  asm volatile("" : "+v"(wqr[0]), "+v"(wqr[1]), "+v"(wqr[2]), "+v"(wqr[3]), "+v"(wqr[4]), "+v"(wqr[5]), "+v"(wqr[6]), "+v"(wqr[7])::"memory");
  STAMP(1); STAMPW(40);
  LDK(13); LDK(14); LDK(15);
  {
#pragma unroll
    for (int k = 0; k < 2; ++k) {
      const int piece = k == 0 ? w : 8 + (w % 3);
      int gr = piece * 64 + lane; gr = gr < 672 ? gr : 671;
      const int arr = gr / 224, e = gr - arr * 224;
      const unsigned char* src = (const unsigned char*)(arr == 0 ? p.w1 : arr == 1 ? p.b1 : p.w2) + e * 16;
      asm volatile("s_mov_b32 m0, %0\n\ts_nop 0\n\tglobal_load_lds_dwordx4 %1, off" ::"s"(ldsb + L_PAR + piece * 1024), "v"(src) : "memory");
    }
  }
  LDV(0); LDV(1);
  v4u wqf[8];
  transpose8(wqr, wqf);
  const int qwr = L_RED + w * 576 + (8 * c + 4 * (g & 1)) * 4, qrd = L_RED + (tid & 3) * 576 + (tid >> 2) * 16;
  const int qpub = WS_QB + ((b * 8 + kvh) * 2 + kh) * 2048 + cg * 512 + (tid >> 2) * 16;
  q_gemv(wqf, qa, L, tid, lane, qwr, qrd, rs, qpub, tag);
  STAMP(2);
  EV(28, 15); KFRAG(1);
  G(); LDV(2); G(); LDV(3);
  EV(30, 19); KFRAG(2);
#define CNT(k) ((k) < WO_EARLY ? (k) : WO_EARLY)
#define LDOE(k) if ((k) < WO_EARLY) { G(); LDO(k); }
#define LDOU(k) if ((k) < WO_EARLY) { LDO(k); }
#define WAITN(n) asm volatile("s_waitcnt vmcnt(%0)" ::"n"(n) : "memory")
  LDOE(0); G(); LDV(4); G(); LDV(5);
  STAMP(3);
  u32 q0, q1;
  const int qo = WS_QB + (b * 8 + kvh) * 4096 + tid * 4;
  G(); asm volatile("buffer_load_dword %0, %1, %2, 0 offen sc1" : "=v"(q0) : "v"(qo), "s"(rs) : "memory");
  asm volatile("buffer_load_dword %0, %1, %2, 0 offen offset:2048 sc1" : "=v"(q1) : "v"(qo), "s"(rs) : "memory");
  WAITN(10 + CNT(1)); KFRAG(3);
  STAMP(4); STAMPW(56);
  LDOE(1); G(); LDV(6); G(); LDV(7); LDOE(2); G(); LDV(8); G(); LDV(9);
  WAITN(4 + CNT(3) - CNT(1));
  asm volatile("" : "+v"(q0), "+v"(q1)::"memory");
  STAMP(5); STAMPW(64);
  LDOU(3); LDV(10); LDV(11); LDOU(4); LDV(12); LDV(13);
  int budget = 100000;
  if (!__all((((q0 ^ tag) | (q1 ^ tag)) & TMASK) == 0)) {
    _Pragma("nounroll") do {
      asm volatile("buffer_load_dword %0, %2, %3, 0 offen sc1\n\tbuffer_load_dword %1, %2, %3, 0 offen offset:2048 sc1\n\ts_waitcnt vmcnt(0)" : "=&v"(q0), "=&v"(q1) : "v"(qo), "s"(rs) : "memory");
    } while (!__all((((q0 ^ tag) | (q1 ^ tag)) & TMASK) == 0) && --budget > 0);
  }
  STAMPV(19, budget);
  const int vbase = L_KV + (g >> 1) * 16384 + (8 * (g & 1) + (c >> 2)) * 256 + ((w ^ ((c >> 2) | ((g & 1) << 2))) * 32) + (c & 3) * 8;
  const int a_qwr = L_QL + (tid >> 7) * QSTR + (tid & 127) * 2, a_qrd = L_QL + (c & 3) * QSTR + g * 16;
  const int a_mwr = L_MB + ((lane & 3) * 8 + w) * 4, a_mrd = L_MB + (c & 3) * 32;
  const int a_pwr = L_PL + (c >> 2) * PSTR + ((2 * (w & 1) + (g >> 1)) * 16 + (w >> 1) * 4 + (c & 3)) * 16 + (g & 1) * 8;
  const int a_lwr = L_LB + (w * 16 + g * 4 + (c & 3)) * 4;
  f4 acc[4];
  float M = attn_scores(asf(q0 ^ tag) + asf(q1 ^ tag), kf, L, a_qwr, a_qrd, a_mwr, a_mrd, a_pwr, a_lwr, lane);
  STAMP(6);
  LDOE(5); G(); LDV(14); G(); LDV(15); LDOE(6); LDOE(7);
  WAITN(4 + CNT(8) - CNT(4));
  BAR();
  const int pub_o = ((b * 8 + kvh) * 8 + s) * 2048 + w * 256 + (c & 3) * 64 + g * 16, pub_s = ((b * 8 + kvh) * 8 + s) * 128 + (lane & 7) * 16;
  attn_publish_stats(M, L, w, lane, rs, WS_OS + pub_s, tag, T0);
#pragma unroll
  for (int u = 0; u < 4; ++u) acc[u] = f4{0.f, 0.f, 0.f, 0.f};
  pv_chunk(0, acc, L, vbase, lane);
  pv_chunk(1, acc, L, vbase, lane);
  pv_chunk(2, acc, L, vbase, lane);
  v4u pf3 = *(const LAS v4u*)(L + L_PL + 3 * PSTR + lane * 16);
  PIN1(pf3);
  SB();
  WAITN(CNT(8) - CNT(6));
  STAMP(7); STAMPW(72);
  BAR();
  {
    v4u vf3[4];
#pragma unroll
    for (int u = 0; u < 4; ++u) vf3[u] = v_frag(3, u, L, vbase);
    SB();
#pragma unroll
    for (int k = WO_EARLY; k < 8; ++k) { switch (k) { case 0: LDO(0); break; case 1: LDO(1); break; case 2: LDO(2); break; case 3: LDO(3); break; case 4: LDO(4); break; case 5: LDO(5); break; case 6: LDO(6); break; default: LDO(7); } }
    SB();
#pragma unroll
    for (int u = 0; u < 4; ++u) acc[u] = mfma(vf3[u], pf3, acc[u]);
  }
  int po1 = WS_OB + pub_o;
  attn_publish(acc, lane, rs, po1, T0);
  STAMP(8);
  v4u wof[8];
  const float b2 = asf(b2raw << 16);
  int g_oo = (b * 8 + kvh) * 8 * 2048 + (lane >> 3) * 2048 + (4 * (w & 1) + ((lane >> 2) & 1)) * 256 + (w >> 1) * 64 + (lane & 3) * 16;
  int g_so = (b * 8 + kvh) * 8 * 128 + (lane & 7) * 128 + (w >> 1) * 16;
  int o_xw = L_PL + w * 1024 + (lane ^ (lane >> 4)) * 16, o_xr = L_PL + w * 1024 + (((c & 7) * 8 + 4 * (c >> 3) + g) ^ ((c & 7) >> 1)) * 16;
  int o_wr = L_RED + (2 * w + (g >> 1)) * 320 + (8 * (c & 7) + 4 * (g & 1)) * 4, o_rd = L_RED + (tid & 3) * 320 + (tid >> 2) * 16;
  int o_pub = WS_PB + (b * 8 + kvh) * 2048 + so_ * 256 + (tid >> 2) * 16;
  g_oo += WS_OB; g_so += WS_OS;
  int g_pb = WS_OB + (b * 8 + kvh) * 8 * 2048 + 7 * 256 + 192 + w * 4;
  PIN4(g_oo, g_so, o_wr, o_rd); PIN1(o_pub); PIN2(o_xw, o_xr);
  BAR();

  if (DLY_B) __builtin_amdgcn_s_sleep(DLY_B);
  o_gemv<true>(wof, wor, L, tid, lane, rs, g_oo, g_so, g_pb, o_xw, o_xr, o_wr, o_rd, o_pub, tag, T0, budget, w, 20);
  STAMP(10);
  {
    const f4 r = mixer(xres, L, tid, w, lane, j, rs, WS_PB + b * 8 * 2048, tag, b2, budget, 22);
    if (tid < 2) { v4u o = {tagf(r[0], tag), tagf(r[1], tag), tagf(r[2], tag), tagf(r[3], tag)}; st128s(o, rs, WS_XB + b * 2048 + j * 32 + tid * 16); }
  }
  STAMP(11);
  {
    int xo_ = WS_XB + b * 2048 + tid * 4, xw = L_XL + tid * 4, xq = xqa, qw = qwr, qr = qrd, qp = qpub + 4 * 8 * 4096;
    PIN4(xo_, xw, xq, qw); PIN2(qr, qp);
    if (DLY_E) __builtin_amdgcn_s_sleep(DLY_E);
    u32 xv;
    for (;;) { CBAR(); xv = ld32s(rs, xo_) ^ tag; if (__all((xv & TMASK) == 0) || --budget <= 0) break; }
    STAMP(26); STAMPV(27, budget); STAMPW(120);
    xres = asf(xv);
    *(LAS float*)(L + xw) = xres;
    float mean, var;
    ln_stats(xres, L, w, lane, mean, var);
    const v4u qa2 = q_operand(L, xq, mean, var);
    STAMP(12);
    q_gemv(wqf, qa2, L, tid, lane, qw, qr, rs, qp, tag);
  }
  STAMP(13);
  {
    v4u vf[16];
#pragma unroll
    for (int t = 0; t < 4; ++t)
#pragma unroll
      for (int u = 0; u < 4; ++u) vf[t * 4 + u] = v_frag(t, u, L, vbase);
    PIN8(vf); { v4u* v2 = vf + 8; PIN8(v2); }
    int qo2 = qo + 4 * 8 * 4096, b0 = a_qwr, b1 = a_qrd, b2_ = a_mwr, b3 = a_mrd, b4 = a_pwr, b5 = a_lwr, pr = L_PL + lane * 16, po2 = WS_OB + (1 << 19) + pub_o, ps2 = WS_OS + 32768 + pub_s;
    PIN4(qo2, b0, b1, b2_); PIN4(b3, b4, b5, pr); PIN2(po2, ps2);
    if (DLY_A) __builtin_amdgcn_s_sleep(DLY_A);
    for (;;) { CBAR(); q0 = ld32s(rs, qo2) ^ tag; q1 = ld32s(rs, qo2 + 2048) ^ tag; if (__all(((q0 | q1) & TMASK) == 0) || --budget <= 0) break; }
    STAMP(14); STAMPV(18, budget);
    M = attn_scores(asf(q0) + asf(q1), kf, L, b0, b1, b2_, b3, b4, b5, lane);
    BAR();
    v4u pf[4];
#pragma unroll
    for (int t = 0; t < 4; ++t) pf[t] = *(const LAS v4u*)(L + pr + t * PSTR);
    SB();
#pragma unroll
    for (int t = 0; t < 4; ++t)
#pragma unroll
      for (int u = 0; u < 4; ++u) acc[u] = mfma(vf[t * 4 + u], pf[t], t == 0 ? f4{0.f, 0.f, 0.f, 0.f} : acc[u]);
    attn_publish_stats(M, L, w, lane, rs, ps2, tag, T0);
    attn_publish(acc, lane, rs, po2, T0);
  }
  STAMP(15);
  g_oo += 1 << 19; g_so += 32768; o_pub += 65536; g_pb += 1 << 19;
  PIN2(g_oo, g_so); PIN1(o_pub);
  BAR();
  if (DLY_B) __builtin_amdgcn_s_sleep(DLY_B);
  o_gemv<false>(wof, wor, L, tid, lane, rs, g_oo, g_so, g_pb, o_xw, o_xr, o_wr, o_rd, o_pub, tag, T0, budget, w, 28);
  STAMP(16);
  {
    const f4 r = mixer(xres, L, tid, w, lane, j, rs, WS_PB + 65536 + b * 8 * 2048, tag, b2, budget, 30);
    if (w == 0) {
      const u32 lo = cvtpk(r[0], r[1]), hi = cvtpk(r[2], r[3]);
      if (lane < 2) { v2u o = {lo, hi}; *(GAS v2u*)((GAS unsigned char*)p.out + (b * 512 + 8 * j + 4 * lane) * 2) = o; }
      if (lane == 0 && j == 0) __builtin_amdgcn_raw_buffer_store_b32(T0, rs, WS_EP + b * 256, 0, 16);
    }
  }
  STAMP(17); STAMPR(37);
}

extern "C" void kernel_launch(void* const* d_in, const int* in_sizes, int n_in, void* d_out, int out_size, void* d_ws, size_t ws_size, hipStream_t stream) {
  Params p;
  p.x = (const u16*)d_in[0]; p.K = (const unsigned char*)d_in[1]; p.V = (const unsigned char*)d_in[2];
  p.wq = (const unsigned char*)d_in[3]; p.wo = (const unsigned char*)d_in[4];
  p.w1 = (const u16*)d_in[6]; p.b1 = (const u16*)d_in[7]; p.w2 = (const u16*)d_in[8]; p.b2 = (const u16*)d_in[9];
  p.out = (u16*)d_out; p.ws = (unsigned char*)d_ws;
  hipLaunchKernelGGL(fused_decode_kernel, dim3(256), dim3(512), 0, stream, p);
}
```

```cpp
#include <hip/hip_runtime.h>

typedef unsigned int u32;
typedef unsigned short u16;
typedef unsigned long long u64;
typedef u32 v4u __attribute__((ext_vector_type(4)));
typedef u32 v2u __attribute__((ext_vector_type(2)));
typedef float f4 __attribute__((ext_vector_type(4)));
typedef float f2 __attribute__((ext_vector_type(2)));
typedef short s8v __attribute__((ext_vector_type(8)));
typedef short s4v __attribute__((ext_vector_type(4)));
typedef __bf16 bf2v __attribute__((ext_vector_type(2)));
#define LAS __attribute__((address_space(3)))
#define GAS __attribute__((address_space(1)))
#define DI __device__ __forceinline__

#define STAMPS 0
#define MAPMODE 0
#define DLY_B 4
#define DLY_CD 12
#define DLY_E 11
#define DLY_A 5
#define STW 0
#define KVPOL " nt"
#define WPOL ""
#define PRETOUCH 2
#define SROT 0
#define WO_EARLY 8
#define OROT 4
#define PROBE 0
#define TLBTOUCH 0
#define WD 5
#define CODE_KB 0

constexpr int SK = 16384, WIN = 4096;
constexpr float EPS = 1e-5f;
constexpr float SCL = 0.08838834764831845f * 1.4426950408889634f;
constexpr float GK0 = -2.0f * 0.7978845608028654f * 1.4426950408889634f;
constexpr float GK1 = GK0 * 0.044715f;

constexpr int WS_EP = 0;
constexpr int WS_QB = 4096;
constexpr int WS_OB = WS_QB + 2 * 4 * 8 * 2 * 2048;
constexpr int WS_OS = WS_OB + 2 * 4 * 8 * 8 * 2048;
constexpr int WS_PB = WS_OS + 2 * 4 * 8 * 8 * 128;
constexpr int WS_XB = WS_PB + 2 * 4 * 8 * 2048;
constexpr int WS_END = WS_XB + 4 * 2048;
constexpr int WS_STAMP = 4 << 20;

constexpr int L_KV = 0;
constexpr int L_PAR = 131072;
constexpr int PSTR = 1088;
constexpr int L_PL = L_PAR + 11264;
constexpr int L_HB = L_PL + 4 * PSTR;
constexpr int L_ZERO = L_HB + 1024;
constexpr int QSTR = 288;
constexpr int L_QL = L_ZERO + 64;
constexpr int L_XL = L_QL + 4 * QSTR;
constexpr int L_RED = L_XL + 2048;
constexpr int L_ST = L_RED + 5120;
constexpr int L_MB = L_ST + 64;
constexpr int L_LB = L_MB + 128;
constexpr int L_R2 = L_LB + 512;
constexpr int L_STAMP = L_R2 + 128;
constexpr int L_END = L_STAMP + 1024 + 64;
static_assert(L_END <= 163840, "LDS");

struct Params {
  const u16* x; const unsigned char* K; const unsigned char* V; const unsigned char* wq; const unsigned char* wo;
  const u16* w1; const u16* b1; const u16* w2; const u16* b2; u16* out; unsigned char* ws;
};

template <int CTRL> DI float dpp(float x) {
  return __builtin_bit_cast(float, __builtin_amdgcn_update_dpp(0, __builtin_bit_cast(int, x), CTRL, 0xf, 0xf, true));
}
template <int CTRL, int RM> DI float dppm(float x) {
  return __builtin_bit_cast(float, __builtin_amdgcn_update_dpp(0, __builtin_bit_cast(int, x), CTRL, RM, 0xf, false));
}
constexpr int XOR1 = 0xB1, XOR2 = 0x4E, HMIR = 0x141, MIR = 0x140, ROR4 = 0x124, ROR8 = 0x128, BC15 = 0x142, BC31 = 0x143;
constexpr int SHL4 = 0x104, SHL8 = 0x108, SHL12 = 0x10C;
DI float asf(u32 x) { return __builtin_bit_cast(float, x); }
DI u32 asu(float x) { return __builtin_bit_cast(u32, x); }
DI u32 cvtpk(float lo, float hi) { f2 f = {lo, hi}; return __builtin_bit_cast(u32, __builtin_convertvector(f, bf2v)); }
DI f4 mfma(v4u a, v4u b, f4 c) {
  return __builtin_amdgcn_mfma_f32_16x16x32_bf16(__builtin_bit_cast(s8v, a), __builtin_bit_cast(s8v, b), c, 0, 0, 0);
}
DI float fmx(float a, float b) { return __builtin_elementwise_maximum(a, b); }
DI float xrow_max(float x) {
  auto s = __builtin_amdgcn_permlane16_swap(asu(x), asu(x), false, false);
  x = fmx(asf(s[0]), asf(s[1]));
  auto t = __builtin_amdgcn_permlane32_swap(asu(x), asu(x), false, false);
  return fmx(asf(t[0]), asf(t[1]));
}
DI u32 xo(u32 a, u32 t, u32 acc) { return __builtin_amdgcn_bitop3_b32(a, t, acc, 0xBE); }
#define PIN1(a) asm volatile("" : "+v"(a))
#define PIN2(a, b) asm volatile("" : "+v"(a), "+v"(b))
#define PIN4(a, b, c, d) asm volatile("" : "+v"(a), "+v"(b), "+v"(c), "+v"(d))
#define PIN8(x) asm volatile("" : "+v"(x[0]), "+v"(x[1]), "+v"(x[2]), "+v"(x[3]), "+v"(x[4]), "+v"(x[5]), "+v"(x[6]), "+v"(x[7]))
DI float xrow_sum(float x) {
  auto s = __builtin_amdgcn_permlane16_swap(asu(x), asu(x), false, false);
  x = asf(s[0]) + asf(s[1]);
  auto t = __builtin_amdgcn_permlane32_swap(asu(x), asu(x), false, false);
  return asf(t[0]) + asf(t[1]);
}
#define BAR() asm volatile("s_waitcnt lgkmcnt(0)\n\ts_barrier" ::: "memory")
#define CBAR() asm volatile("" ::: "memory")
#define SB() __builtin_amdgcn_sched_barrier(0)
#define WAITV(n) asm volatile("s_waitcnt vmcnt(" #n ")" ::: "memory")

#define STAMP(i) asm volatile("; MARK " #i)
#define STAMPR(i)
#define STAMPV(i, v)
#define STAMPW(i)

constexpr u32 TMASK = 0x3ffu;
DI u32 tagf(float v, u32 tag) { return __builtin_amdgcn_bitop3_b32(asu(v) + 0x200u, TMASK, tag, 0xBA); }
DI float untag(u32 v) { return asf(v & ~TMASK); }

DI u32 ld32s(__amdgpu_buffer_rsrc_t rs, int off) { return __builtin_amdgcn_raw_buffer_load_b32(rs, off, 0, 16); }
DI v4u ld128s(__amdgpu_buffer_rsrc_t rs, int off) { return __builtin_amdgcn_raw_buffer_load_b128(rs, off, 0, 16); }
DI void st128s(v4u v, __amdgpu_buffer_rsrc_t rs, int off) { __builtin_amdgcn_raw_buffer_store_b128(v, rs, off, 0, 16); }

DI void ln_stats(float x, LAS unsigned char* L, int w, int lane, float& mean, float& var) {
  const float c = asf(__builtin_amdgcn_readfirstlane(asu(x)));
  const float d = x - c;
  float s1 = d, s2 = d * d;
  s1 += dpp<XOR1>(s1); s2 += dpp<XOR1>(s2);
  s1 += dpp<XOR2>(s1); s2 += dpp<XOR2>(s2);
  s1 += dpp<HMIR>(s1); s2 += dpp<HMIR>(s2);
  s1 += dpp<MIR>(s1); s2 += dpp<MIR>(s2);
  s1 += dppm<BC15, 0xa>(s1); s2 += dppm<BC15, 0xa>(s2);
  s1 += dppm<BC31, 0xc>(s1); s2 += dppm<BC31, 0xc>(s2);
  const f2 pv = {fmaf(s1, 1.0f / 64, c), fmaf(s1 * s1, -1.0f / 64, s2)};
  if (lane == 63) *(LAS f2*)(L + L_ST + w * 8) = pv;
  BAR();
  const f2 v = *(const LAS f2*)(L + L_ST + (lane & 7) * 8);
  float m = v.x;
  m += dpp<XOR1>(m); m += dpp<XOR2>(m); m += dpp<HMIR>(m);
  mean = m * 0.125f;
  const float dv = v.x - mean;
  float t = fmaf(64.0f * dv, dv, v.y);
  t += dpp<XOR1>(t); t += dpp<XOR2>(t); t += dpp<HMIR>(t);
  var = t * (1.0f / 512);
}

DI void transpose8(const v4u (&raw)[8], v4u (&fr)[8]) {
#pragma unroll
  for (int i = 0; i < 8; ++i) {
    const u32 sel = (i & 1) ? 0x07060302u : 0x05040100u;
#pragma unroll
    for (int m = 0; m < 4; ++m) fr[i][m] = __builtin_amdgcn_perm(raw[2 * m + 1][i >> 1], raw[2 * m][i >> 1], sel);
  }
}

DI v4u q_operand(LAS unsigned char* L, int xaddr, float mean, float var) {
  const f4 a = *(const LAS f4*)(L + xaddr), b = *(const LAS f4*)(L + xaddr + 16);
  const float rs_ = __builtin_amdgcn_rsqf(var + EPS), mr = -mean * rs_;
  return v4u{cvtpk(fmaf(a[0], rs_, mr), fmaf(a[1], rs_, mr)), cvtpk(fmaf(a[2], rs_, mr), fmaf(a[3], rs_, mr)),
             cvtpk(fmaf(b[0], rs_, mr), fmaf(b[1], rs_, mr)), cvtpk(fmaf(b[2], rs_, mr), fmaf(b[3], rs_, mr))};
}

DI void q_gemv(const v4u (&wf)[8], v4u a, LAS unsigned char* L, int tid, int lane, int wraddr, int rdaddr, __amdgpu_buffer_rsrc_t rs, int qoff, u32 tag) {
  f4 d[8];
#pragma unroll
  for (int i = 0; i < 8; ++i) d[i] = mfma(a, wf[i], f4{0.f, 0.f, 0.f, 0.f});
  f4 v;
#pragma unroll
  for (int m = 0; m < 4; ++m) v[m] = (lane & 16) ? d[4 + m][0] : d[m][0];
  if (lane < 32) *(LAS f4*)(L + wraddr) = v;
  BAR();
  if (tid < 128) {
    f4 r = *(const LAS f4*)(L + rdaddr) + *(const LAS f4*)(L + rdaddr + 4 * 576);
#pragma unroll
    for (int e = 0; e < 4; ++e) { float t = r[e]; t += dpp<XOR1>(t); t += dpp<XOR2>(t); r[e] = t; }
    if ((tid & 3) == 0) { v4u o = {tagf(r[0], tag), tagf(r[1], tag), tagf(r[2], tag), tagf(r[3], tag)}; st128s(o, rs, qoff); }
  }
}

DI float attn_scores(float qv, const v4u (&kf)[16], LAS unsigned char* L, int qwr, int qrd, int mwr, int mrd, int pwr, int lwr, int lane) {
  *(LAS u16*)(L + qwr) = (u16)(cvtpk(qv, qv) & 0xffffu);
  BAR();
  v4u qf[4];
#pragma unroll
  for (int st = 0; st < 4; ++st) qf[st] = *(const LAS v4u*)(L + qrd + st * 64);
  SB();
  f4 d[4];
#pragma unroll
  for (int st = 0; st < 4; ++st)
#pragma unroll
    for (int t = 0; t < 4; ++t) d[t] = mfma(kf[t * 4 + st], qf[st], st == 0 ? f4{0.f, 0.f, 0.f, 0.f} : d[t]);
  float sc[4];
#pragma unroll
  for (int i = 0; i < 4; ++i) {
    const float a = (lane & 4) ? d[1][i] : d[0][i], bq = (lane & 4) ? d[3][i] : d[2][i];
    sc[i] = (lane & 8) ? bq : a;
  }
  float m = fmx(fmx(sc[0], sc[1]), fmx(sc[2], sc[3]));
  m = fmx(m, dpp<ROR4>(m)); m = fmx(m, dpp<ROR8>(m));
  m = xrow_max(m);
  if (lane < 4) *(LAS float*)(L + mwr) = m;
  BAR();
  const f4 m0 = *(const LAS f4*)(L + mrd), m1 = *(const LAS f4*)(L + mrd + 16);
  float M = fmx(fmx(fmx(m0[0], m0[1]), fmx(m0[2], m0[3])), fmx(fmx(m1[0], m1[1]), fmx(m1[2], m1[3])));
  M = asf(asu(M * SCL) & ~TMASK);
  float p[4];
#pragma unroll
  for (int i = 0; i < 4; ++i) p[i] = __builtin_amdgcn_exp2f(fmaf(sc[i], SCL, -M));
  v2u pk = {cvtpk(p[0], p[1]), cvtpk(p[2], p[3])};
  *(LAS v2u*)(L + pwr) = pk;
  float l = (p[0] + p[1]) + (p[2] + p[3]);
  l += dpp<ROR4>(l); l += dpp<ROR8>(l);
  if ((lane & 12) == 0) *(LAS float*)(L + lwr) = l;
  return M;
}

DI v4u v_frag(int t, int u, LAS unsigned char* L, int vbase) {
  const s4v lo = __builtin_amdgcn_ds_read_tr16_b64_v4i16((LAS s4v*)(L + vbase + u * 32768 + t * 4096));
  const s4v hi = __builtin_amdgcn_ds_read_tr16_b64_v4i16((LAS s4v*)(L + vbase + u * 32768 + t * 4096 + 1024));
  const v2u l2 = __builtin_bit_cast(v2u, lo), h2 = __builtin_bit_cast(v2u, hi);
  return v4u{l2[0], l2[1], h2[0], h2[1]};
}
DI void pv_chunk(int t, f4 (&acc)[4], LAS unsigned char* L, int vbase, int lane) {
  const v4u pf = *(const LAS v4u*)(L + L_PL + t * PSTR + lane * 16);
#pragma unroll
  for (int u = 0; u < 4; ++u) acc[u] = mfma(v_frag(t, u, L, vbase), pf, acc[u]);
}

DI void attn_publish_stats(float M, LAS unsigned char* L, int w, int lane, __amdgpu_buffer_rsrc_t rs, int saddr, u32 tag, u32 T0) {
  if (w == 7) {
    float l = *(const LAS float*)(L + L_LB + lane * 4) + *(const LAS float*)(L + L_LB + (lane + 64) * 4);
    l += dpp<ROR4>(l); l += dpp<ROR8>(l);
    l = xrow_sum(l);
    if (lane < 8) { v4u sv = {asu(M) | tag, tagf(l, tag), T0, T0}; st128s(sv, rs, saddr); }
  }
}
DI void attn_publish(const f4 (&acc)[4], int lane, __amdgpu_buffer_rsrc_t rs, int oaddr, u32 T0) {
  float v[4];
#pragma unroll
  for (int e = 0; e < 4; ++e) {
    float t = acc[0][e];
    t += dpp<SHL4>(acc[1][e]); t += dpp<SHL8>(acc[2][e]); t += dpp<SHL12>(acc[3][e]);
    v[e] = t;
  }
  const u32 p01 = cvtpk(v[0], v[1]), p23 = cvtpk(v[2], v[3]);
  const v4u o = {__builtin_amdgcn_perm(p01, T0, 0x05040100u), __builtin_amdgcn_perm(p01, T0, 0x07060100u),
                 __builtin_amdgcn_perm(p23, T0, 0x05040100u), __builtin_amdgcn_perm(p23, T0, 0x07060100u)};
  if ((lane & 12) == 0) st128s(o, rs, oaddr);
}

DI bool o_check(const v4u& x0, const v4u& x1, const v4u& sv, u32 T0) {
  u32 d = xo(x0[0], T0, 0u);
  d = xo(x0[1], T0, d); d = xo(x0[2], T0, d); d = xo(x0[3], T0, d);
  d = xo(x1[0], T0, d); d = xo(x1[1], T0, d); d = xo(x1[2], T0, d); d = xo(x1[3], T0, d);
  d = xo(sv[0], T0, d); d = xo(sv[1], T0, d);
  d = xo(sv[3], T0, xo(sv[2], T0, d & TMASK));
  return __all(d == 0);
}
template <bool FIRST>
DI void o_gemv(v4u (&wf)[8], v4u (&wraw)[8], LAS unsigned char* L, int tid, int lane, __amdgpu_buffer_rsrc_t rs, int oo, int so, int pb, int xw, int xr, int wraddr, int rdaddr, int poff, u32 tag, u32 T0, int& budget, int w, int sb) {
  v4u x0, x1, sv = {T0, T0, T0, T0};
  CBAR();
  x0 = ld128s(rs, oo); x1 = ld128s(rs, oo + 512);
  if (lane < 8) sv = ld128s(rs, so);
  if (FIRST) {
    asm volatile("s_waitcnt vmcnt(3)" : "+v"(wraw[0]), "+v"(wraw[1]), "+v"(wraw[2]), "+v"(wraw[3]), "+v"(wraw[4]), "+v"(wraw[5]), "+v"(wraw[6]), "+v"(wraw[7])::"memory");
    STAMP(9); STAMPW(80);
    transpose8(wraw, wf);
    PIN8(wf);
  }
  while (!o_check(x0, x1, sv, T0) && --budget > 0) {
    CBAR();
    x0 = ld128s(rs, oo); x1 = ld128s(rs, oo + 512);
    if (lane < 8) sv = ld128s(rs, so);
  }
  STAMP(sb); STAMPV(sb + 1, budget); if (sb == 28) STAMPW(104);
  const v4u pk = {__builtin_amdgcn_perm(x0[1], x0[0], 0x07060302u), __builtin_amdgcn_perm(x0[3], x0[2], 0x07060302u),
                  __builtin_amdgcn_perm(x1[1], x1[0], 0x07060302u), __builtin_amdgcn_perm(x1[3], x1[2], 0x07060302u)};
  *(LAS v4u*)(L + xw) = pk;
  CBAR();
  const v4u a = *(const LAS v4u*)(L + xr);
  const float ms = untag(sv[0]), ls = untag(sv[1]);
  float M = ms;
  M = fmx(M, dpp<XOR1>(M)); M = fmx(M, dpp<XOR2>(M)); M = fmx(M, dpp<HMIR>(M));
  const float f = __builtin_amdgcn_exp2f(ms - M);
  float Ls = f * ls;
  Ls += dpp<XOR1>(Ls); Ls += dpp<XOR2>(Ls); Ls += dpp<HMIR>(Ls);
  const float cs = f * __builtin_amdgcn_rcpf(Ls);
  f4 d[8];
#pragma unroll
  for (int i = 0; i < 8; ++i) d[i] = mfma(a, wf[i], f4{0.f, 0.f, 0.f, 0.f});
  float cc[8];
#pragma unroll
  for (int i = 0; i < 8; ++i) cc[i] = asf(__builtin_amdgcn_readlane(asu(cs), i));
  const bool odd = lane & 16;
  const f2 c01 = {odd ? cc[4] : cc[0], odd ? cc[5] : cc[1]}, c23 = {odd ? cc[6] : cc[2], odd ? cc[7] : cc[3]};
  float e[8];
#pragma unroll
  for (int i = 0; i < 8; ++i) { const f2 t = f2{d[i][2], d[i][3]} * c23 + f2{d[i][0], d[i][1]} * c01; e[i] = t[0] + t[1]; }
  f4 v;
#pragma unroll
  for (int m = 0; m < 4; ++m) { auto t = __builtin_amdgcn_permlane16_swap(asu(e[m]), asu(e[m + 4]), false, false); v[m] = asf(t[0]) + asf(t[1]); }
  if ((lane >> 5) == ((lane >> 3) & 1)) *(LAS f4*)(L + wraddr) = v;
  BAR();
  if (tid < 64) {
    f4 r = (*(const LAS f4*)(L + rdaddr) + *(const LAS f4*)(L + rdaddr + 4 * 320)) + (*(const LAS f4*)(L + rdaddr + 8 * 320) + *(const LAS f4*)(L + rdaddr + 12 * 320));
#pragma unroll
    for (int k = 0; k < 4; ++k) { float t = r[k]; t += dpp<XOR1>(t); t += dpp<XOR2>(t); r[k] = t; }
    if ((tid & 3) == 0) { v4u ov4 = {tagf(r[0], tag), tagf(r[1], tag), tagf(r[2], tag), tagf(r[3], tag)}; st128s(ov4, rs, poff); }
  }
}

DI f4 mixer(float& xres, LAS unsigned char* L, int tid, int w, int lane, int j, __amdgpu_buffer_rsrc_t rs, int pbase, u32 tag, float b2, int& budget, int sb) {
  float pw1[7], pb1[7], pw2[7];
  const int fl = tid & 255, q = tid >> 8;
#pragma unroll
  for (int i = 0; i < 7; ++i) {
    pw1[i] = asf((u32) * (const LAS u16*)(L + L_PAR + (fl + 256 * i) * 2) << 16);
    pb1[i] = asf((u32) * (const LAS u16*)(L + L_PAR + 3584 + (fl + 256 * i) * 2) << 16);
    pw2[i] = asf((u32) * (const LAS u16*)(L + L_PAR + 7168 + (fl + 256 * i) * 2) << 16);
  }
#pragma unroll
  for (int i = 0; i < 7; ++i) asm volatile("" : "+v"(pw1[i]), "+v"(pb1[i]), "+v"(pw2[i]));
  int po = pbase + tid * 4, xa = L_XL + (8 * j + 4 * q) * 4, xw = L_XL + tid * 4;
  asm volatile("" : "+v"(po), "+v"(xa), "+v"(xw));
  if (DLY_CD) __builtin_amdgcn_s_sleep(DLY_CD);
  u32 p[8];
  for (;;) {
    CBAR();
#pragma unroll
    for (int i = 0; i < 8; ++i) p[i] = ld32s(rs, po + i * 2048) ^ tag;
    const u32 d = (p[0] | p[1] | p[2]) | (p[3] | p[4] | p[5]) | (p[6] | p[7]);
    if (__all((d & TMASK) == 0) || --budget <= 0) break;
  }
  STAMP(sb); STAMPV(sb + 1, budget); if (sb == 30) STAMPW(112);
  const float x1 = xres + (((asf(p[0]) + asf(p[1])) + (asf(p[2]) + asf(p[3]))) + ((asf(p[4]) + asf(p[5])) + (asf(p[6]) + asf(p[7]))));
  xres = x1;
  *(LAS float*)(L + xw) = x1;
  float mean, var;
  ln_stats(x1, L, w, lane, mean, var);
  const f4 xv = *(const LAS f4*)(L + xa);
  const float r1 = __builtin_amdgcn_rsqf(var + EPS);
  const float r2 = __builtin_amdgcn_rsqf(var * r1 * r1 + EPS);
  const float rr = r1 * r2, mr = -mean * rr;
  STAMP(sb + 2); if (sb == 22) STAMPW(88);
  const f2 hA = {fmaf(xv[0], rr, mr), fmaf(xv[1], rr, mr)}, hB = {fmaf(xv[2], rr, mr), fmaf(xv[3], rr, mr)};
  f2 aA = {0.f, 0.f}, aB = {0.f, 0.f};
#pragma unroll
  for (int i = 0; i < 7; ++i) {
    const f2 w1 = {pw1[i], pw1[i]}, b1 = {pb1[i], pb1[i]}, w2 = {pw2[i], pw2[i]};
    const f2 k0 = {GK0, GK0}, k1 = {GK1, GK1}, one = {1.f, 1.f};
    {
      const f2 u = hA * w1 + b1, z = u * (u * u * k1 + k0);
      const f2 e = {__builtin_amdgcn_exp2f(z[0]), __builtin_amdgcn_exp2f(z[1])}, dd = e + one;
      const f2 rc = {__builtin_amdgcn_rcpf(dd[0]), __builtin_amdgcn_rcpf(dd[1])};
      aA += (u * rc) * w2;
    }
    {
      const f2 u = hB * w1 + b1, z = u * (u * u * k1 + k0);
      const f2 e = {__builtin_amdgcn_exp2f(z[0]), __builtin_amdgcn_exp2f(z[1])}, dd = e + one;
      const f2 rc = {__builtin_amdgcn_rcpf(dd[0]), __builtin_amdgcn_rcpf(dd[1])};
      aB += (u * rc) * w2;
    }
  }
  float t0 = (lane & 1) ? aA[1] : aA[0], t1 = (lane & 1) ? aA[0] : aA[1];
  t0 += dpp<XOR1>(t1);
  float t2 = (lane & 1) ? aB[1] : aB[0], t3 = (lane & 1) ? aB[0] : aB[1];
  t2 += dpp<XOR1>(t3);
  float s0 = (lane & 2) ? t2 : t0, s1 = (lane & 2) ? t0 : t2;
  s0 += dpp<XOR2>(s1);
  s0 += dpp<ROR4>(s0); s0 += dpp<ROR8>(s0);
  s0 = xrow_sum(s0);
  STAMP(sb + 3); if (sb == 22) STAMPW(96);
  if (lane < 4) *(LAS float*)(L + L_R2 + (w * 4 + lane) * 4) = s0;
  BAR();
  f4 res = {0.f, 0.f, 0.f, 0.f};
  if (w == 0) {
    const int qq = lane & 1;
    const f4 a = (*(const LAS f4*)(L + L_R2 + (qq * 4 + 0) * 16) + *(const LAS f4*)(L + L_R2 + (qq * 4 + 1) * 16)) +
                 (*(const LAS f4*)(L + L_R2 + (qq * 4 + 2) * 16) + *(const LAS f4*)(L + L_R2 + (qq * 4 + 3) * 16));
    const f4 xr = *(const LAS f4*)(L + L_XL + (8 * j + 4 * qq) * 4);
    res = xr + a + f4{b2, b2, b2, b2};
  }
  return res;
}

__global__ void __launch_bounds__(512, 2) fused_decode_kernel(Params p) {
  __shared__ __attribute__((aligned(1024))) unsigned char smem[L_END];
  LAS unsigned char* L = (LAS unsigned char*)smem;
  const int tid = threadIdx.x, lane = tid & 63, w = __builtin_amdgcn_readfirstlane(tid >> 6);
  const int bid = blockIdx.x;
  int b, kvh, s;
  if (MAPMODE == 0) { kvh = bid & 7; b = bid >> 6; s = ((bid >> 3) + SROT) & 7; }
  else { const int x = bid & 7, i = (bid >> 3) + 32 * (x & 1); b = x >> 1; kvh = i >> 3; s = i & 7; }
  const int j = kvh * 8 + s;
  const int g = lane >> 4, c = lane & 15;
  STAMPR(36); STAMP(0);

  const size_t slab = ((size_t)(b * 8 + kvh) * SK + (SK - WIN) + s * 512) * 256;
  const __amdgpu_buffer_rsrc_t rK = __builtin_amdgcn_make_buffer_rsrc((void*)(p.K + slab), 0, 512 * 256, 0x00020000);
  const __amdgpu_buffer_rsrc_t rV = __builtin_amdgcn_make_buffer_rsrc((void*)(p.V + slab), 0, 512 * 256, 0x00020000);
  const __amdgpu_buffer_rsrc_t rQ = __builtin_amdgcn_make_buffer_rsrc((void*)p.wq, 0, 512 * 4096 * 2, 0x00020000);
  const __amdgpu_buffer_rsrc_t rO = __builtin_amdgcn_make_buffer_rsrc((void*)p.wo, 0, 512 * 4096 * 2, 0x00020000);
  const __amdgpu_buffer_rsrc_t rs = __builtin_amdgcn_make_buffer_rsrc((void*)p.ws, 0, WS_END, 0x00020000);
  const __amdgpu_buffer_rsrc_t rX = __builtin_amdgcn_make_buffer_rsrc((void*)p.x, 0, 4096, 0x00020000);
  const u32 ldsb = (u32)(size_t)L;

  v4u dummy = {0u, 0u, 0u, 0u};
  u32 ev, xraw, b2raw, dummy2 = 0, dummy3 = 0;
  asm volatile("s_nop 4\n\tbuffer_load_dword %0, %1, %2, 0 offen sc1" : "=v"(ev) : "v"(WS_EP + b * 256), "s"(rs) : "memory");
  asm volatile("buffer_load_ushort %0, %1, %2, 0 offen" : "=v"(xraw) : "v"((b * 512 + tid) * 2), "s"(rX) : "memory");
  asm volatile("global_load_ushort %0, %1, %2" : "=v"(b2raw) : "v"(0), "s"(p.b2) : "memory");
  if (w == 7) {
    const int me = (b * 8 + kvh) * 8 + s;
    int a_ = lane < 16 ? WS_OB + me * 2048 + lane * 128
           : lane < 32 ? WS_OB + (1 << 19) + me * 2048 + (lane - 16) * 128
           : lane < 34 ? WS_OS + (lane - 32) * 32768 + me * 128
           : lane < 42 ? WS_QB + ((lane - 34) >> 2) * (4 * 8 * 4096) + ((b * 8 + kvh) * 2 + (s & 1)) * 2048 + (s >> 1) * 512 + ((lane - 34) & 3) * 128
           : lane < 46 ? WS_PB + ((lane - 42) >> 1) * 65536 + (b * 8 + kvh) * 2048 + ((s + OROT) & 7) * 256 + ((lane - 42) & 1) * 128
           : WS_XB + b * 2048 + j * 32;
    asm volatile("buffer_load_dword %0, %1, %2, 0 offen" : "+v"(dummy2) : "v"(a_), "s"(rs) : "memory");
  }
  const int rot = __builtin_amdgcn_readfirstlane((int)(((u32)(bid * 8 + w) * 2654435761u) >> 28));
  const int cg = s >> 1, kh = s & 1;
  const int voq = ((256 * kh + 32 * w + 8 * g) * 4096 + kvh * 512 + 128 * cg + 8 * c) * 2;
  const int so_ = (s + OROT) & 7;
  const int voo = ((kvh * 512 + 64 * w + 16 * (c >> 3) + 4 * g) * 512 + 64 * so_ + 8 * (c & 7)) * 2;
  int vok[4], vov[2];
#pragma unroll
  for (int i = 0; i < 4; ++i) vok[i] = (64 * w + g) * 256 + ((c ^ (4 * i + g)) * 16);
#pragma unroll
  for (int i = 0; i < 2; ++i) vov[i] = (64 * w + g) * 256 + ((((c >> 1) ^ (g | (i << 2))) * 32) + (c & 1) * 16);
  v4u wqr[8], wor[8], kf[16];
#define LDQ(jj) asm volatile("buffer_load_dwordx4 %0, %1, %2, %3 offen" WPOL : "=v"(wqr[jj]) : "v"(voq), "s"(rQ), "s"((jj) * 8192) : "memory")
#define LDO(jj) asm volatile("buffer_load_dwordx4 %0, %1, %2, %3 offen" WPOL : "=v"(wor[jj]) : "v"(voo), "s"(rO), "s"((32 * ((jj) >> 2) + ((jj) & 3)) * 1024) : "memory")
#define LDK(i) asm volatile("s_mov_b32 m0, %0\n\ts_nop 0\n\tbuffer_load_dwordx4 %1, %2, %3 offen" KVPOL " lds" ::"s"(ldsb + L_KV + w * 16384 + (i) * 1024), "v"(vok[(i) & 3]), "s"(rK), "s"((((i) + rot) & 15) * 1024) : "memory")
#define LDV(i) asm volatile("s_mov_b32 m0, %0\n\ts_nop 0\n\tbuffer_load_dwordx4 %1, %2, %3 offen" KVPOL " lds" ::"s"(ldsb + L_KV + w * 16384 + (i) * 1024), "v"(vov[((i) >> 1) & 1]), "s"(rV), "s"((((i) + rot) & 15) * 1024) : "memory")
#define G() asm volatile("s_waitcnt vmcnt(%0)" ::"n"(WD - 1) : "memory")
#define EV(n, i) asm volatile("s_waitcnt vmcnt(%0)" ::"n"((n) - (i) - 1) : "memory")
#define KFRAG(t)                                                                                                     \
  _Pragma("unroll") for (int st = 0; st < 4; ++st)                                                                   \
      kf[(t) * 4 + st] = *(const LAS v4u*)(L + L_KV + w * 16384 + (16 * (t) + c) * 256 + (((4 * st + g) ^ c) * 16)); \
  asm volatile("" : "+v"(kf[(t) * 4]), "+v"(kf[(t) * 4 + 1]), "+v"(kf[(t) * 4 + 2]), "+v"(kf[(t) * 4 + 3])::"memory")
  LDQ(0); LDK(0); LDQ(1); LDK(1); LDQ(2); LDK(2);
  G(); LDQ(3); G(); LDK(3);
  asm volatile("" : "+v"(ev), "+v"(xraw), "+v"(b2raw), "+v"(dummy), "+v"(dummy2), "+v"(dummy3)::"memory");
  const u32 T0 = __builtin_amdgcn_readfirstlane(ev) + 1u;
  const u32 tag = T0 & TMASK;
  float xres = asf(xraw << 16);
  const int xqa = L_XL + (256 * kh + 32 * w + 8 * g) * 4;
  v4u qa;
  {
    *(LAS float*)(L + L_XL + tid * 4) = xres;
    float mean, var;
    ln_stats(xres, L, w, lane, mean, var);
    qa = q_operand(L, xqa, mean, var);
    PIN1(qa);
  }
  G(); LDQ(4); G(); LDK(4); G(); LDQ(5); G(); LDK(5);
  G(); LDQ(6); G(); LDK(6); G(); LDQ(7); G(); LDK(7);
  EV(16, 7); STAMPW(48); KFRAG(0);
  G(); LDK(8); G(); LDK(9); G(); LDK(10); G(); LDK(11); G(); LDK(12);
  EV(21, 14);
  asm volatile("" : "+v"(wqr[0]), "+v"(wqr[1]), "+v"(wqr[2]), "+v"(wqr[3]), "+v"(wqr[4]), "+v"(wqr[5]), "+v"(wqr[6]), "+v"(wqr[7])::"memory");
  STAMP(1); STAMPW(40);
  LDK(13); LDK(14); LDK(15);
  {
#pragma unroll
    for (int k = 0; k < 2; ++k) {
      const int piece = k == 0 ? w : 8 + (w % 3);
      int gr = piece * 64 + lane; gr = gr < 672 ? gr : 671;
      const int arr = gr / 224, e = gr - arr * 224;
      const unsigned char* src = (const unsigned char*)(arr == 0 ? p.w1 : arr == 1 ? p.b1 : p.w2) + e * 16;
      asm volatile("s_mov_b32 m0, %0\n\ts_nop 0\n\tglobal_load_lds_dwordx4 %1, off" ::"s"(ldsb + L_PAR + piece * 1024), "v"(src) : "memory");
    }
  }
  LDV(0); LDV(1);
  v4u wqf[8];
  transpose8(wqr, wqf);
  const int qwr = L_RED + w * 576 + (8 * c + 4 * (g & 1)) * 4, qrd = L_RED + (tid & 3) * 576 + (tid >> 2) * 16;
  const int qpub = WS_QB + ((b * 8 + kvh) * 2 + kh) * 2048 + cg * 512 + (tid >> 2) * 16;
  q_gemv(wqf, qa, L, tid, lane, qwr, qrd, rs, qpub, tag);
  STAMP(2);
  EV(28, 15); KFRAG(1);
  G(); LDV(2); G(); LDV(3);
  EV(30, 19); KFRAG(2);
#define CNT(k) ((k) < WO_EARLY ? (k) : WO_EARLY)
#define LDOE(k) if ((k) < WO_EARLY) { G(); LDO(k); }
#define LDOU(k) if ((k) < WO_EARLY) { LDO(k); }
#define WAITN(n) asm volatile("s_waitcnt vmcnt(%0)" ::"n"(n) : "memory")
  LDOE(0); G(); LDV(4); G(); LDV(5);
  STAMP(3);
  u32 q0, q1;
  const int qo = WS_QB + (b * 8 + kvh) * 4096 + tid * 4;
  G(); asm volatile("buffer_load_dword %0, %1, %2, 0 offen sc1" : "=v"(q0) : "v"(qo), "s"(rs) : "memory");
  asm volatile("buffer_load_dword %0, %1, %2, 0 offen offset:2048 sc1" : "=v"(q1) : "v"(qo), "s"(rs) : "memory");
  WAITN(10 + CNT(1)); KFRAG(3);
  STAMP(4); STAMPW(56);
  LDOE(1); G(); LDV(6); G(); LDV(7); LDOE(2); G(); LDV(8); G(); LDV(9);
  WAITN(4 + CNT(3) - CNT(1));
  asm volatile("" : "+v"(q0), "+v"(q1)::"memory");
  STAMP(5); STAMPW(64);
  LDOU(3); LDV(10); LDV(11); LDOU(4); LDV(12); LDV(13);
  int budget = 100000;
  if (!__all((((q0 ^ tag) | (q1 ^ tag)) & TMASK) == 0)) {
    _Pragma("nounroll") do {
      asm volatile("buffer_load_dword %0, %2, %3, 0 offen sc1\n\tbuffer_load_dword %1, %2, %3, 0 offen offset:2048 sc1\n\ts_waitcnt vmcnt(0)" : "=&v"(q0), "=&v"(q1) : "v"(qo), "s"(rs) : "memory");
    } while (!__all((((q0 ^ tag) | (q1 ^ tag)) & TMASK) == 0) && --budget > 0);
  }
  STAMPV(19, budget);
  const int vbase = L_KV + (g >> 1) * 16384 + (8 * (g & 1) + (c >> 2)) * 256 + ((w ^ ((c >> 2) | ((g & 1) << 2))) * 32) + (c & 3) * 8;
  const int a_qwr = L_QL + (tid >> 7) * QSTR + (tid & 127) * 2, a_qrd = L_QL + (c & 3) * QSTR + g * 16;
  const int a_mwr = L_MB + ((lane & 3) * 8 + w) * 4, a_mrd = L_MB + (c & 3) * 32;
  const int a_pwr = L_PL + (c >> 2) * PSTR + ((2 * (w & 1) + (g >> 1)) * 16 + (w >> 1) * 4 + (c & 3)) * 16 + (g & 1) * 8;
  const int a_lwr = L_LB + (w * 16 + g * 4 + (c & 3)) * 4;
  f4 acc[4];
  float M = attn_scores(asf(q0 ^ tag) + asf(q1 ^ tag), kf, L, a_qwr, a_qrd, a_mwr, a_mrd, a_pwr, a_lwr, lane);
  STAMP(6);
  LDOE(5); G(); LDV(14); G(); LDV(15); LDOE(6); LDOE(7);
  WAITN(4 + CNT(8) - CNT(4));
  BAR();
  const int pub_o = ((b * 8 + kvh) * 8 + s) * 2048 + w * 256 + (c & 3) * 64 + g * 16, pub_s = ((b * 8 + kvh) * 8 + s) * 128 + (lane & 7) * 16;
  attn_publish_stats(M, L, w, lane, rs, WS_OS + pub_s, tag, T0);
#pragma unroll
  for (int u = 0; u < 4; ++u) acc[u] = f4{0.f, 0.f, 0.f, 0.f};
  pv_chunk(0, acc, L, vbase, lane);
  pv_chunk(1, acc, L, vbase, lane);
  pv_chunk(2, acc, L, vbase, lane);
  v4u pf3 = *(const LAS v4u*)(L + L_PL + 3 * PSTR + lane * 16);
  PIN1(pf3);
  SB();
  WAITN(CNT(8) - CNT(6));
  STAMP(7); STAMPW(72);
  BAR();
  {
    v4u vf3[4];
#pragma unroll
    for (int u = 0; u < 4; ++u) vf3[u] = v_frag(3, u, L, vbase);
    SB();
#pragma unroll
    for (int k = WO_EARLY; k < 8; ++k) { switch (k) { case 0: LDO(0); break; case 1: LDO(1); break; case 2: LDO(2); break; case 3: LDO(3); break; case 4: LDO(4); break; case 5: LDO(5); break; case 6: LDO(6); break; default: LDO(7); } }
    SB();
#pragma unroll
    for (int u = 0; u < 4; ++u) acc[u] = mfma(vf3[u], pf3, acc[u]);
  }
  int po1 = WS_OB + pub_o;
  attn_publish(acc, lane, rs, po1, T0);
  STAMP(8);
  v4u wof[8];
  const float b2 = asf(b2raw << 16);
  int g_oo = (b * 8 + kvh) * 8 * 2048 + (lane >> 3) * 2048 + (4 * (w & 1) + ((lane >> 2) & 1)) * 256 + (w >> 1) * 64 + (lane & 3) * 16;
  int g_so = (b * 8 + kvh) * 8 * 128 + (lane & 7) * 128 + (w >> 1) * 16;
  int o_xw = L_PL + w * 1024 + (lane ^ (lane >> 4)) * 16, o_xr = L_PL + w * 1024 + (((c & 7) * 8 + 4 * (c >> 3) + g) ^ ((c & 7) >> 1)) * 16;
  int o_wr = L_RED + (2 * w + (g >> 1)) * 320 + (8 * (c & 7) + 4 * (g & 1)) * 4, o_rd = L_RED + (tid & 3) * 320 + (tid >> 2) * 16;
  int o_pub = WS_PB + (b * 8 + kvh) * 2048 + so_ * 256 + (tid >> 2) * 16;
  g_oo += WS_OB; g_so += WS_OS;
  int g_pb = WS_OB + (b * 8 + kvh) * 8 * 2048 + 7 * 256 + 192 + w * 4;
  PIN4(g_oo, g_so, o_wr, o_rd); PIN1(o_pub); PIN2(o_xw, o_xr);
  BAR();

  if (DLY_B) __builtin_amdgcn_s_sleep(DLY_B);
  o_gemv<true>(wof, wor, L, tid, lane, rs, g_oo, g_so, g_pb, o_xw, o_xr, o_wr, o_rd, o_pub, tag, T0, budget, w, 20);
  STAMP(10);
  {
    const f4 r = mixer(xres, L, tid, w, lane, j, rs, WS_PB + b * 8 * 2048, tag, b2, budget, 22);
    if (tid < 2) { v4u o = {tagf(r[0], tag), tagf(r[1], tag), tagf(r[2], tag), tagf(r[3], tag)}; st128s(o, rs, WS_XB + b * 2048 + j * 32 + tid * 16); }
  }
  STAMP(11);
  {
    int xo_ = WS_XB + b * 2048 + tid * 4, xw = L_XL + tid * 4, xq = xqa, qw = qwr, qr = qrd, qp = qpub + 4 * 8 * 4096;
    PIN4(xo_, xw, xq, qw); PIN2(qr, qp);
    if (DLY_E) __builtin_amdgcn_s_sleep(DLY_E);
    u32 xv;
    for (;;) { CBAR(); xv = ld32s(rs, xo_) ^ tag; if (__all((xv & TMASK) == 0) || --budget <= 0) break; }
    STAMP(26); STAMPV(27, budget); STAMPW(120);
    xres = asf(xv);
    *(LAS float*)(L + xw) = xres;
    float mean, var;
    ln_stats(xres, L, w, lane, mean, var);
    const v4u qa2 = q_operand(L, xq, mean, var);
    STAMP(12);
    q_gemv(wqf, qa2, L, tid, lane, qw, qr, rs, qp, tag);
  }
  STAMP(13);
  {
    v4u vf[16];
#pragma unroll
    for (int t = 0; t < 4; ++t)
#pragma unroll
      for (int u = 0; u < 4; ++u) vf[t * 4 + u] = v_frag(t, u, L, vbase);
    PIN8(vf); { v4u* v2 = vf + 8; PIN8(v2); }
    int qo2 = qo + 4 * 8 * 4096, b0 = a_qwr, b1 = a_qrd, b2_ = a_mwr, b3 = a_mrd, b4 = a_pwr, b5 = a_lwr, pr = L_PL + lane * 16, po2 = WS_OB + (1 << 19) + pub_o, ps2 = WS_OS + 32768 + pub_s;
    PIN4(qo2, b0, b1, b2_); PIN4(b3, b4, b5, pr); PIN2(po2, ps2);
    if (DLY_A) __builtin_amdgcn_s_sleep(DLY_A);
    for (;;) { CBAR(); q0 = ld32s(rs, qo2) ^ tag; q1 = ld32s(rs, qo2 + 2048) ^ tag; if (__all(((q0 | q1) & TMASK) == 0) || --budget <= 0) break; }
    STAMP(14); STAMPV(18, budget);
    M = attn_scores(asf(q0) + asf(q1), kf, L, b0, b1, b2_, b3, b4, b5, lane);
    BAR();
    v4u pf[4];
#pragma unroll
    for (int t = 0; t < 4; ++t) pf[t] = *(const LAS v4u*)(L + pr + t * PSTR);
    SB();
#pragma unroll
    for (int t = 0; t < 4; ++t)
#pragma unroll
      for (int u = 0; u < 4; ++u) acc[u] = mfma(vf[t * 4 + u], pf[t], t == 0 ? f4{0.f, 0.f, 0.f, 0.f} : acc[u]);
    attn_publish_stats(M, L, w, lane, rs, ps2, tag, T0);
    attn_publish(acc, lane, rs, po2, T0);
  }
  STAMP(15);
  g_oo += 1 << 19; g_so += 32768; o_pub += 65536; g_pb += 1 << 19;
  PIN2(g_oo, g_so); PIN1(o_pub);
  BAR();
  if (DLY_B) __builtin_amdgcn_s_sleep(DLY_B);
  o_gemv<false>(wof, wor, L, tid, lane, rs, g_oo, g_so, g_pb, o_xw, o_xr, o_wr, o_rd, o_pub, tag, T0, budget, w, 28);
  STAMP(16);
  {
    const f4 r = mixer(xres, L, tid, w, lane, j, rs, WS_PB + 65536 + b * 8 * 2048, tag, b2, budget, 30);
    if (w == 0) {
      const u32 lo = cvtpk(r[0], r[1]), hi = cvtpk(r[2], r[3]);
      if (lane < 2) { v2u o = {lo, hi}; *(GAS v2u*)((GAS unsigned char*)p.out + (b * 512 + 8 * j + 4 * lane) * 2) = o; }
      if (lane == 0 && j == 0) __builtin_amdgcn_raw_buffer_store_b32(T0, rs, WS_EP + b * 256, 0, 16);
    }
  }
  STAMP(17); STAMPR(37);
}

extern "C" void kernel_launch(void* const* d_in, const int* in_sizes, int n_in, void* d_out, int out_size, void* d_ws, size_t ws_size, hipStream_t stream) {
  Params p;
  p.x = (const u16*)d_in[0]; p.K = (const unsigned char*)d_in[1]; p.V = (const unsigned char*)d_in[2];
  p.wq = (const unsigned char*)d_in[3]; p.wo = (const unsigned char*)d_in[4];
  p.w1 = (const u16*)d_in[6]; p.b1 = (const u16*)d_in[7]; p.w2 = (const u16*)d_in[8]; p.b2 = (const u16*)d_in[9];
  p.out = (u16*)d_out; p.ws = (unsigned char*)d_ws;
  hipLaunchKernelGGL(fused_decode_kernel, dim3(256), dim3(512), 0, stream, p);
}
```

```cpp
#include <hip/hip_runtime.h>

typedef unsigned int u32;
typedef unsigned short u16;
typedef unsigned long long u64;
typedef u32 v4u __attribute__((ext_vector_type(4)));
typedef u32 v2u __attribute__((ext_vector_type(2)));
typedef float f4 __attribute__((ext_vector_type(4)));
typedef float f2 __attribute__((ext_vector_type(2)));
typedef short s8v __attribute__((ext_vector_type(8)));
typedef short s4v __attribute__((ext_vector_type(4)));
typedef __bf16 bf2v __attribute__((ext_vector_type(2)));
#define LAS __attribute__((address_space(3)))
#define GAS __attribute__((address_space(1)))
#define DI __device__ __forceinline__

#define STAMPS 0
#define MAPMODE 0
#define DLY_B 4
#define DLY_CD 10
#define DLY_E 11
#define DLY_A 5
#define STW 0
#define KVPOL " nt"
#define WPOL " nt"
#define PRETOUCH 2
#define SROT 0
#define WO_EARLY 8
#define OROT 4
#define PROBE 0
#define TLBTOUCH 1
#define WO_SLOW 8
#define XWIN 0
#define XWHO 0
#define WO_MODE 0
#define WO_PRE 1
#define WD2 5
#define WD3 5
#define WD 5
#define CODE_KB 21

constexpr int SK = 16384, WIN = 4096;
constexpr float EPS = 1e-5f;
constexpr float SCL = 0.08838834764831845f * 1.4426950408889634f;
constexpr float GK0 = -2.0f * 0.7978845608028654f * 1.4426950408889634f;
constexpr float GK1 = GK0 * 0.044715f;

constexpr int WS_EP = 0;
constexpr int WS_QB = 4096;
constexpr int WS_OB = WS_QB + 2 * 4 * 8 * 2 * 2048;
constexpr int WS_OS = WS_OB + 2 * 4 * 8 * 8 * 2048;
constexpr int WS_PB = WS_OS + 2 * 4 * 8 * 8 * 128;
constexpr int WS_XB = WS_PB + 2 * 4 * 8 * 2048;
constexpr int WS_END = WS_XB + 4 * 2048;
constexpr int WS_STAMP = 4 << 20;

constexpr int L_KV = 0;
constexpr int L_PAR = 131072;
constexpr int PSTR = 1088;
constexpr int L_PL = L_PAR + 11264;
constexpr int L_HB = L_PL + 4 * PSTR;
constexpr int L_ZERO = L_HB + 1024;
constexpr int QSTR = 288;
constexpr int L_QL = L_ZERO + 64;
constexpr int L_XL = L_QL + 4 * QSTR;
constexpr int L_RED = L_XL + 2048;
constexpr int L_ST = L_RED + 5120;
constexpr int L_MB = L_ST + 64;
constexpr int L_LB = L_MB + 128;
constexpr int L_R2 = L_LB + 512;
constexpr int L_STAMP = L_R2 + 128;
constexpr int L_END = L_STAMP + 1024 + 64;
static_assert(L_END <= 163840, "LDS");

struct Params {
  const u16* x; const unsigned char* K; const unsigned char* V; const unsigned char* wq; const unsigned char* wo;
  const u16* w1; const u16* b1; const u16* w2; const u16* b2; u16* out; unsigned char* ws;
};

template <int CTRL> DI float dpp(float x) {
  return __builtin_bit_cast(float, __builtin_amdgcn_update_dpp(0, __builtin_bit_cast(int, x), CTRL, 0xf, 0xf, true));
}
template <int CTRL, int RM> DI float dppm(float x) {
  return __builtin_bit_cast(float, __builtin_amdgcn_update_dpp(0, __builtin_bit_cast(int, x), CTRL, RM, 0xf, false));
}
constexpr int XOR1 = 0xB1, XOR2 = 0x4E, HMIR = 0x141, MIR = 0x140, ROR4 = 0x124, ROR8 = 0x128, BC15 = 0x142, BC31 = 0x143;
constexpr int SHL4 = 0x104, SHL8 = 0x108, SHL12 = 0x10C;
DI float asf(u32 x) { return __builtin_bit_cast(float, x); }
DI u32 asu(float x) { return __builtin_bit_cast(u32, x); }
DI u32 cvtpk(float lo, float hi) { f2 f = {lo, hi}; return __builtin_bit_cast(u32, __builtin_convertvector(f, bf2v)); }
DI f4 mfma(v4u a, v4u b, f4 c) {
  return __builtin_amdgcn_mfma_f32_16x16x32_bf16(__builtin_bit_cast(s8v, a), __builtin_bit_cast(s8v, b), c, 0, 0, 0);
}
DI float fmx(float a, float b) { return __builtin_elementwise_maximum(a, b); }
DI float xrow_max(float x) {
  auto s = __builtin_amdgcn_permlane16_swap(asu(x), asu(x), false, false);
  x = fmx(asf(s[0]), asf(s[1]));
  auto t = __builtin_amdgcn_permlane32_swap(asu(x), asu(x), false, false);
  return fmx(asf(t[0]), asf(t[1]));
}
DI float quad_reduce4(f4 r, int lane) {
  float t0 = (lane & 1) ? r[1] : r[0], t1 = (lane & 1) ? r[0] : r[1];
  t0 += dpp<XOR1>(t1);
  float t2 = (lane & 1) ? r[3] : r[2], t3 = (lane & 1) ? r[2] : r[3];
  t2 += dpp<XOR1>(t3);
  float s0 = (lane & 2) ? t2 : t0; const float s1 = (lane & 2) ? t0 : t2;
  s0 += dpp<XOR2>(s1);
  return s0;
}
DI void st32s(u32 v, __amdgpu_buffer_rsrc_t rs, int off) { __builtin_amdgcn_raw_buffer_store_b32(v, rs, off, 0, 16); }
DI u32 xo(u32 a, u32 t, u32 acc) { return __builtin_amdgcn_bitop3_b32(a, t, acc, 0xBE); }
#define PIN1(a) asm volatile("" : "+v"(a))
#define PIN2(a, b) asm volatile("" : "+v"(a), "+v"(b))
#define PIN4(a, b, c, d) asm volatile("" : "+v"(a), "+v"(b), "+v"(c), "+v"(d))
#define PIN8(x) asm volatile("" : "+v"(x[0]), "+v"(x[1]), "+v"(x[2]), "+v"(x[3]), "+v"(x[4]), "+v"(x[5]), "+v"(x[6]), "+v"(x[7]))
DI float xrow_sum(float x) {
  auto s = __builtin_amdgcn_permlane16_swap(asu(x), asu(x), false, false);
  x = asf(s[0]) + asf(s[1]);
  auto t = __builtin_amdgcn_permlane32_swap(asu(x), asu(x), false, false);
  return asf(t[0]) + asf(t[1]);
}
#define BAR() asm volatile("s_waitcnt lgkmcnt(0)\n\ts_barrier" ::: "memory")
#define CBAR() asm volatile("" ::: "memory")
#define SB() __builtin_amdgcn_sched_barrier(0)
#define WAITV(n) asm volatile("s_waitcnt vmcnt(" #n ")" ::: "memory")

#define STAMP(i) asm volatile("; MARK " #i)
#define STAMPR(i)
#define STAMPV(i, v)
#define STAMPW(i)

constexpr u32 TMASK = 0x3ffu;
DI u32 tagf(float v, u32 tag) { return __builtin_amdgcn_bitop3_b32(asu(v) + 0x200u, TMASK, tag, 0xBA); }
DI float untag(u32 v) { return asf(v & ~TMASK); }

DI u32 ld32s(__amdgpu_buffer_rsrc_t rs, int off) { return __builtin_amdgcn_raw_buffer_load_b32(rs, off, 0, 16); }
DI v4u ld128s(__amdgpu_buffer_rsrc_t rs, int off) { return __builtin_amdgcn_raw_buffer_load_b128(rs, off, 0, 16); }
DI void st128s(v4u v, __amdgpu_buffer_rsrc_t rs, int off) { __builtin_amdgcn_raw_buffer_store_b128(v, rs, off, 0, 16); }

DI void ln_stats(float x, LAS unsigned char* L, int w, int lane, float& mean, float& var) {
  const float c = asf(__builtin_amdgcn_readfirstlane(asu(x)));
  const float d = x - c;
  float s1 = d, s2 = d * d;
  s1 += dpp<XOR1>(s1); s2 += dpp<XOR1>(s2);
  s1 += dpp<XOR2>(s1); s2 += dpp<XOR2>(s2);
  s1 += dpp<HMIR>(s1); s2 += dpp<HMIR>(s2);
  s1 += dpp<MIR>(s1); s2 += dpp<MIR>(s2);
  s1 += dppm<BC15, 0xa>(s1); s2 += dppm<BC15, 0xa>(s2);
  s1 += dppm<BC31, 0xc>(s1); s2 += dppm<BC31, 0xc>(s2);
  const f2 pv = {fmaf(s1, 1.0f / 64, c), fmaf(s1 * s1, -1.0f / 64, s2)};
  if (lane == 63) *(LAS f2*)(L + L_ST + w * 8) = pv;
  BAR();
  const f2 v = *(const LAS f2*)(L + L_ST + (lane & 7) * 8);
  float m = v.x;
  m += dpp<XOR1>(m); m += dpp<XOR2>(m); m += dpp<HMIR>(m);
  mean = m * 0.125f;
  const float dv = v.x - mean;
  float t = fmaf(64.0f * dv, dv, v.y);
  t += dpp<XOR1>(t); t += dpp<XOR2>(t); t += dpp<HMIR>(t);
  var = t * (1.0f / 512);
}

DI void transpose8(const v4u (&raw)[8], v4u (&fr)[8]) {
#pragma unroll
  for (int i = 0; i < 8; ++i) {
    const u32 sel = (i & 1) ? 0x07060302u : 0x05040100u;
#pragma unroll
    for (int m = 0; m < 4; ++m) fr[i][m] = __builtin_amdgcn_perm(raw[2 * m + 1][i >> 1], raw[2 * m][i >> 1], sel);
  }
}

DI v4u q_operand(LAS unsigned char* L, int xaddr, float mean, float var) {
  const f4 a = *(const LAS f4*)(L + xaddr), b = *(const LAS f4*)(L + xaddr + 16);
  const float rs_ = __builtin_amdgcn_rsqf(var + EPS), mr = -mean * rs_;
  return v4u{cvtpk(fmaf(a[0], rs_, mr), fmaf(a[1], rs_, mr)), cvtpk(fmaf(a[2], rs_, mr), fmaf(a[3], rs_, mr)),
             cvtpk(fmaf(b[0], rs_, mr), fmaf(b[1], rs_, mr)), cvtpk(fmaf(b[2], rs_, mr), fmaf(b[3], rs_, mr))};
}

DI void q_gemv(const v4u (&wf)[8], v4u a, LAS unsigned char* L, int tid, int lane, int wraddr, int rdaddr, __amdgpu_buffer_rsrc_t rs, int qoff, u32 tag) {
  f4 d[8];
#pragma unroll
  for (int i = 0; i < 8; ++i) d[i] = mfma(a, wf[i], f4{0.f, 0.f, 0.f, 0.f});
  f4 v;
#pragma unroll
  for (int m = 0; m < 4; ++m) v[m] = (lane & 16) ? d[4 + m][0] : d[m][0];
  if (lane < 32) *(LAS f4*)(L + wraddr) = v;
  BAR();
  if (tid < 128) {
    const f4 r = *(const LAS f4*)(L + rdaddr) + *(const LAS f4*)(L + rdaddr + 4 * 576);
    st32s(tagf(quad_reduce4(r, lane), tag), rs, qoff);
  }
}

DI float attn_scores(float qv, const v4u (&kf)[16], LAS unsigned char* L, int qwr, int qrd, int mwr, int mrd, int pwr, int lwr, int lane) {
  *(LAS u16*)(L + qwr) = (u16)(cvtpk(qv, qv) & 0xffffu);
  BAR();
  v4u qf[4];
#pragma unroll
  for (int st = 0; st < 4; ++st) qf[st] = *(const LAS v4u*)(L + qrd + st * 64);
  SB();
  f4 d[4];
#pragma unroll
  for (int st = 0; st < 4; ++st)
#pragma unroll
    for (int t = 0; t < 4; ++t) d[t] = mfma(kf[t * 4 + st], qf[st], st == 0 ? f4{0.f, 0.f, 0.f, 0.f} : d[t]);
  float sc[4];
#pragma unroll
  for (int i = 0; i < 4; ++i) {
    const float a = (lane & 4) ? d[1][i] : d[0][i], bq = (lane & 4) ? d[3][i] : d[2][i];
    sc[i] = (lane & 8) ? bq : a;
  }
  float m = fmx(fmx(sc[0], sc[1]), fmx(sc[2], sc[3]));
  m = fmx(m, dpp<ROR4>(m)); m = fmx(m, dpp<ROR8>(m));
  m = xrow_max(m);
  if (lane < 4) *(LAS float*)(L + mwr) = m;
  BAR();
  const f4 m0 = *(const LAS f4*)(L + mrd), m1 = *(const LAS f4*)(L + mrd + 16);
  float M = fmx(fmx(fmx(m0[0], m0[1]), fmx(m0[2], m0[3])), fmx(fmx(m1[0], m1[1]), fmx(m1[2], m1[3])));
  M = asf(asu(M * SCL) & ~TMASK);
  float p[4];
#pragma unroll
  for (int i = 0; i < 4; ++i) p[i] = __builtin_amdgcn_exp2f(fmaf(sc[i], SCL, -M));
  v2u pk = {cvtpk(p[0], p[1]), cvtpk(p[2], p[3])};
  *(LAS v2u*)(L + pwr) = pk;
  float l = (p[0] + p[1]) + (p[2] + p[3]);
  l += dpp<ROR4>(l); l += dpp<ROR8>(l);
  if ((lane & 12) == 0) *(LAS float*)(L + lwr) = l;
  return M;
}

DI v4u v_frag(int t, int u, LAS unsigned char* L, int vbase) {
  const s4v lo = __builtin_amdgcn_ds_read_tr16_b64_v4i16((LAS s4v*)(L + vbase + u * 32768 + t * 4096));
  const s4v hi = __builtin_amdgcn_ds_read_tr16_b64_v4i16((LAS s4v*)(L + vbase + u * 32768 + t * 4096 + 1024));
  const v2u l2 = __builtin_bit_cast(v2u, lo), h2 = __builtin_bit_cast(v2u, hi);
  return v4u{l2[0], l2[1], h2[0], h2[1]};
}
DI void pv_chunk(int t, f4 (&acc)[4], LAS unsigned char* L, int vbase, int lane) {
  const v4u pf = *(const LAS v4u*)(L + L_PL + t * PSTR + lane * 16);
#pragma unroll
  for (int u = 0; u < 4; ++u) acc[u] = mfma(pf, v_frag(t, u, L, vbase), acc[u]);
}

DI void attn_publish_stats(float M, LAS unsigned char* L, int w, int lane, __amdgpu_buffer_rsrc_t rs, int saddr, u32 tag, u32 T0) {
  if (w == 7) {
    float l = *(const LAS float*)(L + L_LB + lane * 4) + *(const LAS float*)(L + L_LB + (lane + 64) * 4);
    l += dpp<ROR4>(l); l += dpp<ROR8>(l);
    l = xrow_sum(l);
    if (lane < 8) { v4u sv = {asu(M) | tag, tagf(l, tag), T0, T0}; st128s(sv, rs, saddr); }
  }
}
template <int RM> DI float rowput(float old, float x) {
  return __builtin_bit_cast(float, __builtin_amdgcn_update_dpp(__builtin_bit_cast(int, old), __builtin_bit_cast(int, x), 0xE4, RM, 0xf, false));
}
DI void attn_publish(const f4 (&acc)[4], int lane, __amdgpu_buffer_rsrc_t rs, int oaddr, u32 T0) {
  float y[4];
#pragma unroll
  for (int h = 0; h < 4; ++h) y[h] = rowput<0x8>(rowput<0x4>(rowput<0x2>(acc[0][h], acc[1][h]), acc[2][h]), acc[3][h]);
  auto s01 = __builtin_amdgcn_permlane16_swap(asu(y[0]), asu(y[1]), false, false);
  auto s23 = __builtin_amdgcn_permlane16_swap(asu(y[2]), asu(y[3]), false, false);
  const float z01 = asf(s01[0]) + asf(s01[1]), z23 = asf(s23[0]) + asf(s23[1]);
  auto sf = __builtin_amdgcn_permlane32_swap(asu(z01), asu(z23), false, false);
  const float v = asf(sf[0]) + asf(sf[1]);
  st32s(__builtin_amdgcn_perm(cvtpk(v, v), T0, 0x05040100u), rs, oaddr);
}

DI bool o_check(const v4u& x0, const v4u& x1, const v4u& sv, u32 T0) {
  u32 d = xo(x0[0], T0, 0u);
  d = xo(x0[1], T0, d); d = xo(x0[2], T0, d); d = xo(x0[3], T0, d);
  d = xo(x1[0], T0, d); d = xo(x1[1], T0, d); d = xo(x1[2], T0, d); d = xo(x1[3], T0, d);
  d = xo(sv[0], T0, d); d = xo(sv[1], T0, d);
  d = xo(sv[3], T0, xo(sv[2], T0, d & TMASK));
  return __all(d == 0);
}
template <bool FIRST>
DI void o_gemv(v4u (&wf)[8], v4u (&wraw)[8], LAS unsigned char* L, int tid, int lane, __amdgpu_buffer_rsrc_t rs, int oo, int so, int pb, int xw, int xr, int wraddr, int rdaddr, int poff, u32 tag, u32 T0, int& budget, int w, int sb) {
  v4u x0, x1, sv = {T0, T0, T0, T0};
  if (sb == 28) STAMP(39);
  CBAR();
  x0 = ld128s(rs, oo); x1 = ld128s(rs, oo + 256);
  if (lane < 16) sv = ld128s(rs, so);
  if (FIRST) {
    asm volatile("s_waitcnt vmcnt(3)" : "+v"(wraw[0]), "+v"(wraw[1]), "+v"(wraw[2]), "+v"(wraw[3]), "+v"(wraw[4]), "+v"(wraw[5]), "+v"(wraw[6]), "+v"(wraw[7])::"memory");
    STAMP(9); STAMPW(80);
    transpose8(wraw, wf);
    PIN8(wf);
  }
  while (!o_check(x0, x1, sv, T0) && --budget > 0) {
    CBAR();
    x0 = ld128s(rs, oo); x1 = ld128s(rs, oo + 256);
    if (lane < 16) sv = ld128s(rs, so);
  }
  STAMP(sb); STAMPV(sb + 1, budget); if (sb == 28) STAMPW(104);
  const v4u pk = {__builtin_amdgcn_perm(x0[1], x0[0], 0x07060302u), __builtin_amdgcn_perm(x0[3], x0[2], 0x07060302u),
                  __builtin_amdgcn_perm(x1[1], x1[0], 0x07060302u), __builtin_amdgcn_perm(x1[3], x1[2], 0x07060302u)};
  *(LAS v4u*)(L + xw) = pk;
  CBAR();
  const v4u a = *(const LAS v4u*)(L + xr);
  const float ms = untag(sv[0]), ls = untag(sv[1]);
  float M = ms;
  M = fmx(M, dpp<XOR1>(M)); M = fmx(M, dpp<XOR2>(M)); M = fmx(M, dpp<HMIR>(M));
  const float f = __builtin_amdgcn_exp2f(ms - M);
  float Ls = f * ls;
  Ls += dpp<XOR1>(Ls); Ls += dpp<XOR2>(Ls); Ls += dpp<HMIR>(Ls);
  const float cs = f * __builtin_amdgcn_rcpf(Ls);
  f4 d[8];
#pragma unroll
  for (int i = 0; i < 8; ++i) d[i] = mfma(a, wf[i], f4{0.f, 0.f, 0.f, 0.f});
  const int bp = (lane >> 4) * 16;
  const float cr0 = asf(__builtin_amdgcn_ds_bpermute(bp, asu(cs))), cr1 = asf(__builtin_amdgcn_ds_bpermute(bp + 4, asu(cs)));
  const float cr2 = asf(__builtin_amdgcn_ds_bpermute(bp + 8, asu(cs))), cr3 = asf(__builtin_amdgcn_ds_bpermute(bp + 12, asu(cs)));
  const f2 c01 = {cr0, cr1}, c23 = {cr2, cr3};
  float e[8];
#pragma unroll
  for (int i = 0; i < 8; ++i) { const f2 t = f2{d[i][2], d[i][3]} * c23 + f2{d[i][0], d[i][1]} * c01; e[i] = t[0] + t[1]; }
  f4 v;
#pragma unroll
  for (int m = 0; m < 4; ++m) { auto t = __builtin_amdgcn_permlane16_swap(asu(e[m]), asu(e[m + 4]), false, false); v[m] = asf(t[0]) + asf(t[1]); }
  if ((lane >> 5) == ((lane >> 3) & 1)) *(LAS f4*)(L + wraddr) = v;
  BAR();
  if (tid < 64) {
    const f4 r = (*(const LAS f4*)(L + rdaddr) + *(const LAS f4*)(L + rdaddr + 4 * 320)) + (*(const LAS f4*)(L + rdaddr + 8 * 320) + *(const LAS f4*)(L + rdaddr + 12 * 320));
    st32s(tagf(quad_reduce4(r, lane), tag), rs, poff);
  }
}

DI f4 mixer(float& xres, LAS unsigned char* L, int tid, int w, int lane, int j, __amdgpu_buffer_rsrc_t rs, int pbase, u32 tag, float b2, int& budget, int sb) {
  float pw1[7], pb1[7], pw2[7];
  const int fl = tid & 255, q = tid >> 8;
#pragma unroll
  for (int i = 0; i < 7; ++i) {
    pw1[i] = asf((u32) * (const LAS u16*)(L + L_PAR + (fl + 256 * i) * 2) << 16);
    pb1[i] = asf((u32) * (const LAS u16*)(L + L_PAR + 3584 + (fl + 256 * i) * 2) << 16);
    pw2[i] = asf((u32) * (const LAS u16*)(L + L_PAR + 7168 + (fl + 256 * i) * 2) << 16);
  }
#pragma unroll
  for (int i = 0; i < 7; ++i) asm volatile("" : "+v"(pw1[i]), "+v"(pb1[i]), "+v"(pw2[i]));
  int po = pbase + tid * 4, xa = L_XL + (8 * j + 4 * q) * 4, xw = L_XL + tid * 4;
  asm volatile("" : "+v"(po), "+v"(xa), "+v"(xw));
  if (DLY_CD) __builtin_amdgcn_s_sleep(DLY_CD);
  if (sb == 30) STAMP(35);
  u32 p[8];
  for (;;) {
    CBAR();
#pragma unroll
    for (int i = 0; i < 8; ++i) p[i] = ld32s(rs, po + i * 2048) ^ tag;
    const u32 d = (p[0] | p[1] | p[2]) | (p[3] | p[4] | p[5]) | (p[6] | p[7]);
    if (__all((d & TMASK) == 0) || --budget <= 0) break;
  }
  STAMP(sb); STAMPV(sb + 1, budget); if (sb == 30) STAMPW(112);
  const float x1 = xres + (((asf(p[0]) + asf(p[1])) + (asf(p[2]) + asf(p[3]))) + ((asf(p[4]) + asf(p[5])) + (asf(p[6]) + asf(p[7]))));
  xres = x1;
  *(LAS float*)(L + xw) = x1;
  float mean, var;
  ln_stats(x1, L, w, lane, mean, var);
  const f4 xv = *(const LAS f4*)(L + xa);
  const float r1 = __builtin_amdgcn_rsqf(var + EPS);
  const float r2 = __builtin_amdgcn_rsqf(var * r1 * r1 + EPS);
  const float rr = r1 * r2, mr = -mean * rr;
  STAMP(sb + 2); if (sb == 22) STAMPW(88);
  const f2 hA = {fmaf(xv[0], rr, mr), fmaf(xv[1], rr, mr)}, hB = {fmaf(xv[2], rr, mr), fmaf(xv[3], rr, mr)};
  f2 aA = {0.f, 0.f}, aB = {0.f, 0.f};
#pragma unroll
  for (int i = 0; i < 7; ++i) {
    const f2 w1 = {pw1[i], pw1[i]}, b1 = {pb1[i], pb1[i]}, w2 = {pw2[i], pw2[i]};
    const f2 k0 = {GK0, GK0}, k1 = {GK1, GK1}, one = {1.f, 1.f};
    {
      const f2 u = hA * w1 + b1, z = u * (u * u * k1 + k0);
      const f2 e = {__builtin_amdgcn_exp2f(z[0]), __builtin_amdgcn_exp2f(z[1])}, dd = e + one;
      const f2 rc = {__builtin_amdgcn_rcpf(dd[0]), __builtin_amdgcn_rcpf(dd[1])};
      aA += (u * rc) * w2;
    }
    {
      const f2 u = hB * w1 + b1, z = u * (u * u * k1 + k0);
      const f2 e = {__builtin_amdgcn_exp2f(z[0]), __builtin_amdgcn_exp2f(z[1])}, dd = e + one;
      const f2 rc = {__builtin_amdgcn_rcpf(dd[0]), __builtin_amdgcn_rcpf(dd[1])};
      aB += (u * rc) * w2;
    }
  }
  float t0 = (lane & 1) ? aA[1] : aA[0], t1 = (lane & 1) ? aA[0] : aA[1];
  t0 += dpp<XOR1>(t1);
  float t2 = (lane & 1) ? aB[1] : aB[0], t3 = (lane & 1) ? aB[0] : aB[1];
  t2 += dpp<XOR1>(t3);
  float s0 = (lane & 2) ? t2 : t0, s1 = (lane & 2) ? t0 : t2;
  s0 += dpp<XOR2>(s1);
  s0 += dpp<ROR4>(s0); s0 += dpp<ROR8>(s0);
  s0 = xrow_sum(s0);
  STAMP(sb + 3); if (sb == 22) STAMPW(96);
  if (lane < 4) *(LAS float*)(L + L_R2 + (w * 4 + lane) * 4) = s0;
  BAR();
  f4 res = {0.f, 0.f, 0.f, 0.f};
  if (w == 0) {
    const int qq = lane & 1;
    const f4 a = (*(const LAS f4*)(L + L_R2 + (qq * 4 + 0) * 16) + *(const LAS f4*)(L + L_R2 + (qq * 4 + 1) * 16)) +
                 (*(const LAS f4*)(L + L_R2 + (qq * 4 + 2) * 16) + *(const LAS f4*)(L + L_R2 + (qq * 4 + 3) * 16));
    const f4 xr = *(const LAS f4*)(L + L_XL + (8 * j + 4 * qq) * 4);
    res = xr + a + f4{b2, b2, b2, b2};
  }
  return res;
}

__global__ void __launch_bounds__(512, 2) fused_decode_kernel(Params p) {
  __shared__ __attribute__((aligned(1024))) unsigned char smem[L_END];
  LAS unsigned char* L = (LAS unsigned char*)smem;
  const int tid = threadIdx.x, lane = tid & 63, w = __builtin_amdgcn_readfirstlane(tid >> 6);
  const int bid = blockIdx.x;
  int b, kvh, s;
  if (MAPMODE == 0) { kvh = bid & 7; b = bid >> 6; s = ((bid >> 3) + SROT) & 7; }
  else { const int x = bid & 7, i = (bid >> 3) + 32 * (x & 1); b = x >> 1; kvh = i >> 3; s = i & 7; }
  const int j = kvh * 8 + s;
  const int g = lane >> 4, c = lane & 15;
  STAMPR(36); STAMP(0);

  const size_t slab = ((size_t)(b * 8 + kvh) * SK + (SK - WIN) + s * 512) * 256;
  const __amdgpu_buffer_rsrc_t rK = __builtin_amdgcn_make_buffer_rsrc((void*)(p.K + slab), 0, 512 * 256, 0x00020000);
  const __amdgpu_buffer_rsrc_t rV = __builtin_amdgcn_make_buffer_rsrc((void*)(p.V + slab), 0, 512 * 256, 0x00020000);
  const __amdgpu_buffer_rsrc_t rQ = __builtin_amdgcn_make_buffer_rsrc((void*)p.wq, 0, 512 * 4096 * 2, 0x00020000);
  const __amdgpu_buffer_rsrc_t rO = __builtin_amdgcn_make_buffer_rsrc((void*)p.wo, 0, 512 * 4096 * 2, 0x00020000);
  const __amdgpu_buffer_rsrc_t rs = __builtin_amdgcn_make_buffer_rsrc((void*)p.ws, 0, WS_END, 0x00020000);
  const __amdgpu_buffer_rsrc_t rX = __builtin_amdgcn_make_buffer_rsrc((void*)p.x, 0, 4096, 0x00020000);
  const u32 ldsb = (u32)(size_t)L;

  v4u dummy = {0u, 0u, 0u, 0u};
  u32 ev, xraw, b2raw, dummy2 = 0, dummy3 = 0;
  asm volatile("s_nop 4\n\tbuffer_load_dword %0, %1, %2, 0 offen sc1" : "=v"(ev) : "v"(WS_EP + b * 256), "s"(rs) : "memory");
  asm volatile("buffer_load_ushort %0, %1, %2, 0 offen" : "=v"(xraw) : "v"((b * 512 + tid) * 2), "s"(rX) : "memory");
  asm volatile("global_load_ushort %0, %1, %2" : "=v"(b2raw) : "v"(0), "s"(p.b2) : "memory");
  const int rot = __builtin_amdgcn_readfirstlane((int)(((u32)(bid * 8 + w) * 2654435761u) >> 28));
  const int cg = s >> 1, kh = s & 1;
  const int voq = ((256 * kh + 32 * w + 8 * g) * 4096 + kvh * 512 + 128 * cg + 8 * c) * 2;
  const int so_ = (s + OROT) & 7;
  const int voo = ((kvh * 512 + (2 * (w >> 2) + (c >> 3)) * 128 + 32 * (w & 3) + 4 * g) * 512 + 64 * so_ + 8 * (c & 7)) * 2;
  int vok[4], vov[2];
#pragma unroll
  for (int i = 0; i < 4; ++i) vok[i] = (64 * w + g) * 256 + ((c ^ (4 * i + g)) * 16);
#pragma unroll
  for (int i = 0; i < 2; ++i) vov[i] = (64 * w + g) * 256 + ((((c >> 1) ^ (g | (i << 2))) * 32) + (c & 1) * 16);
  v4u wqr[8], wor[8], kf[16];
#define LDQ(jj) asm volatile("buffer_load_dwordx4 %0, %1, %2, %3 offen" WPOL : "=v"(wqr[jj]) : "v"(voq), "s"(rQ), "s"((jj) * 8192) : "memory")
#define LDO(jj) asm volatile("buffer_load_dwordx4 %0, %1, %2, %3 offen" WPOL : "+v"(wor[jj]) : "v"(voo), "s"(rO), "s"((16 * ((jj) >> 2) + ((jj) & 3)) * 1024) : "memory")
#define LDK(i) asm volatile("s_mov_b32 m0, %0\n\ts_nop 0\n\tbuffer_load_dwordx4 %1, %2, %3 offen" KVPOL " lds" ::"s"(ldsb + L_KV + w * 16384 + (i) * 1024), "v"(vok[(i) & 3]), "s"(rK), "s"((((i) + rot) & 15) * 1024) : "memory")
#define LDV(i) asm volatile("s_mov_b32 m0, %0\n\ts_nop 0\n\tbuffer_load_dwordx4 %1, %2, %3 offen" KVPOL " lds" ::"s"(ldsb + L_KV + w * 16384 + (i) * 1024), "v"(vov[((i) >> 1) & 1]), "s"(rV), "s"((((i) + rot) & 15) * 1024) : "memory")
  u32 wide_v = XWHO == 0 ? (u32)(((so_ ^ 3) - 1) >> 31) & 1u : XWHO == 1 ? ((u32)(((so_ ^ 3) - 1) >> 31) | (u32)(((cg ^ 1) - 1) >> 31)) & 1u : (u32)(((cg ^ 1) - 1) >> 31) & 1u;
  asm volatile("" : "+v"(wide_v));
  const u32 wide = __builtin_amdgcn_readfirstlane(wide_v);
#define GW(d) asm volatile("s_waitcnt vmcnt(%0)" ::"n"((d) - 1) : "memory")
#define G() GW(GWIN)
#define GWIN WD
#define EV(n, i) asm volatile("s_waitcnt vmcnt(%0)" ::"n"((n) - (i) - 1) : "memory")
#define KFRAG(t)                                                                                                     \
  _Pragma("unroll") for (int st = 0; st < 4; ++st)                                                                   \
      kf[(t) * 4 + st] = *(const LAS v4u*)(L + L_KV + w * 16384 + (16 * (t) + c) * 256 + (((4 * st + g) ^ c) * 16)); \
  asm volatile("" : "+v"(kf[(t) * 4]), "+v"(kf[(t) * 4 + 1]), "+v"(kf[(t) * 4 + 2]), "+v"(kf[(t) * 4 + 3])::"memory")
  LDQ(0); LDK(0); LDQ(1); LDK(1); LDQ(2); LDK(2);
  STAMP(38);
  {
    const int piece = (bid >> 3) * 8 + w;
    if (piece < CODE_KB) {
      u64 pc; asm volatile("s_getpc_b64 %0" : "=s"(pc));
      const u64 a = (pc & ~(u64)1023) + (u64)piece * 1024 + lane * 16;
      asm volatile("global_load_dwordx4 %0, %1, off" : "+v"(dummy) : "v"(a) : "memory");
    }
  }
  if (w == 7) {
    const int me = (b * 8 + kvh) * 8 + s;
    int a_ = lane < 16 ? WS_OB + me * 2048 + lane * 128
           : lane < 32 ? WS_OB + (1 << 19) + me * 2048 + (lane - 16) * 128
           : lane < 34 ? WS_OS + (lane - 32) * 32768 + me * 128
           : lane < 42 ? WS_QB + ((lane - 34) >> 2) * (4 * 8 * 4096) + ((b * 8 + kvh) * 2 + (s & 1)) * 2048 + (s >> 1) * 512 + ((lane - 34) & 3) * 128
           : lane < 46 ? WS_PB + ((lane - 42) >> 1) * 65536 + (b * 8 + kvh) * 2048 + ((s + OROT) & 7) * 256 + ((lane - 42) & 1) * 128
           : WS_XB + b * 2048 + j * 32;
    asm volatile("buffer_load_dword %0, %1, %2, 0 offen" : "+v"(dummy2) : "v"(a_), "s"(rs) : "memory");
  }
  if (w == 6) {
    const unsigned char* a_ = lane == 0 ? p.V + slab : lane == 1 ? p.V + slab + 131068 : lane == 2 ? p.wo + (size_t)kvh * 524288 : lane == 3 ? p.wo + (size_t)kvh * 524288 + 524284
                            : lane == 4 ? (const unsigned char*)p.w1 : lane == 5 ? (const unsigned char*)p.b1 : lane == 6 ? (const unsigned char*)p.w2 : lane == 7 ? p.K + slab + 131068
                            : lane == 8 ? (const unsigned char*)p.out : p.wq + (size_t)(s & 1) * 2097152 + 2097148;
    asm volatile("global_load_dword %0, %1, off" : "+v"(dummy3) : "v"(a_) : "memory");
  }
  G(); LDQ(3); G(); LDK(3);
  asm volatile("s_waitcnt vmcnt(8)" : "+v"(ev), "+v"(xraw), "+v"(b2raw)::"memory");
  const u32 T0 = __builtin_amdgcn_readfirstlane(ev) + 1u;
  const u32 tag = T0 & TMASK;
  float xres = asf(xraw << 16);
  const int xqa = L_XL + (256 * kh + 32 * w + 8 * g) * 4;
  v4u qa;
  {
    *(LAS float*)(L + L_XL + tid * 4) = xres;
    float mean, var;
    ln_stats(xres, L, w, lane, mean, var);
    qa = q_operand(L, xqa, mean, var);
    PIN1(qa);
  }
  G(); LDQ(4); G(); LDK(4); G(); LDQ(5); G(); LDK(5);
  G(); LDQ(6); G(); LDK(6); G(); LDQ(7); G(); LDK(7);
  EV(16, 7); STAMPW(48); KFRAG(0);
  G(); LDK(8); G(); LDK(9); G(); LDK(10); G(); LDK(11); G(); LDK(12);
  EV(21, 14);
  asm volatile("" : "+v"(wqr[0]), "+v"(wqr[1]), "+v"(wqr[2]), "+v"(wqr[3]), "+v"(wqr[4]), "+v"(wqr[5]), "+v"(wqr[6]), "+v"(wqr[7]), "+v"(dummy), "+v"(dummy2), "+v"(dummy3)::"memory");
  STAMP(1); STAMPW(40);
  LDK(13); LDK(14); LDK(15);
  {
#pragma unroll
    for (int k = 0; k < 2; ++k) {
      const int piece = k == 0 ? w : 8 + (w % 3);
      int gr = piece * 64 + lane; gr = gr < 672 ? gr : 671;
      const int arr = gr / 224, e = gr - arr * 224;
      const unsigned char* src = (const unsigned char*)(arr == 0 ? p.w1 : arr == 1 ? p.b1 : p.w2) + e * 16;
      asm volatile("s_mov_b32 m0, %0\n\ts_nop 0\n\tglobal_load_lds_dwordx4 %1, off" ::"s"(ldsb + L_PAR + piece * 1024), "v"(src) : "memory");
    }
  }
  LDV(0); LDV(1);
  v4u wqf[8];
  transpose8(wqr, wqf);
  const int qwr = L_RED + w * 576 + (8 * c + 4 * (g & 1)) * 4, qrd = L_RED + (tid & 3) * 576 + (tid >> 2) * 16;
  const int qpub = WS_QB + ((b * 8 + kvh) * 2 + kh) * 2048 + cg * 512 + tid * 4;
  q_gemv(wqf, qa, L, tid, lane, qwr, qrd, rs, qpub, tag);
  STAMP(2);
#undef GWIN
#define GWIN WD2
  EV(28, 15); KFRAG(1);
  G(); LDV(2); G(); LDV(3);
  EV(30, 19); KFRAG(2);
  asm volatile("" : "=v"(wor[0]), "=v"(wor[1]), "=v"(wor[2]), "=v"(wor[3]), "=v"(wor[4]), "=v"(wor[5]), "=v"(wor[6]), "=v"(wor[7]));
  const u32 m_early = __builtin_amdgcn_readfirstlane(so_ == 3 ? 0u : ~0u), m_late = ~m_early;
#define LDOM(jj, m) asm volatile("s_mov_b32 exec_lo, %4\n\ts_mov_b32 exec_hi, %4\n\tbuffer_load_dwordx4 %0, %1, %2, %3 offen" WPOL "\n\ts_mov_b64 exec, -1" : "+v"(wor[jj]) : "v"(voo), "s"(rO), "s"((16 * ((jj) >> 2) + ((jj) & 3)) * 1024), "s"(m) : "memory")
#define CNS(k) ((k) < WO_SLOW ? (k) : WO_SLOW)
#define LDOE(k) do { G(); if ((k) < WO_SLOW) LDO(k); else LDOM(k, m_early); } while (0)
#define LDOU(k) do { if ((k) < WO_SLOW) LDO(k); else LDOM(k, m_early); } while (0)
#define WAITN(n) asm volatile("s_waitcnt vmcnt(%0)" ::"n"(n) : "memory")
  LDOE(0); G(); LDV(4); G(); LDV(5);
  STAMP(3);
  u32 q0, q1;
  const int qo = WS_QB + (b * 8 + kvh) * 4096 + tid * 4;
  G(); asm volatile("buffer_load_dword %0, %1, %2, 0 offen sc1" : "=v"(q0) : "v"(qo), "s"(rs) : "memory");
  asm volatile("buffer_load_dword %0, %1, %2, 0 offen offset:2048 sc1" : "=v"(q1) : "v"(qo), "s"(rs) : "memory");
  WAITN(10 + CNS(1)); KFRAG(3);
  STAMP(4); STAMPW(56);
  LDOE(1); G(); LDV(6); G(); LDV(7); LDOE(2); G(); LDV(8); G(); LDV(9);
  WAITN(4 + CNS(3) - CNS(1));
  asm volatile("" : "+v"(q0), "+v"(q1)::"memory");
  STAMP(5); STAMPW(64);
  LDOU(3); LDV(10); LDV(11); LDOU(4); LDV(12); LDV(13);
  int budget = 100000;
  if (!__all((((q0 ^ tag) | (q1 ^ tag)) & TMASK) == 0)) {
    _Pragma("nounroll") do {
      asm volatile("buffer_load_dword %0, %2, %3, 0 offen sc1\n\tbuffer_load_dword %1, %2, %3, 0 offen offset:2048 sc1\n\ts_waitcnt vmcnt(0)" : "=&v"(q0), "=&v"(q1) : "v"(qo), "s"(rs) : "memory");
    } while (!__all((((q0 ^ tag) | (q1 ^ tag)) & TMASK) == 0) && --budget > 0);
  }
  STAMPV(19, budget);
  const int vbase = L_KV + (g >> 1) * 16384 + (8 * (g & 1) + (c >> 2)) * 256 + ((w ^ ((c >> 2) | ((g & 1) << 2))) * 32) + (c & 3) * 8;
  const int a_qwr = L_QL + (tid >> 7) * QSTR + (tid & 127) * 2, a_qrd = L_QL + (c & 3) * QSTR + g * 16;
  const int a_mwr = L_MB + ((lane & 3) * 8 + w) * 4, a_mrd = L_MB + (c & 3) * 32;
  const int a_pwr = L_PL + (c >> 2) * PSTR + ((2 * (w & 1) + (g >> 1)) * 16 + (w >> 1) * 4 + (c & 3)) * 16 + (g & 1) * 8;
  const int a_lwr = L_LB + (w * 16 + g * 4 + (c & 3)) * 4;
  f4 acc[4];
  float M = attn_scores(asf(q0 ^ tag) + asf(q1 ^ tag), kf, L, a_qwr, a_qrd, a_mwr, a_mrd, a_pwr, a_lwr, lane);
  STAMP(6);
#undef GWIN
#define GWIN WD3
  LDOE(5); LDOE(6); LDOE(7); G(); LDV(14); G(); LDV(15);
  WAITN(4 + CNS(8) - CNS(4));
  BAR();
  const int pub_o = ((b * 8 + kvh) * 8 + s) * 2048 + w * 256 + lane * 4, pub_s = ((b * 8 + kvh) * 8 + s) * 128 + (lane & 7) * 16;
  attn_publish_stats(M, L, w, lane, rs, WS_OS + pub_s, tag, T0);
#pragma unroll
  for (int u = 0; u < 4; ++u) acc[u] = f4{0.f, 0.f, 0.f, 0.f};
  pv_chunk(0, acc, L, vbase, lane);
  pv_chunk(1, acc, L, vbase, lane);
  pv_chunk(2, acc, L, vbase, lane);
  v4u pf3 = *(const LAS v4u*)(L + L_PL + 3 * PSTR + lane * 16);
  PIN1(pf3);
  SB();
  WAITN(0);
  STAMP(7); STAMPW(72);
  BAR();
  {
    v4u vf3[4];
#pragma unroll
    for (int u = 0; u < 4; ++u) vf3[u] = v_frag(3, u, L, vbase);
    SB();
    if (WO_SLOW <= 0) LDOM(0, m_late);
    if (WO_SLOW <= 1) LDOM(1, m_late);
    if (WO_SLOW <= 2) LDOM(2, m_late);
    if (WO_SLOW <= 3) LDOM(3, m_late);
    if (WO_SLOW <= 4) LDOM(4, m_late);
    if (WO_SLOW <= 5) LDOM(5, m_late);
    if (WO_SLOW <= 6) LDOM(6, m_late);
    if (WO_SLOW <= 7) LDOM(7, m_late);
    SB();
#pragma unroll
    for (int u = 0; u < 4; ++u) acc[u] = mfma(pf3, vf3[u], acc[u]);
  }
  int po1 = WS_OB + pub_o;
  attn_publish(acc, lane, rs, po1, T0);
  STAMP(8);
  v4u wof[8];
  const float b2 = asf(b2raw << 16);
  int g_oo = (b * 8 + kvh) * 8 * 2048 + (lane >> 3) * 2048 + 2 * (w & 3) * 256 + (w >> 2) * 128 + (lane & 7) * 16;
  int g_so = (b * 8 + kvh) * 8 * 128 + (lane & 7) * 128 + (2 * (w >> 2) + ((lane >> 3) & 1)) * 16;
  int o_xw = L_PL + w * 1024 + (lane ^ (lane >> 4)) * 16, o_xr = L_PL + w * 1024 + (((c & 7) * 8 + 4 * (c >> 3) + g) ^ ((c & 7) >> 1)) * 16;
  int o_wr = L_RED + (2 * w + (g >> 1)) * 320 + (8 * (c & 7) + 4 * (g & 1)) * 4, o_rd = L_RED + (tid & 3) * 320 + (tid >> 2) * 16;
  int o_pub = WS_PB + (b * 8 + kvh) * 2048 + so_ * 256 + tid * 4;
  g_oo += WS_OB; g_so += WS_OS;
  int g_pb = WS_OB + (b * 8 + kvh) * 8 * 2048 + 7 * 256 + 192 + w * 4;
  PIN4(g_oo, g_so, o_wr, o_rd); PIN1(o_pub); PIN2(o_xw, o_xr);
  BAR();

  if (DLY_B) __builtin_amdgcn_s_sleep(DLY_B);
  o_gemv<true>(wof, wor, L, tid, lane, rs, g_oo, g_so, g_pb, o_xw, o_xr, o_wr, o_rd, o_pub, tag, T0, budget, w, 20);
  STAMP(10);
  {
    const f4 r = mixer(xres, L, tid, w, lane, j, rs, WS_PB + b * 8 * 2048, tag, b2, budget, 22);
    if (tid < 2) { v4u o = {tagf(r[0], tag), tagf(r[1], tag), tagf(r[2], tag), tagf(r[3], tag)}; st128s(o, rs, WS_XB + b * 2048 + j * 32 + tid * 16); }
  }
  STAMP(11);
  {
    int xo_ = WS_XB + b * 2048 + tid * 4, xw = L_XL + tid * 4, xq = xqa, qw = qwr, qr = qrd, qp = qpub + 4 * 8 * 4096;
    PIN4(xo_, xw, xq, qw); PIN2(qr, qp);
    if (DLY_E) __builtin_amdgcn_s_sleep(DLY_E);
    u32 xv;
    for (;;) { CBAR(); xv = ld32s(rs, xo_) ^ tag; if (__all((xv & TMASK) == 0) || --budget <= 0) break; }
    STAMP(26); STAMPV(27, budget); STAMPW(120);
    xres = asf(xv);
    *(LAS float*)(L + xw) = xres;
    float mean, var;
    ln_stats(xres, L, w, lane, mean, var);
    const v4u qa2 = q_operand(L, xq, mean, var);
    STAMP(12);
    q_gemv(wqf, qa2, L, tid, lane, qw, qr, rs, qp, tag);
  }
  STAMP(13);
  {
    v4u vf[16];
#pragma unroll
    for (int t = 0; t < 4; ++t)
#pragma unroll
      for (int u = 0; u < 4; ++u) vf[t * 4 + u] = v_frag(t, u, L, vbase);
    PIN8(vf); { v4u* v2 = vf + 8; PIN8(v2); }
    int qo2 = qo + 4 * 8 * 4096, b0 = a_qwr, b1 = a_qrd, b2_ = a_mwr, b3 = a_mrd, b4 = a_pwr, b5 = a_lwr, pr = L_PL + lane * 16, po2 = WS_OB + (1 << 19) + pub_o, ps2 = WS_OS + 32768 + pub_s;
    PIN4(qo2, b0, b1, b2_); PIN4(b3, b4, b5, pr); PIN2(po2, ps2);
    if (DLY_A) __builtin_amdgcn_s_sleep(DLY_A);
    for (;;) { CBAR(); q0 = ld32s(rs, qo2) ^ tag; q1 = ld32s(rs, qo2 + 2048) ^ tag; if (__all(((q0 | q1) & TMASK) == 0) || --budget <= 0) break; }
    STAMP(14); STAMPV(18, budget);
    M = attn_scores(asf(q0) + asf(q1), kf, L, b0, b1, b2_, b3, b4, b5, lane);
    BAR();
    v4u pf[4];
#pragma unroll
    for (int t = 0; t < 4; ++t) pf[t] = *(const LAS v4u*)(L + pr + t * PSTR);
    SB();
#pragma unroll
    for (int t = 0; t < 4; ++t)
#pragma unroll
      for (int u = 0; u < 4; ++u) acc[u] = mfma(pf[t], vf[t * 4 + u], t == 0 ? f4{0.f, 0.f, 0.f, 0.f} : acc[u]);
    attn_publish_stats(M, L, w, lane, rs, ps2, tag, T0);
    attn_publish(acc, lane, rs, po2, T0);
  }
  STAMP(15);
  g_oo += 1 << 19; g_so += 32768; o_pub += 65536; g_pb += 1 << 19;
  PIN2(g_oo, g_so); PIN1(o_pub);
  BAR();
  if (DLY_B) __builtin_amdgcn_s_sleep(DLY_B);
  o_gemv<false>(wof, wor, L, tid, lane, rs, g_oo, g_so, g_pb, o_xw, o_xr, o_wr, o_rd, o_pub, tag, T0, budget, w, 28);
  STAMP(16);
  {
    const f4 r = mixer(xres, L, tid, w, lane, j, rs, WS_PB + 65536 + b * 8 * 2048, tag, b2, budget, 30);
    if (w == 0) {
      const u32 lo = cvtpk(r[0], r[1]), hi = cvtpk(r[2], r[3]);
      if (lane < 2) { v2u o = {lo, hi}; *(GAS v2u*)((GAS unsigned char*)p.out + (b * 512 + 8 * j + 4 * lane) * 2) = o; }
      if (lane == 0 && j == 0) __builtin_amdgcn_raw_buffer_store_b32(T0, rs, WS_EP + b * 256, 0, 16);
    }
  }
  STAMP(17); STAMPR(37);
}

extern "C" void kernel_launch(void* const* d_in, const int* in_sizes, int n_in, void* d_out, int out_size, void* d_ws, size_t ws_size, hipStream_t stream) {
  Params p;
  p.x = (const u16*)d_in[0]; p.K = (const unsigned char*)d_in[1]; p.V = (const unsigned char*)d_in[2];
  p.wq = (const unsigned char*)d_in[3]; p.wo = (const unsigned char*)d_in[4];
  p.w1 = (const u16*)d_in[6]; p.b1 = (const u16*)d_in[7]; p.w2 = (const u16*)d_in[8]; p.b2 = (const u16*)d_in[9];
  p.out = (u16*)d_out; p.ws = (unsigned char*)d_ws;
  hipLaunchKernelGGL(fused_decode_kernel, dim3(256), dim3(512), 0, stream, p);
}
```

```cpp
#include <hip/hip_runtime.h>

typedef unsigned int u32;
typedef unsigned short u16;
typedef unsigned long long u64;
typedef u32 v4u __attribute__((ext_vector_type(4)));
typedef u32 v2u __attribute__((ext_vector_type(2)));
typedef float f4 __attribute__((ext_vector_type(4)));
typedef float f2 __attribute__((ext_vector_type(2)));
typedef short s8v __attribute__((ext_vector_type(8)));
typedef short s4v __attribute__((ext_vector_type(4)));
typedef __bf16 bf2v __attribute__((ext_vector_type(2)));
#define LAS __attribute__((address_space(3)))
#define GAS __attribute__((address_space(1)))
#define DI __device__ __forceinline__

#define STAMPS 0
#define DLY_B 10
#define DLY_CD 8
#define DLY_E 16
#define DLY_A 6
#define STW 0
#define KVPOL " nt"
#define WPOL " nt"
#define PRETOUCH 1
#define OROT 4
#define XBS 128
#define BAR_B2 0
#define DLY_B2 13
#define DLY_CD2 7
#define RS_CD 0
#define RS_B 0
#define WD2 5
#define WD3 5
#define WD 5
#define CODE_KB 16

constexpr int SK = 16384, WIN = 4096;
constexpr float EPS = 1e-5f;
constexpr float SCL = 0.08838834764831845f * 1.4426950408889634f;
constexpr float GK0 = -2.0f * 0.7978845608028654f * 1.4426950408889634f;
constexpr float GK1 = GK0 * 0.044715f;

constexpr int WS_EP = 0;
constexpr int WS_QB = 4096;
constexpr int WS_CS = WS_QB + 2 * 4 * 8 * 2 * 2048;
constexpr int WS_OB = WS_CS + 4 * 8 * 2 * 2048;
constexpr int WS_OS = WS_OB + 2 * 4 * 8 * 8 * 2048;
constexpr int WS_PB = WS_OS + 2 * 4 * 8 * 8 * 128;
constexpr int WS_XB = WS_PB + 2 * 4 * 8 * 2048;
constexpr int WS_END = WS_XB + 4 * 64 * XBS;
constexpr int WS_STAMP = 4 << 20;

constexpr int L_KV = 0;
constexpr int L_PAR = 131072;
constexpr int PSTR = 1088;
constexpr int L_PL = L_PAR + 11264;
constexpr int QSTR = 288;
constexpr int L_RED = L_PL + 4 * PSTR;
constexpr int L_ST = L_RED + 5120;
constexpr int L_MB = L_ST + 64;
constexpr int L_LB = L_MB + 128;
constexpr int L_R2 = L_LB + 512;
constexpr int L_QL = L_R2 + 128;
constexpr int L_XL = L_QL + 4 * QSTR;
constexpr int L_SC2 = L_QL;
constexpr int L_STAMP = L_SC2 + 8192;
constexpr int L_END = L_STAMP + (STAMPS ? 1024 + 64 : 0);
static_assert(L_END <= 163840, "LDS");

struct Params {
  const u16* x; const unsigned char* K; const unsigned char* V; const unsigned char* wq; const unsigned char* wo;
  const u16* w1; const u16* b1; const u16* w2; const u16* b2; u16* out; unsigned char* ws;
};

template <int CTRL> DI float dpp(float x) {
  return __builtin_bit_cast(float, __builtin_amdgcn_update_dpp(0, __builtin_bit_cast(int, x), CTRL, 0xf, 0xf, true));
}
constexpr int XOR1 = 0xB1, XOR2 = 0x4E, HMIR = 0x141, MIR = 0x140, ROR4 = 0x124, ROR8 = 0x128;
DI float asf(u32 x) { return __builtin_bit_cast(float, x); }
DI u32 asu(float x) { return __builtin_bit_cast(u32, x); }
DI u32 cvtpk(float lo, float hi) { f2 f = {lo, hi}; return __builtin_bit_cast(u32, __builtin_convertvector(f, bf2v)); }
DI f4 mfma(v4u a, v4u b, f4 c) {
  return __builtin_amdgcn_mfma_f32_16x16x32_bf16(__builtin_bit_cast(s8v, a), __builtin_bit_cast(s8v, b), c, 0, 0, 0);
}
DI float fmx(float a, float b) { return __builtin_elementwise_maximum(a, b); }
DI float xrow_max(float x) {
  auto s = __builtin_amdgcn_permlane16_swap(asu(x), asu(x), false, false);
  x = fmx(asf(s[0]), asf(s[1]));
  auto t = __builtin_amdgcn_permlane32_swap(asu(x), asu(x), false, false);
  return fmx(asf(t[0]), asf(t[1]));
}
DI float quad_reduce4(f4 r, int lane) {
  float t0 = (lane & 1) ? r[1] : r[0], t1 = (lane & 1) ? r[0] : r[1];
  t0 += dpp<XOR1>(t1);
  float t2 = (lane & 1) ? r[3] : r[2], t3 = (lane & 1) ? r[2] : r[3];
  t2 += dpp<XOR1>(t3);
  float s0 = (lane & 2) ? t2 : t0; const float s1 = (lane & 2) ? t0 : t2;
  s0 += dpp<XOR2>(s1);
  return s0;
}
DI void st32s(u32 v, __amdgpu_buffer_rsrc_t rs, int off) { __builtin_amdgcn_raw_buffer_store_b32(v, rs, off, 0, 16); }
template <int N> DI u32 rowbc(u32 x) { return (u32)__builtin_amdgcn_update_dpp(0, (int)x, 0x150 + N, 0xf, 0xf, false); }
DI u32 xo(u32 a, u32 t, u32 acc) { return __builtin_amdgcn_bitop3_b32(a, t, acc, 0xBE); }
#define PIN1(a) asm volatile("" : "+v"(a))
#define PIN2(a, b) asm volatile("" : "+v"(a), "+v"(b))
#define PIN4(a, b, c, d) asm volatile("" : "+v"(a), "+v"(b), "+v"(c), "+v"(d))
#define PIN8(x) asm volatile("" : "+v"(x[0]), "+v"(x[1]), "+v"(x[2]), "+v"(x[3]), "+v"(x[4]), "+v"(x[5]), "+v"(x[6]), "+v"(x[7]))
DI float xrow_sum(float x) {
  auto s = __builtin_amdgcn_permlane16_swap(asu(x), asu(x), false, false);
  x = asf(s[0]) + asf(s[1]);
  auto t = __builtin_amdgcn_permlane32_swap(asu(x), asu(x), false, false);
  return asf(t[0]) + asf(t[1]);
}
#define BAR() asm volatile("s_waitcnt lgkmcnt(0)\n\ts_barrier" ::: "memory")
#define CBAR() asm volatile("" ::: "memory")
#define SB() __builtin_amdgcn_sched_barrier(0)

#define STAMP(i)
#define STAMPR(i)
#define STAMPV(i, v)
#define STAMPW(i)

constexpr u32 TMASK = 0x3ffu;
DI u32 tagf(float v, u32 tag) { return __builtin_amdgcn_bitop3_b32(asu(v) + 0x200u, TMASK, tag, 0xBA); }
DI float untag(u32 v) { return asf(v & ~TMASK); }

DI u32 ld32s(__amdgpu_buffer_rsrc_t rs, int off) { return __builtin_amdgcn_raw_buffer_load_b32(rs, off, 0, 16); }
DI v4u ld128s(__amdgpu_buffer_rsrc_t rs, int off) { return __builtin_amdgcn_raw_buffer_load_b128(rs, off, 0, 16); }
DI void st128s(v4u v, __amdgpu_buffer_rsrc_t rs, int off) { __builtin_amdgcn_raw_buffer_store_b128(v, rs, off, 0, 16); }

DI void ln_stats(float x, LAS unsigned char* L, int w, int lane, float& mean, float& var) {
  const float c = asf(__builtin_amdgcn_readfirstlane(asu(x)));
  const float d = x - c;
  float s1 = d, s2 = d * d;
  s1 += dpp<XOR1>(s1); s2 += dpp<XOR1>(s2);
  s1 += dpp<XOR2>(s1); s2 += dpp<XOR2>(s2);
  s1 += dpp<HMIR>(s1); s2 += dpp<HMIR>(s2);
  s1 += dpp<MIR>(s1); s2 += dpp<MIR>(s2);
  asm volatile("s_nop 1\n\tv_add_f32_dpp %0, %0, %0 row_bcast:15 row_mask:0xa bank_mask:0xf\n\tv_add_f32_dpp %1, %1, %1 row_bcast:15 row_mask:0xa bank_mask:0xf\n\t"
               "s_nop 0\n\tv_add_f32_dpp %0, %0, %0 row_bcast:31 row_mask:0xc bank_mask:0xf\n\tv_add_f32_dpp %1, %1, %1 row_bcast:31 row_mask:0xc bank_mask:0xf"
               : "+v"(s1), "+v"(s2));
  const f2 pv = {fmaf(s1, 1.0f / 64, c), fmaf(s1 * s1, -1.0f / 64, s2)};
  if (lane == 63) *(LAS f2*)(L + L_ST + w * 8) = pv;
  BAR();
  const f2 v = *(const LAS f2*)(L + L_ST + (lane & 7) * 8);
  float m = v.x;
  m += dpp<XOR1>(m); m += dpp<XOR2>(m); m += dpp<HMIR>(m);
  mean = m * 0.125f;
  const float dv = v.x - mean;
  float t = fmaf(64.0f * dv, dv, v.y);
  t += dpp<XOR1>(t); t += dpp<XOR2>(t); t += dpp<HMIR>(t);
  var = t * (1.0f / 512);
}

DI void transpose8(const v4u (&raw)[8], v4u (&fr)[8]) {
#pragma unroll
  for (int i = 0; i < 8; ++i) {
    const u32 sel = (i & 1) ? 0x07060302u : 0x05040100u;
#pragma unroll
    for (int m = 0; m < 4; ++m) fr[i][m] = __builtin_amdgcn_perm(raw[2 * m + 1][i >> 1], raw[2 * m][i >> 1], sel);
  }
}

DI v4u q_operand(LAS unsigned char* L, int xaddr, float mean, float var) {
  const f4 a = *(const LAS f4*)(L + xaddr), b = *(const LAS f4*)(L + xaddr + 16);
  const float rs_ = __builtin_amdgcn_rsqf(var + EPS), mr = -mean * rs_;
  return v4u{cvtpk(fmaf(a[0], rs_, mr), fmaf(a[1], rs_, mr)), cvtpk(fmaf(a[2], rs_, mr), fmaf(a[3], rs_, mr)),
             cvtpk(fmaf(b[0], rs_, mr), fmaf(b[1], rs_, mr)), cvtpk(fmaf(b[2], rs_, mr), fmaf(b[3], rs_, mr))};
}

DI void q_gemv1(const v4u (&wf)[8], v4u a, LAS unsigned char* L, int lane, int wraddr) {
  f4 d[8];
#pragma unroll
  for (int i = 0; i < 8; ++i) d[i] = mfma(a, wf[i], f4{0.f, 0.f, 0.f, 0.f});
  f4 v;
#pragma unroll
  for (int m = 0; m < 4; ++m) v[m] = (lane & 16) ? d[4 + m][0] : d[m][0];
  if (lane < 32) *(LAS f4*)(L + wraddr) = v;
}
template <int STR>
DI void q_gemv2(LAS unsigned char* L, int lane, int rdaddr, __amdgpu_buffer_rsrc_t rs, int qoff, u32 tag) {
  const f4 r = *(const LAS f4*)(L + rdaddr) + *(const LAS f4*)(L + rdaddr + 4 * STR);
  st32s(tagf(quad_reduce4(r, lane), tag), rs, qoff);
}
DI void q_gemv(const v4u (&wf)[8], v4u a, LAS unsigned char* L, int tid, int lane, int wraddr, int rdaddr, __amdgpu_buffer_rsrc_t rs, int qoff, u32 tag) {
  q_gemv1(wf, a, L, lane, wraddr);
  BAR();
  if (tid < 128) q_gemv2<576>(L, lane, rdaddr, rs, qoff, tag);
}

DI float attn_scores(float qv, const v4u (&kf)[16], LAS unsigned char* L, int qwr, int qrd, int mwr, int mrd, int pwr, int lwr, int lane) {
  *(LAS u16*)(L + qwr) = (u16)(cvtpk(qv, qv) & 0xffffu);
  BAR();
  v4u qf[4];
#pragma unroll
  for (int st = 0; st < 4; ++st) qf[st] = *(const LAS v4u*)(L + qrd + st * 64);
  SB();
  f4 d[4];
#pragma unroll
  for (int st = 0; st < 4; ++st)
#pragma unroll
    for (int t = 0; t < 4; ++t) d[t] = mfma(kf[t * 4 + st], qf[st], st == 0 ? f4{0.f, 0.f, 0.f, 0.f} : d[t]);
  float sc[4];
#pragma unroll
  for (int i = 0; i < 4; ++i) {
    const float a = (lane & 4) ? d[1][i] : d[0][i], bq = (lane & 4) ? d[3][i] : d[2][i];
    sc[i] = (lane & 8) ? bq : a;
  }
  float m = fmx(fmx(sc[0], sc[1]), fmx(sc[2], sc[3]));
  m = fmx(m, dpp<ROR4>(m)); m = fmx(m, dpp<ROR8>(m));
  m = xrow_max(m);
  if (lane < 4) *(LAS float*)(L + mwr) = m;
  BAR();
  const f4 m0 = *(const LAS f4*)(L + mrd), m1 = *(const LAS f4*)(L + mrd + 16);
  float M = fmx(fmx(fmx(m0[0], m0[1]), fmx(m0[2], m0[3])), fmx(fmx(m1[0], m1[1]), fmx(m1[2], m1[3])));
  M = asf(asu(M * SCL) & ~TMASK);
  float p[4];
#pragma unroll
  for (int i = 0; i < 4; ++i) p[i] = __builtin_amdgcn_exp2f(fmaf(sc[i], SCL, -M));
  v2u pk = {cvtpk(p[0], p[1]), cvtpk(p[2], p[3])};
  *(LAS v2u*)(L + pwr) = pk;
  float l = (p[0] + p[1]) + (p[2] + p[3]);
  l += dpp<ROR4>(l); l += dpp<ROR8>(l);
  if ((lane & 12) == 0) *(LAS float*)(L + lwr) = l;
  return M;
}

DI v4u v_frag(int t, int u, LAS unsigned char* L, int vbase) {
  const s4v lo = __builtin_amdgcn_ds_read_tr16_b64_v4i16((LAS s4v*)(L + vbase + u * 32768 + t * 4096));
  const s4v hi = __builtin_amdgcn_ds_read_tr16_b64_v4i16((LAS s4v*)(L + vbase + u * 32768 + t * 4096 + 1024));
  const v2u l2 = __builtin_bit_cast(v2u, lo), h2 = __builtin_bit_cast(v2u, hi);
  return v4u{l2[0], l2[1], h2[0], h2[1]};
}
DI void pv_chunk(int t, f4 (&acc)[4], LAS unsigned char* L, int vbase, int lane) {
  const v4u pf = *(const LAS v4u*)(L + L_PL + t * PSTR + lane * 16);
#pragma unroll
  for (int u = 0; u < 4; ++u) acc[u] = mfma(pf, v_frag(t, u, L, vbase), acc[u]);
}

DI void attn_publish_stats(float M, LAS unsigned char* L, int w, int lane, __amdgpu_buffer_rsrc_t rs, int saddr, u32 tag, u32 T0) {
  if (w == 7) {
    float l = *(const LAS float*)(L + L_LB + lane * 4) + *(const LAS float*)(L + L_LB + (lane + 64) * 4);
    l += dpp<ROR4>(l); l += dpp<ROR8>(l);
    l = xrow_sum(l);
    if (lane < 8) { v4u sv = {asu(M) | tag, tagf(l, tag), T0, T0}; st128s(sv, rs, saddr); }
  }
}
template <int RM> DI float rowput(float old, float x) {
  return __builtin_bit_cast(float, __builtin_amdgcn_update_dpp(__builtin_bit_cast(int, old), __builtin_bit_cast(int, x), 0xE4, RM, 0xf, false));
}
DI void attn_publish(const f4 (&acc)[4], int lane, __amdgpu_buffer_rsrc_t rs, int oaddr, u32 T0) {
  float y[4];
#pragma unroll
  for (int h = 0; h < 4; ++h) y[h] = rowput<0x8>(rowput<0x4>(rowput<0x2>(acc[0][h], acc[1][h]), acc[2][h]), acc[3][h]);
  auto s01 = __builtin_amdgcn_permlane16_swap(asu(y[0]), asu(y[1]), false, false);
  auto s23 = __builtin_amdgcn_permlane16_swap(asu(y[2]), asu(y[3]), false, false);
  const float z01 = asf(s01[0]) + asf(s01[1]), z23 = asf(s23[0]) + asf(s23[1]);
  auto sf = __builtin_amdgcn_permlane32_swap(asu(z01), asu(z23), false, false);
  const float v = asf(sf[0]) + asf(sf[1]);
  st32s(__builtin_amdgcn_perm(cvtpk(v, v), T0, 0x05040100u), rs, oaddr);
}

DI bool o_check(const v4u& x0, const v4u& x1, const v4u& sv, u32 T0) {
  u32 d = xo(x0[0], T0, 0u);
  d = xo(x0[1], T0, d); d = xo(x0[2], T0, d); d = xo(x0[3], T0, d);
  d = xo(x1[0], T0, d); d = xo(x1[1], T0, d); d = xo(x1[2], T0, d); d = xo(x1[3], T0, d);
  d = xo(sv[0], T0, d); d = xo(sv[1], T0, d);
  d = xo(sv[3], T0, xo(sv[2], T0, d & TMASK));
  return __all(d == 0);
}
template <bool FIRST, class Hook, class Hook2>
DI void o_gemv(Hook&& hook, Hook2&& hook2, v4u (&wf)[8], v4u (&wraw)[8], LAS unsigned char* L, int tid, int lane, __amdgpu_buffer_rsrc_t rs, int oo, int so, int xw, int xr, int wraddr, int rdaddr, int poff, u32 tag, u32 T0, int& budget, int w, int sb) {
  v4u x0, x1, sv = {T0, T0, T0, T0};
  if (sb == 28) STAMP(39);
  CBAR();
  x0 = ld128s(rs, oo); x1 = ld128s(rs, oo + 256);
  if (lane < 16) sv = ld128s(rs, so);
  if (FIRST) {
    asm volatile("" : "+v"(wraw[0]), "+v"(wraw[1]), "+v"(wraw[2]), "+v"(wraw[3]), "+v"(wraw[4]), "+v"(wraw[5]), "+v"(wraw[6]), "+v"(wraw[7])::"memory");
    STAMP(9); STAMPW(80);
    transpose8(wraw, wf);
    PIN8(wf);
    hook();
  }
  while (!o_check(x0, x1, sv, T0) && --budget > 0) {
    if (RS_B) __builtin_amdgcn_s_sleep(RS_B);
    CBAR();
    x0 = ld128s(rs, oo); x1 = ld128s(rs, oo + 256);
    if (lane < 16) sv = ld128s(rs, so);
  }
  STAMP(sb); STAMPV(sb + 1, budget); if (sb == 28) STAMPW(104);
  const v4u pk = {__builtin_amdgcn_perm(x0[1], x0[0], 0x07060302u), __builtin_amdgcn_perm(x0[3], x0[2], 0x07060302u),
                  __builtin_amdgcn_perm(x1[1], x1[0], 0x07060302u), __builtin_amdgcn_perm(x1[3], x1[2], 0x07060302u)};
  *(LAS v4u*)(L + xw) = pk;
  CBAR();
  const v4u a = *(const LAS v4u*)(L + xr);
  const float ms = untag(sv[0]), ls = untag(sv[1]);
  float M = ms;
  M = fmx(M, dpp<XOR1>(M)); M = fmx(M, dpp<XOR2>(M)); M = fmx(M, dpp<HMIR>(M));
  const float f = __builtin_amdgcn_exp2f(ms - M);
  float Ls = f * ls;
  Ls += dpp<XOR1>(Ls); Ls += dpp<XOR2>(Ls); Ls += dpp<HMIR>(Ls);
  const float cs = f * __builtin_amdgcn_rcpf(Ls);
  f4 d[8];
#pragma unroll
  for (int i = 0; i < 8; ++i) d[i] = mfma(a, wf[i], f4{0.f, 0.f, 0.f, 0.f});
  const int bp = (lane >> 4) * 16;
  const float cr0 = asf(__builtin_amdgcn_ds_bpermute(bp, asu(cs))), cr1 = asf(__builtin_amdgcn_ds_bpermute(bp + 4, asu(cs)));
  const float cr2 = asf(__builtin_amdgcn_ds_bpermute(bp + 8, asu(cs))), cr3 = asf(__builtin_amdgcn_ds_bpermute(bp + 12, asu(cs)));
  const f2 c01 = {cr0, cr1}, c23 = {cr2, cr3};
  float e[8];
#pragma unroll
  for (int i = 0; i < 8; ++i) { const f2 t = f2{d[i][2], d[i][3]} * c23 + f2{d[i][0], d[i][1]} * c01; e[i] = t[0] + t[1]; }
  f4 v;
#pragma unroll
  for (int m = 0; m < 4; ++m) { auto t = __builtin_amdgcn_permlane16_swap(asu(e[m]), asu(e[m + 4]), false, false); v[m] = asf(t[0]) + asf(t[1]); }
  if ((lane >> 5) == ((lane >> 3) & 1)) *(LAS f4*)(L + wraddr) = v;
  BAR();
  if (tid < 64) {
    const f4 r = (*(const LAS f4*)(L + rdaddr) + *(const LAS f4*)(L + rdaddr + 4 * 320)) + (*(const LAS f4*)(L + rdaddr + 8 * 320) + *(const LAS f4*)(L + rdaddr + 12 * 320));
    st32s(tagf(quad_reduce4(r, lane), tag), rs, poff);
  }
  hook2();
}

template <int DLY, class Hook>
DI f4 mixer(Hook&& hook, float& xres, LAS unsigned char* L, int tid, int w, int lane, int j, __amdgpu_buffer_rsrc_t rs, int pbase, u32 tag, float b2, int& budget, int sb, float& mean_out) {
  float pw1[7], pb1[7], pw2[7];
  const int fl = tid & 255, q = tid >> 8;
#pragma unroll
  for (int i = 0; i < 7; ++i) {
    pw1[i] = asf((u32) * (const LAS u16*)(L + L_PAR + (fl + 256 * i) * 2) << 16);
    pb1[i] = asf((u32) * (const LAS u16*)(L + L_PAR + 3584 + (fl + 256 * i) * 2) << 16);
    pw2[i] = asf((u32) * (const LAS u16*)(L + L_PAR + 7168 + (fl + 256 * i) * 2) << 16);
  }
#pragma unroll
  for (int i = 0; i < 7; ++i) asm volatile("" : "+v"(pw1[i]), "+v"(pb1[i]), "+v"(pw2[i]));
  int po = pbase + tid * 4, xa = L_XL + (8 * j + 4 * q) * 4, xw = L_XL + tid * 4;
  asm volatile("" : "+v"(po), "+v"(xa), "+v"(xw));
  if (DLY) __builtin_amdgcn_s_sleep(DLY);
  if (sb == 30) STAMP(35);
  u32 p[8];
  for (;;) {
    CBAR();
#pragma unroll
    for (int i = 0; i < 8; ++i) p[i] = ld32s(rs, po + i * 2048) ^ tag;
    const u32 d = (p[0] | p[1] | p[2]) | (p[3] | p[4] | p[5]) | (p[6] | p[7]);
    if (__all((d & TMASK) == 0) || --budget <= 0) break;
    if (RS_CD) __builtin_amdgcn_s_sleep(RS_CD);
  }
  STAMP(sb); STAMPV(sb + 1, budget); if (sb == 30) STAMPW(112);
  hook();
  const float x1 = xres + (((asf(p[0]) + asf(p[1])) + (asf(p[2]) + asf(p[3]))) + ((asf(p[4]) + asf(p[5])) + (asf(p[6]) + asf(p[7]))));
  xres = x1;
  *(LAS float*)(L + xw) = x1;
  float mean, var;
  ln_stats(x1, L, w, lane, mean, var);
  mean_out = mean;
  const f4 xv = *(const LAS f4*)(L + xa);
  const float r1 = __builtin_amdgcn_rsqf(var + EPS);
  const float r2 = __builtin_amdgcn_rsqf(var * r1 * r1 + EPS);
  const float rr = r1 * r2, mr = -mean * rr;
  STAMP(sb + 2); if (sb == 22) STAMPW(88);
  const f2 hA = {fmaf(xv[0], rr, mr), fmaf(xv[1], rr, mr)}, hB = {fmaf(xv[2], rr, mr), fmaf(xv[3], rr, mr)};
  f2 aA = {0.f, 0.f}, aB = {0.f, 0.f};
#pragma unroll
  for (int i = 0; i < 7; ++i) {
    const f2 w1 = {pw1[i], pw1[i]}, b1 = {pb1[i], pb1[i]}, w2 = {pw2[i], pw2[i]};
    const f2 k0 = {GK0, GK0}, k1 = {GK1, GK1}, one = {1.f, 1.f};
    {
      const f2 u = hA * w1 + b1, z = u * (u * u * k1 + k0);
      const f2 e = {__builtin_amdgcn_exp2f(z[0]), __builtin_amdgcn_exp2f(z[1])}, dd = e + one;
      const f2 rc = {__builtin_amdgcn_rcpf(dd[0]), __builtin_amdgcn_rcpf(dd[1])};
      aA += (u * rc) * w2;
    }
    {
      const f2 u = hB * w1 + b1, z = u * (u * u * k1 + k0);
      const f2 e = {__builtin_amdgcn_exp2f(z[0]), __builtin_amdgcn_exp2f(z[1])}, dd = e + one;
      const f2 rc = {__builtin_amdgcn_rcpf(dd[0]), __builtin_amdgcn_rcpf(dd[1])};
      aB += (u * rc) * w2;
    }
  }
  float t0 = (lane & 1) ? aA[1] : aA[0], t1 = (lane & 1) ? aA[0] : aA[1];
  t0 += dpp<XOR1>(t1);
  float t2 = (lane & 1) ? aB[1] : aB[0], t3 = (lane & 1) ? aB[0] : aB[1];
  t2 += dpp<XOR1>(t3);
  float s0 = (lane & 2) ? t2 : t0, s1 = (lane & 2) ? t0 : t2;
  s0 += dpp<XOR2>(s1);
  s0 += dpp<ROR4>(s0); s0 += dpp<ROR8>(s0);
  s0 = xrow_sum(s0);
  STAMP(sb + 3); if (sb == 22) STAMPW(96);
  if (lane < 4) *(LAS float*)(L + L_R2 + (w * 4 + lane) * 4) = s0;
  BAR();
  f4 res = {0.f, 0.f, 0.f, 0.f};
  if (w == 0) {
    const int qq = lane & 1;
    const f4 a = (*(const LAS f4*)(L + L_R2 + (qq * 4 + 0) * 16) + *(const LAS f4*)(L + L_R2 + (qq * 4 + 1) * 16)) +
                 (*(const LAS f4*)(L + L_R2 + (qq * 4 + 2) * 16) + *(const LAS f4*)(L + L_R2 + (qq * 4 + 3) * 16));
    const f4 xr = *(const LAS f4*)(L + L_XL + (8 * j + 4 * qq) * 4);
    res = xr + a + f4{b2, b2, b2, b2};
  }
  return res;
}

__global__ void __launch_bounds__(512, 2) fused_decode_kernel(Params p) {
  __shared__ __attribute__((aligned(1024))) unsigned char smem[L_END];
  LAS unsigned char* L = (LAS unsigned char*)smem;
  const int tid = threadIdx.x, lane = tid & 63, w = __builtin_amdgcn_readfirstlane(tid >> 6);
  const int bid = blockIdx.x;
  const int kvh = bid & 7, b = bid >> 6, s = (bid >> 3) & 7;
  const int j = kvh * 8 + s;
  const int g = lane >> 4, c = lane & 15;
  STAMPR(36); STAMP(0);

  const size_t slab = ((size_t)(b * 8 + kvh) * SK + (SK - WIN) + s * 512) * 256;
  const __amdgpu_buffer_rsrc_t rK = __builtin_amdgcn_make_buffer_rsrc((void*)(p.K + slab), 0, 512 * 256, 0x00020000);
  const __amdgpu_buffer_rsrc_t rV = __builtin_amdgcn_make_buffer_rsrc((void*)(p.V + slab), 0, 512 * 256, 0x00020000);
  const __amdgpu_buffer_rsrc_t rQ = __builtin_amdgcn_make_buffer_rsrc((void*)p.wq, 0, 512 * 4096 * 2, 0x00020000);
  const __amdgpu_buffer_rsrc_t rO = __builtin_amdgcn_make_buffer_rsrc((void*)p.wo, 0, 512 * 4096 * 2, 0x00020000);
  const __amdgpu_buffer_rsrc_t rs = __builtin_amdgcn_make_buffer_rsrc((void*)p.ws, 0, WS_END, 0x00020000);
  const __amdgpu_buffer_rsrc_t rX = __builtin_amdgcn_make_buffer_rsrc((void*)p.x, 0, 4096, 0x00020000);
  const u32 ldsb = (u32)(size_t)L;

  v4u dummy = {0u, 0u, 0u, 0u};
  u32 ev, xraw, b2raw, dummy2 = 0;
  asm volatile("s_nop 4\n\tbuffer_load_dword %0, %1, %2, 0 offen sc1" : "=v"(ev) : "v"(WS_EP + b * 256), "s"(rs) : "memory");
  asm volatile("buffer_load_ushort %0, %1, %2, 0 offen" : "=v"(xraw) : "v"((b * 512 + tid) * 2), "s"(rX) : "memory");
  asm volatile("global_load_ushort %0, %1, %2" : "=v"(b2raw) : "v"(0), "s"(p.b2) : "memory");
  const int rot = __builtin_amdgcn_readfirstlane((int)(((u32)(bid * 8 + w) * 2654435761u) >> 28));
  const int cg = s >> 1, kh = s & 1;
  const int voq = ((256 * kh + 32 * w + 8 * g) * 4096 + kvh * 512 + 128 * cg + 8 * c) * 2;
  const int so_ = (s + OROT) & 7;
  const int voo = ((kvh * 512 + (2 * (w >> 2) + (c >> 3)) * 128 + 32 * (w & 3) + 4 * g) * 512 + 64 * so_ + 8 * (c & 7)) * 2;
  int vok[4], vov[2];
#pragma unroll
  for (int i = 0; i < 4; ++i) vok[i] = (64 * w + g) * 256 + ((c ^ (4 * i + g)) * 16);
#pragma unroll
  for (int i = 0; i < 2; ++i) vov[i] = (64 * w + g) * 256 + ((((c >> 1) ^ (g | (i << 2))) * 32) + (c & 1) * 16);
  v4u wqr[8], wor[8], kf[16];
#define LDQ(jj) asm volatile("buffer_load_dwordx4 %0, %1, %2, %3 offen" WPOL : "=v"(wqr[jj]) : "v"(voq), "s"(rQ), "s"((jj) * 8192) : "memory")
#define LDO(jj) asm volatile("buffer_load_dwordx4 %0, %1, %2, %3 offen" WPOL : "=v"(wor[jj]) : "v"(voo), "s"(rO), "s"((16 * ((jj) >> 2) + ((jj) & 3)) * 1024) : "memory")
#define LDK(i) asm volatile("s_mov_b32 m0, %0\n\ts_nop 0\n\tbuffer_load_dwordx4 %1, %2, %3 offen" KVPOL " lds" ::"s"(ldsb + L_KV + w * 16384 + (i) * 1024), "v"(vok[(i) & 3]), "s"(rK), "s"((((i) + rot) & 15) * 1024) : "memory")
#define LDV(i) asm volatile("s_mov_b32 m0, %0\n\ts_nop 0\n\tbuffer_load_dwordx4 %1, %2, %3 offen" KVPOL " lds" ::"s"(ldsb + L_KV + w * 16384 + (i) * 1024), "v"(vov[((i) >> 1) & 1]), "s"(rV), "s"((((i) + rot) & 15) * 1024) : "memory")
#define GW(d) asm volatile("s_waitcnt vmcnt(%0)" ::"n"((d) - 1) : "memory")
#define G() GW(GWIN)
#define GWIN WD
#define EV(n, i) asm volatile("s_waitcnt vmcnt(%0)" ::"n"((n) - (i) - 1) : "memory")
#define KFRAG(t)                                                                                                     \
  _Pragma("unroll") for (int st = 0; st < 4; ++st)                                                                   \
      kf[(t) * 4 + st] = *(const LAS v4u*)(L + L_KV + w * 16384 + (16 * (t) + c) * 256 + (((4 * st + g) ^ c) * 16)); \
  asm volatile("" : "+v"(kf[(t) * 4]), "+v"(kf[(t) * 4 + 1]), "+v"(kf[(t) * 4 + 2]), "+v"(kf[(t) * 4 + 3])::"memory")
  LDQ(0); LDK(0); LDQ(1); LDK(1); LDQ(2); LDK(2);
  STAMP(38);
  {
    const int piece = (bid >> 3) * 8 + w;
    if (piece < CODE_KB) {
      u64 pc; asm volatile("s_getpc_b64 %0" : "=s"(pc));
      const u64 a = (pc & ~(u64)1023) + (u64)piece * 1024 + lane * 16;
      asm volatile("global_load_dwordx4 %0, %1, off" : "+v"(dummy) : "v"(a) : "memory");
    }
  }
  if (w == 7) {
    const int me = (b * 8 + kvh) * 8 + s;
    int a_ = lane < 16 ? WS_OB + me * 2048 + lane * 128
           : lane < 32 ? WS_OB + (1 << 19) + me * 2048 + (lane - 16) * 128
           : lane < 34 ? WS_OS + (lane - 32) * 32768 + me * 128
           : lane < 42 ? WS_QB + ((lane - 34) >> 2) * (4 * 8 * 4096) + ((b * 8 + kvh) * 2 + (s & 1)) * 2048 + (s >> 1) * 512 + ((lane - 34) & 3) * 128
           : lane < 46 ? WS_PB + ((lane - 42) >> 1) * 65536 + (b * 8 + kvh) * 2048 + ((s + OROT) & 7) * 256 + ((lane - 42) & 1) * 128
           : WS_XB + (b * 64 + j) * XBS;
    asm volatile("buffer_load_dword %0, %1, %2, 0 offen" : "+v"(dummy2) : "v"(a_), "s"(rs) : "memory");
  }
  G(); LDQ(3); G(); LDK(3);
  asm volatile("s_waitcnt vmcnt(8)" : "+v"(ev), "+v"(xraw), "+v"(b2raw)::"memory");
  const u32 T0 = __builtin_amdgcn_readfirstlane(ev) + 1u;
  const u32 tag = T0 & TMASK;
  float xres = asf(xraw << 16);
  const int xqa = L_XL + (256 * kh + 32 * w + 8 * g) * 4;
  v4u qa;
  {
    *(LAS float*)(L + L_XL + tid * 4) = xres;
    float mean, var;
    ln_stats(xres, L, w, lane, mean, var);
    qa = q_operand(L, xqa, mean, var);
    PIN1(qa);
  }
  G(); LDQ(4); G(); LDK(4); G(); LDQ(5); G(); LDK(5);
  G(); LDQ(6); G(); LDK(6); G(); LDQ(7); G(); LDK(7);
  EV(16, 7); STAMPW(48); KFRAG(0);
  G(); LDK(8); G(); LDK(9); G(); LDK(10); G(); LDK(11); G(); LDK(12);
  EV(21, 14);
  asm volatile("" : "+v"(wqr[0]), "+v"(wqr[1]), "+v"(wqr[2]), "+v"(wqr[3]), "+v"(wqr[4]), "+v"(wqr[5]), "+v"(wqr[6]), "+v"(wqr[7]), "+v"(dummy), "+v"(dummy2)::"memory");
  STAMP(1); STAMPW(40);
  LDK(13); LDK(14); LDK(15);
  {
#pragma unroll
    for (int k = 0; k < 2; ++k) {
      const int piece = k == 0 ? w : 8 + (w % 3);
      int gr = piece * 64 + lane; gr = gr < 672 ? gr : 671;
      const int arr = gr / 224, e = gr - arr * 224;
      const unsigned char* src = (const unsigned char*)(arr == 0 ? p.w1 : arr == 1 ? p.b1 : p.w2) + e * 16;
      asm volatile("s_mov_b32 m0, %0\n\ts_nop 0\n\tglobal_load_lds_dwordx4 %1, off" ::"s"(ldsb + L_PAR + piece * 1024), "v"(src) : "memory");
    }
  }
  LDV(0); LDV(1);
  v4u wqf[8];
  transpose8(wqr, wqf);
  const int qwr = L_RED + w * 576 + (8 * c + 4 * (g & 1)) * 4, qrd = L_RED + (tid & 3) * 576 + (tid >> 2) * 16;
  const int qpub = WS_QB + ((b * 8 + kvh) * 2 + kh) * 2048 + cg * 512 + tid * 4;
  q_gemv(wqf, qa, L, tid, lane, qwr, qrd, rs, qpub, tag);
  STAMP(2);
#undef GWIN
#define GWIN WD2
  EV(28, 15); KFRAG(1);
  G(); LDV(2); G(); LDV(3);
  EV(30, 19); KFRAG(2);
#define WAITN(n) asm volatile("s_waitcnt vmcnt(%0)" ::"n"(n) : "memory")
  G(); LDO(0); G(); LDV(4); G(); LDV(5);
  STAMP(3);
  u32 q0, q1;
  const int qo = WS_QB + (b * 8 + kvh) * 4096 + tid * 4;
  G(); asm volatile("buffer_load_dword %0, %1, %2, 0 offen sc1" : "=v"(q0) : "v"(qo), "s"(rs) : "memory");
  asm volatile("buffer_load_dword %0, %1, %2, 0 offen offset:2048 sc1" : "=v"(q1) : "v"(qo), "s"(rs) : "memory");
  WAITN(11); KFRAG(3);
  STAMP(4); STAMPW(56);
  G(); LDO(1); G(); LDV(6); G(); LDV(7); G(); LDO(2); G(); LDV(8); G(); LDV(9);
  WAITN(6);
  asm volatile("" : "+v"(q0), "+v"(q1)::"memory");
  STAMP(5); STAMPW(64);
  LDO(3); LDV(10); LDV(11); LDO(4); LDV(12); LDV(13);
  int budget = 100000;
  if (!__all((((q0 ^ tag) | (q1 ^ tag)) & TMASK) == 0)) {
    _Pragma("nounroll") do {
      asm volatile("buffer_load_dword %0, %2, %3, 0 offen sc1\n\tbuffer_load_dword %1, %2, %3, 0 offen offset:2048 sc1\n\ts_waitcnt vmcnt(0)" : "=&v"(q0), "=&v"(q1) : "v"(qo), "s"(rs) : "memory");
    } while (!__all((((q0 ^ tag) | (q1 ^ tag)) & TMASK) == 0) && --budget > 0);
  }
  STAMPV(19, budget);
  const int vbase = L_KV + (g >> 1) * 16384 + (8 * (g & 1) + (c >> 2)) * 256 + ((w ^ ((c >> 2) | ((g & 1) << 2))) * 32) + (c & 3) * 8;
  const int a_qwr = L_QL + (tid >> 7) * QSTR + (tid & 127) * 2, a_qrd = L_QL + (c & 3) * QSTR + g * 16;
  const int a_mwr = L_MB + ((lane & 3) * 8 + w) * 4, a_mrd = L_MB + (c & 3) * 32;
  const int a_pwr = L_PL + (c >> 2) * PSTR + ((2 * (w & 1) + (g >> 1)) * 16 + (w >> 1) * 4 + (c & 3)) * 16 + (g & 1) * 8;
  const int a_lwr = L_LB + (w * 16 + g * 4 + (c & 3)) * 4;
  f4 acc[4];
  float M = attn_scores(asf(q0 ^ tag) + asf(q1 ^ tag), kf, L, a_qwr, a_qrd, a_mwr, a_mrd, a_pwr, a_lwr, lane);
  STAMP(6);
#undef GWIN
#define GWIN WD3
  G(); LDO(5); G(); LDO(6); G(); LDO(7); G(); LDV(14); G(); LDV(15);
  WAITN(8);
  BAR();
  const int pub_o = ((b * 8 + kvh) * 8 + s) * 2048 + w * 256 + lane * 4, pub_s = ((b * 8 + kvh) * 8 + s) * 128 + (lane & 7) * 16;
  attn_publish_stats(M, L, w, lane, rs, WS_OS + pub_s, tag, T0);
#pragma unroll
  for (int u = 0; u < 4; ++u) acc[u] = f4{0.f, 0.f, 0.f, 0.f};
  pv_chunk(0, acc, L, vbase, lane);
  pv_chunk(1, acc, L, vbase, lane);
  pv_chunk(2, acc, L, vbase, lane);
  v4u pf3 = *(const LAS v4u*)(L + L_PL + 3 * PSTR + lane * 16);
  PIN1(pf3);
  SB();
  WAITN(0);
  STAMP(7); STAMPW(72);
  BAR();
  {
    v4u vf3[4];
#pragma unroll
    for (int u = 0; u < 4; ++u) vf3[u] = v_frag(3, u, L, vbase);
    SB();
#pragma unroll
    for (int u = 0; u < 4; ++u) acc[u] = mfma(pf3, vf3[u], acc[u]);
  }
  int po1 = WS_OB + pub_o;
  attn_publish(acc, lane, rs, po1, T0);
  STAMP(8);
  v4u wof[8];
  const float b2 = asf(b2raw << 16);
  int g_oo = (b * 8 + kvh) * 8 * 2048 + (lane >> 3) * 2048 + 2 * (w & 3) * 256 + (w >> 2) * 128 + (lane & 7) * 16;
  int g_so = (b * 8 + kvh) * 8 * 128 + (lane & 7) * 128 + (2 * (w >> 2) + ((lane >> 3) & 1)) * 16;
  int o_xw = L_SC2 + w * 1024 + (lane ^ (lane >> 4)) * 16, o_xr = L_SC2 + w * 1024 + (((c & 7) * 8 + 4 * (c >> 3) + g) ^ ((c & 7) >> 1)) * 16;
  int o_wr = L_RED + (2 * w + (g >> 1)) * 320 + (8 * (c & 7) + 4 * (g & 1)) * 4, o_rd = L_RED + (tid & 3) * 320 + (tid >> 2) * 16;
  int o_pub = WS_PB + (b * 8 + kvh) * 2048 + so_ * 256 + tid * 4;
  g_oo += WS_OB; g_so += WS_OS;
  PIN4(g_oo, g_so, o_wr, o_rd); PIN1(o_pub); PIN2(o_xw, o_xr);
  BAR();

  if (DLY_B) __builtin_amdgcn_s_sleep(DLY_B);
  o_gemv<true>([&]() { q_gemv1(wqf, v4u{0x3f803f80u, 0x3f803f80u, 0x3f803f80u, 0x3f803f80u}, L, lane, L_PL + w * 544 + (8 * c + 4 * (g & 1)) * 4); },
               [&]() { if (w == 2 || w == 3) q_gemv2<544>(L, lane, L_PL + (lane & 3) * 544 + ((w - 2) * 16 + (lane >> 2)) * 16, rs, qpub - WS_QB + WS_CS - 128 * 4, tag); }, wof, wor, L, tid, lane, rs, g_oo, g_so, o_xw, o_xr, o_wr, o_rd, o_pub, tag, T0, budget, w, 20);
  STAMP(10);
  u32 cs0, cs1;
  const int cso = WS_CS + (b * 8 + kvh) * 4096 + tid * 4;
  float mprev, csum;
  {
    const f4 r = mixer<DLY_CD>([&]() { cs0 = ld32s(rs, cso); cs1 = ld32s(rs, cso + 2048); }, xres, L, tid, w, lane, j, rs, WS_PB + b * 8 * 2048, tag, b2, budget, 22, mprev);
    cs0 ^= tag; cs1 ^= tag;
    if (!__all(((cs0 | cs1) & TMASK) == 0)) {
      do { CBAR(); cs0 = ld32s(rs, cso) ^ tag; cs1 = ld32s(rs, cso + 2048) ^ tag; } while (!__all(((cs0 | cs1) & TMASK) == 0) && --budget > 0);
    }
    csum = asf(cs0) + asf(cs1);
    PIN1(csum);
    if (tid < XBS / 16) { v4u o = {tagf(r[0], tag), tagf(r[1], tag), tagf(r[2], tag), tagf(r[3], tag)}; st128s(o, rs, WS_XB + (b * 64 + j) * XBS + tid * 16); }
  }
  STAMP(11);
  float rstd2, dmean;
  int qo2 = qo + 4 * 8 * 4096, b0 = a_qwr, b1 = a_qrd, b2_ = a_mwr, b3 = a_mrd, b4 = a_pwr, b5 = a_lwr, pr = L_PL + lane * 16, po2 = WS_OB + (1 << 19) + pub_o, ps2 = WS_OS + 32768 + pub_s;
  PIN4(qo2, b0, b1, b2_); PIN4(b3, b4, b5, pr); PIN2(po2, ps2);
  {
    int xo_ = WS_XB + (b * 64 + (tid >> 3)) * XBS + (tid & 7) * 4, ao = WS_XB + (b * 64 + 32 * kh + 4 * w + g) * XBS + (c & 1) * 16, qw = qwr, qr = qrd, qp = qpub + 4 * 8 * 4096;
    PIN4(xo_, ao, qw, qr); PIN1(qp);
    if (DLY_E) __builtin_amdgcn_s_sleep(DLY_E);
    v4u a0;
    u32 xv;
    for (;;) {
      CBAR();
      a0 = v4u{tag, tag, tag, tag};
      if (c < 2) a0 = ld128s(rs, ao);
      xv = ld32s(rs, xo_) ^ tag;
#pragma unroll
      for (int i = 0; i < 4; ++i) a0[i] ^= tag;
      if (__all((((a0[0] | a0[1] | a0[2]) | (a0[3] | xv)) & TMASK) == 0) || --budget <= 0) break;
    }
    STAMP(26); STAMPV(27, budget); STAMPW(120);
    const u32 p0 = cvtpk(asf(a0[0]) - mprev, asf(a0[1]) - mprev), p1 = cvtpk(asf(a0[2]) - mprev, asf(a0[3]) - mprev);
    const v4u qa2 = {rowbc<0>(p0), rowbc<0>(p1), rowbc<1>(p0), rowbc<1>(p1)};
    STAMP(12);
    q_gemv(wqf, qa2, L, tid, lane, qw, qr, rs, qp, tag);
    STAMP(13);
    if (DLY_A) __builtin_amdgcn_s_sleep(DLY_A);
    CBAR();
    q0 = ld32s(rs, qo2); q1 = ld32s(rs, qo2 + 2048);
    CBAR();
    xres = asf(xv);
    float mean, var;
    ln_stats(xres, L, w, lane, mean, var);
    rstd2 = __builtin_amdgcn_rsqf(var + EPS);
    dmean = (mean - mprev) * csum;
    PIN2(rstd2, dmean);
  }
  {
    v4u vf[16];
#pragma unroll
    for (int t = 0; t < 4; ++t)
#pragma unroll
      for (int u = 0; u < 4; ++u) vf[t * 4 + u] = v_frag(t, u, L, vbase);
    PIN8(vf); { v4u* v2 = vf + 8; PIN8(v2); }
    q0 ^= tag; q1 ^= tag;
    while (!__all(((q0 | q1) & TMASK) == 0) && --budget > 0) { CBAR(); q0 = ld32s(rs, qo2) ^ tag; q1 = ld32s(rs, qo2 + 2048) ^ tag; }
    STAMP(14); STAMPV(18, budget);
    M = attn_scores(rstd2 * ((asf(q0) + asf(q1)) - dmean), kf, L, b0, b1, b2_, b3, b4, b5, lane);
    BAR();
    v4u pf[4];
#pragma unroll
    for (int t = 0; t < 4; ++t) pf[t] = *(const LAS v4u*)(L + pr + t * PSTR);
    SB();
#pragma unroll
    for (int t = 0; t < 4; ++t)
#pragma unroll
      for (int u = 0; u < 4; ++u) acc[u] = mfma(pf[t], vf[t * 4 + u], t == 0 ? f4{0.f, 0.f, 0.f, 0.f} : acc[u]);
    attn_publish_stats(M, L, w, lane, rs, ps2, tag, T0);
    attn_publish(acc, lane, rs, po2, T0);
  }
  STAMP(15);
  g_oo += 1 << 19; g_so += 32768; o_pub += 65536;
  PIN2(g_oo, g_so); PIN1(o_pub);
  if (DLY_B2) __builtin_amdgcn_s_sleep(DLY_B2);
  o_gemv<false>([]() {}, []() {}, wof, wor, L, tid, lane, rs, g_oo, g_so, o_xw, o_xr, o_wr, o_rd, o_pub, tag, T0, budget, w, 28);
  STAMP(16);
  {
    const f4 r = mixer<DLY_CD2>([]() {}, xres, L, tid, w, lane, j, rs, WS_PB + 65536 + b * 8 * 2048, tag, b2, budget, 30, mprev);
    if (w == 0) {
      const u32 lo = cvtpk(r[0], r[1]), hi = cvtpk(r[2], r[3]);
      if (lane < 2) { v2u o = {lo, hi}; *(GAS v2u*)((GAS unsigned char*)p.out + (b * 512 + 8 * j + 4 * lane) * 2) = o; }
      if (lane == 0 && j == 0) __builtin_amdgcn_raw_buffer_store_b32(T0, rs, WS_EP + b * 256, 0, 16);
    }
  }
  STAMP(17); STAMPR(37);
}

extern "C" void kernel_launch(void* const* d_in, const int* in_sizes, int n_in, void* d_out, int out_size, void* d_ws, size_t ws_size, hipStream_t stream) {
  Params p;
  p.x = (const u16*)d_in[0]; p.K = (const unsigned char*)d_in[1]; p.V = (const unsigned char*)d_in[2];
  p.wq = (const unsigned char*)d_in[3]; p.wo = (const unsigned char*)d_in[4];
  p.w1 = (const u16*)d_in[6]; p.b1 = (const u16*)d_in[7]; p.w2 = (const u16*)d_in[8]; p.b2 = (const u16*)d_in[9];
  p.out = (u16*)d_out; p.ws = (unsigned char*)d_ws;
  hipLaunchKernelGGL(fused_decode_kernel, dim3(256), dim3(512), 0, stream, p);
}
```

```cpp
#include <hip/hip_runtime.h>

typedef unsigned int u32;
typedef unsigned short u16;
typedef unsigned long long u64;
typedef u32 v4u __attribute__((ext_vector_type(4)));
typedef u32 v2u __attribute__((ext_vector_type(2)));
typedef float f4 __attribute__((ext_vector_type(4)));
typedef float f2 __attribute__((ext_vector_type(2)));
typedef short s8v __attribute__((ext_vector_type(8)));
typedef short s4v __attribute__((ext_vector_type(4)));
typedef __bf16 bf2v __attribute__((ext_vector_type(2)));
#define LAS __attribute__((address_space(3)))
#define GAS __attribute__((address_space(1)))
#define DI __device__ __forceinline__

#define STAMPS 0
#define DLY_B 10
#define DLY_CD 8
#define DLY_E 16
#define DLY_A 6
#define STW 0
#define KVPOL " nt"
#define WPOL " nt"
#define PRETOUCH 1
#define OROT 4
#define XBS 128
#define BAR_B2 0
#define DLY_B2 13
#define DLY_CD2 7
#define RS_CD 0
#define RS_B 0
#define WD2 5
#define WD3 5
#define WD 5
#define CODE_KB 17

constexpr int SK = 16384, WIN = 4096;
constexpr float EPS = 1e-5f;
constexpr float SCL = 0.08838834764831845f * 1.4426950408889634f;
constexpr float GK0 = -2.0f * 0.7978845608028654f * 1.4426950408889634f;
constexpr float GK1 = GK0 * 0.044715f;

constexpr int WS_EP = 0;
constexpr int WS_QB = 4096;
constexpr int WS_CS = WS_QB + 2 * 4 * 8 * 2 * 2048;
constexpr int WS_OB = WS_CS + 4 * 8 * 2 * 2048;
constexpr int WS_OS = WS_OB + 2 * 4 * 8 * 8 * 2048;
constexpr int WS_PB = WS_OS + 2 * 4 * 8 * 8 * 128;
constexpr int WS_XB = WS_PB + 2 * 4 * 8 * 2048;
constexpr int WS_END = WS_XB + 4 * 64 * XBS;
constexpr int WS_STAMP = 4 << 20;

constexpr int L_KV = 0;
constexpr int L_PAR = 131072;
constexpr int PSTR = 1088;
constexpr int L_PL = L_PAR + 11264;
constexpr int QSTR = 288;
constexpr int L_RED = L_PL + 4 * PSTR;
constexpr int L_ST = L_RED + 5120;
constexpr int L_MB = L_ST + 64;
constexpr int L_LB = L_MB + 128;
constexpr int L_R2 = L_LB + 512;
constexpr int L_QL = L_R2 + 128;
constexpr int L_XL = L_QL + 4 * QSTR;
constexpr int L_SC2 = L_QL;
constexpr int L_STAMP = L_SC2 + 8192;
constexpr int L_END = L_STAMP + (STAMPS ? 1024 + 64 : 0);
static_assert(L_END <= 163840, "LDS");

struct Params {
  const u16* x; const unsigned char* K; const unsigned char* V; const unsigned char* wq; const unsigned char* wo;
  const u16* w1; const u16* b1; const u16* w2; const u16* b2; u16* out; unsigned char* ws;
};

template <int CTRL> DI float dpp(float x) {
  return __builtin_bit_cast(float, __builtin_amdgcn_update_dpp(0, __builtin_bit_cast(int, x), CTRL, 0xf, 0xf, true));
}
constexpr int XOR1 = 0xB1, XOR2 = 0x4E, HMIR = 0x141, MIR = 0x140, ROR4 = 0x124, ROR8 = 0x128;
DI float asf(u32 x) { return __builtin_bit_cast(float, x); }
DI u32 asu(float x) { return __builtin_bit_cast(u32, x); }
DI u32 cvtpk(float lo, float hi) { f2 f = {lo, hi}; return __builtin_bit_cast(u32, __builtin_convertvector(f, bf2v)); }
DI f4 mfma(v4u a, v4u b, f4 c) {
  return __builtin_amdgcn_mfma_f32_16x16x32_bf16(__builtin_bit_cast(s8v, a), __builtin_bit_cast(s8v, b), c, 0, 0, 0);
}
DI float fmx(float a, float b) { return __builtin_elementwise_maximum(a, b); }
DI float xrow_max(float x) {
  auto s = __builtin_amdgcn_permlane16_swap(asu(x), asu(x), false, false);
  x = fmx(asf(s[0]), asf(s[1]));
  auto t = __builtin_amdgcn_permlane32_swap(asu(x), asu(x), false, false);
  return fmx(asf(t[0]), asf(t[1]));
}
DI float quad_reduce4(f4 r, int lane) {
  float t0 = (lane & 1) ? r[1] : r[0], t1 = (lane & 1) ? r[0] : r[1];
  t0 += dpp<XOR1>(t1);
  float t2 = (lane & 1) ? r[3] : r[2], t3 = (lane & 1) ? r[2] : r[3];
  t2 += dpp<XOR1>(t3);
  float s0 = (lane & 2) ? t2 : t0; const float s1 = (lane & 2) ? t0 : t2;
  s0 += dpp<XOR2>(s1);
  return s0;
}
DI void st32s(u32 v, __amdgpu_buffer_rsrc_t rs, int off) { __builtin_amdgcn_raw_buffer_store_b32(v, rs, off, 0, 16); }
template <int N> DI u32 rowbc(u32 x) { return (u32)__builtin_amdgcn_update_dpp(0, (int)x, 0x150 + N, 0xf, 0xf, false); }
DI u32 xo(u32 a, u32 t, u32 acc) { return __builtin_amdgcn_bitop3_b32(a, t, acc, 0xBE); }
#define PIN1(a) asm volatile("" : "+v"(a))
#define PIN2(a, b) asm volatile("" : "+v"(a), "+v"(b))
#define PIN4(a, b, c, d) asm volatile("" : "+v"(a), "+v"(b), "+v"(c), "+v"(d))
#define PIN8(x) asm volatile("" : "+v"(x[0]), "+v"(x[1]), "+v"(x[2]), "+v"(x[3]), "+v"(x[4]), "+v"(x[5]), "+v"(x[6]), "+v"(x[7]))
DI float xrow_sum(float x) {
  auto s = __builtin_amdgcn_permlane16_swap(asu(x), asu(x), false, false);
  x = asf(s[0]) + asf(s[1]);
  auto t = __builtin_amdgcn_permlane32_swap(asu(x), asu(x), false, false);
  return asf(t[0]) + asf(t[1]);
}
#define BAR() asm volatile("s_waitcnt lgkmcnt(0)\n\ts_barrier" ::: "memory")
#define CBAR() asm volatile("" ::: "memory")
#define SB() __builtin_amdgcn_sched_barrier(0)

#define STAMP(i)
#define STAMPR(i)
#define STAMPV(i, v)
#define STAMPW(i)

constexpr u32 TMASK = 0x3ffu;
DI u32 tagf(float v, u32 tag) { return __builtin_amdgcn_bitop3_b32(asu(v) + 0x200u, TMASK, tag, 0xBA); }
DI float untag(u32 v) { return asf(v & ~TMASK); }

DI u32 ld32s(__amdgpu_buffer_rsrc_t rs, int off) { return __builtin_amdgcn_raw_buffer_load_b32(rs, off, 0, 16); }
DI v4u ld128s(__amdgpu_buffer_rsrc_t rs, int off) { return __builtin_amdgcn_raw_buffer_load_b128(rs, off, 0, 16); }
DI void st128s(v4u v, __amdgpu_buffer_rsrc_t rs, int off) { __builtin_amdgcn_raw_buffer_store_b128(v, rs, off, 0, 16); }

DI void ln_stats(float x, LAS unsigned char* L, int w, int lane, float& mean, float& var) {
  const float c = asf(__builtin_amdgcn_readfirstlane(asu(x)));
  const float d = x - c;
  float s1 = d, s2 = d * d;
  s1 += dpp<XOR1>(s1); s2 += dpp<XOR1>(s2);
  s1 += dpp<XOR2>(s1); s2 += dpp<XOR2>(s2);
  s1 += dpp<HMIR>(s1); s2 += dpp<HMIR>(s2);
  s1 += dpp<MIR>(s1); s2 += dpp<MIR>(s2);
  asm volatile("s_nop 1\n\tv_add_f32_dpp %0, %0, %0 row_bcast:15 row_mask:0xa bank_mask:0xf\n\tv_add_f32_dpp %1, %1, %1 row_bcast:15 row_mask:0xa bank_mask:0xf\n\t"
               "s_nop 0\n\tv_add_f32_dpp %0, %0, %0 row_bcast:31 row_mask:0xc bank_mask:0xf\n\tv_add_f32_dpp %1, %1, %1 row_bcast:31 row_mask:0xc bank_mask:0xf"
               : "+v"(s1), "+v"(s2));
  const f2 pv = {fmaf(s1, 1.0f / 64, c), fmaf(s1 * s1, -1.0f / 64, s2)};
  if (lane == 63) *(LAS f2*)(L + L_ST + w * 8) = pv;
  BAR();
  const f2 v = *(const LAS f2*)(L + L_ST + (lane & 7) * 8);
  float m = v.x;
  m += dpp<XOR1>(m); m += dpp<XOR2>(m); m += dpp<HMIR>(m);
  mean = m * 0.125f;
  const float dv = v.x - mean;
  float t = fmaf(64.0f * dv, dv, v.y);
  t += dpp<XOR1>(t); t += dpp<XOR2>(t); t += dpp<HMIR>(t);
  var = t * (1.0f / 512);
}

DI void transpose8(const v4u (&raw)[8], v4u (&fr)[8]) {
#pragma unroll
  for (int i = 0; i < 8; ++i) {
    const u32 sel = (i & 1) ? 0x07060302u : 0x05040100u;
#pragma unroll
    for (int m = 0; m < 4; ++m) fr[i][m] = __builtin_amdgcn_perm(raw[2 * m + 1][i >> 1], raw[2 * m][i >> 1], sel);
  }
}

DI v4u q_operand(LAS unsigned char* L, int xaddr, float mean, float var) {
  const f4 a = *(const LAS f4*)(L + xaddr), b = *(const LAS f4*)(L + xaddr + 16);
  const float rs_ = __builtin_amdgcn_rsqf(var + EPS), mr = -mean * rs_;
  return v4u{cvtpk(fmaf(a[0], rs_, mr), fmaf(a[1], rs_, mr)), cvtpk(fmaf(a[2], rs_, mr), fmaf(a[3], rs_, mr)),
             cvtpk(fmaf(b[0], rs_, mr), fmaf(b[1], rs_, mr)), cvtpk(fmaf(b[2], rs_, mr), fmaf(b[3], rs_, mr))};
}

DI void q_gemv1(const v4u (&wf)[8], v4u a, LAS unsigned char* L, int lane, int wraddr) {
  f4 d[8];
#pragma unroll
  for (int i = 0; i < 8; ++i) d[i] = mfma(a, wf[i], f4{0.f, 0.f, 0.f, 0.f});
  f4 v;
#pragma unroll
  for (int m = 0; m < 4; ++m) v[m] = (lane & 16) ? d[4 + m][0] : d[m][0];
  if (lane < 32) *(LAS f4*)(L + wraddr) = v;
}
template <int STR>
DI void q_gemv2(LAS unsigned char* L, int lane, int rdaddr, __amdgpu_buffer_rsrc_t rs, int qoff, u32 tag) {
  const f4 r = *(const LAS f4*)(L + rdaddr) + *(const LAS f4*)(L + rdaddr + 4 * STR);
  st32s(tagf(quad_reduce4(r, lane), tag), rs, qoff);
}
DI void q_gemv(const v4u (&wf)[8], v4u a, LAS unsigned char* L, int tid, int lane, int wraddr, int rdaddr, __amdgpu_buffer_rsrc_t rs, int qoff, u32 tag) {
  q_gemv1(wf, a, L, lane, wraddr);
  BAR();
  if (tid < 128) q_gemv2<576>(L, lane, rdaddr, rs, qoff, tag);
}

DI float attn_scores(float qv, const v4u (&kf)[16], LAS unsigned char* L, int qwr, int qrd, int mwr, int mrd, int pwr, int lwr, int lane) {
  *(LAS u16*)(L + qwr) = (u16)(cvtpk(qv, qv) & 0xffffu);
  BAR();
  v4u qf[4];
#pragma unroll
  for (int st = 0; st < 4; ++st) qf[st] = *(const LAS v4u*)(L + qrd + st * 64);
  SB();
  f4 d[4];
#pragma unroll
  for (int st = 0; st < 4; ++st)
#pragma unroll
    for (int t = 0; t < 4; ++t) d[t] = mfma(kf[t * 4 + st], qf[st], st == 0 ? f4{0.f, 0.f, 0.f, 0.f} : d[t]);
  float sc[4];
#pragma unroll
  for (int i = 0; i < 4; ++i) {
    const float a = (lane & 4) ? d[1][i] : d[0][i], bq = (lane & 4) ? d[3][i] : d[2][i];
    sc[i] = (lane & 8) ? bq : a;
  }
  float m = fmx(fmx(sc[0], sc[1]), fmx(sc[2], sc[3]));
  m = fmx(m, dpp<ROR4>(m)); m = fmx(m, dpp<ROR8>(m));
  m = xrow_max(m);
  if (lane < 4) *(LAS float*)(L + mwr) = m;
  BAR();
  const f4 m0 = *(const LAS f4*)(L + mrd), m1 = *(const LAS f4*)(L + mrd + 16);
  float M = fmx(fmx(fmx(m0[0], m0[1]), fmx(m0[2], m0[3])), fmx(fmx(m1[0], m1[1]), fmx(m1[2], m1[3])));
  M = asf(asu(M * SCL) & ~TMASK);
  float p[4];
#pragma unroll
  for (int i = 0; i < 4; ++i) p[i] = __builtin_amdgcn_exp2f(fmaf(sc[i], SCL, -M));
  v2u pk = {cvtpk(p[0], p[1]), cvtpk(p[2], p[3])};
  *(LAS v2u*)(L + pwr) = pk;
  float l = (p[0] + p[1]) + (p[2] + p[3]);
  l += dpp<ROR4>(l); l += dpp<ROR8>(l);
  if ((lane & 12) == 0) *(LAS float*)(L + lwr) = l;
  return M;
}

DI v4u v_frag(int t, int u, LAS unsigned char* L, int vbase) {
  const s4v lo = __builtin_amdgcn_ds_read_tr16_b64_v4i16((LAS s4v*)(L + vbase + u * 32768 + t * 4096));
  const s4v hi = __builtin_amdgcn_ds_read_tr16_b64_v4i16((LAS s4v*)(L + vbase + u * 32768 + t * 4096 + 1024));
  const v2u l2 = __builtin_bit_cast(v2u, lo), h2 = __builtin_bit_cast(v2u, hi);
  return v4u{l2[0], l2[1], h2[0], h2[1]};
}
DI void pv_chunk(int t, f4 (&acc)[4], LAS unsigned char* L, int vbase, int lane) {
  const v4u pf = *(const LAS v4u*)(L + L_PL + t * PSTR + lane * 16);
#pragma unroll
  for (int u = 0; u < 4; ++u) acc[u] = mfma(pf, v_frag(t, u, L, vbase), acc[u]);
}

DI void attn_publish_stats(float M, LAS unsigned char* L, int w, int lane, __amdgpu_buffer_rsrc_t rs, int saddr, u32 tag, u32 T0) {
  if (w == 7) {
    float l = *(const LAS float*)(L + L_LB + lane * 4) + *(const LAS float*)(L + L_LB + (lane + 64) * 4);
    l += dpp<ROR4>(l); l += dpp<ROR8>(l);
    l = xrow_sum(l);
    if (lane < 8) { v4u sv = {asu(M) | tag, tagf(l, tag), T0, T0}; st128s(sv, rs, saddr); }
  }
}
template <int RM> DI float rowput(float old, float x) {
  return __builtin_bit_cast(float, __builtin_amdgcn_update_dpp(__builtin_bit_cast(int, old), __builtin_bit_cast(int, x), 0xE4, RM, 0xf, false));
}
DI void attn_publish(const f4 (&acc)[4], int lane, __amdgpu_buffer_rsrc_t rs, int oaddr, u32 T0) {
  float y[4];
#pragma unroll
  for (int h = 0; h < 4; ++h) y[h] = rowput<0x8>(rowput<0x4>(rowput<0x2>(acc[0][h], acc[1][h]), acc[2][h]), acc[3][h]);
  auto s01 = __builtin_amdgcn_permlane16_swap(asu(y[0]), asu(y[1]), false, false);
  auto s23 = __builtin_amdgcn_permlane16_swap(asu(y[2]), asu(y[3]), false, false);
  const float z01 = asf(s01[0]) + asf(s01[1]), z23 = asf(s23[0]) + asf(s23[1]);
  auto sf = __builtin_amdgcn_permlane32_swap(asu(z01), asu(z23), false, false);
  const float v = asf(sf[0]) + asf(sf[1]);
  st32s(__builtin_amdgcn_perm(cvtpk(v, v), T0, 0x05040100u), rs, oaddr);
}

DI bool o_check(const v4u& x0, const v4u& x1, const v4u& sv, u32 T0) {
  u32 d = xo(x0[0], T0, 0u);
  d = xo(x0[1], T0, d); d = xo(x0[2], T0, d); d = xo(x0[3], T0, d);
  d = xo(x1[0], T0, d); d = xo(x1[1], T0, d); d = xo(x1[2], T0, d); d = xo(x1[3], T0, d);
  d = xo(sv[0], T0, d); d = xo(sv[1], T0, d);
  d = xo(sv[3], T0, xo(sv[2], T0, d & TMASK));
  return __all(d == 0);
}
template <bool FIRST, class Hook, class Hook2>
DI void o_gemv(Hook&& hook, Hook2&& hook2, v4u (&wf)[8], v4u (&wraw)[8], LAS unsigned char* L, int tid, int lane, __amdgpu_buffer_rsrc_t rs, int oo, int so, int xw, int xr, int wraddr, int rdaddr, int poff, u32 tag, u32 T0, int& budget, int w, int sb) {
  v4u x0, x1, sv = {T0, T0, T0, T0};
  if (sb == 28) STAMP(39);
  CBAR();
  x0 = ld128s(rs, oo); x1 = ld128s(rs, oo + 256);
  if (lane < 16) sv = ld128s(rs, so);
  if (FIRST) {
    asm volatile("" : "+v"(wraw[0]), "+v"(wraw[1]), "+v"(wraw[2]), "+v"(wraw[3]), "+v"(wraw[4]), "+v"(wraw[5]), "+v"(wraw[6]), "+v"(wraw[7])::"memory");
    STAMP(9); STAMPW(80);
    transpose8(wraw, wf);
    PIN8(wf);
    hook();
  }
  while (!o_check(x0, x1, sv, T0) && --budget > 0) {
    if (RS_B) __builtin_amdgcn_s_sleep(RS_B);
    CBAR();
    x0 = ld128s(rs, oo); x1 = ld128s(rs, oo + 256);
    if (lane < 16) sv = ld128s(rs, so);
  }
  STAMP(sb); STAMPV(sb + 1, budget); if (sb == 28) STAMPW(104);
  const v4u pk = {__builtin_amdgcn_perm(x0[1], x0[0], 0x07060302u), __builtin_amdgcn_perm(x0[3], x0[2], 0x07060302u),
                  __builtin_amdgcn_perm(x1[1], x1[0], 0x07060302u), __builtin_amdgcn_perm(x1[3], x1[2], 0x07060302u)};
  *(LAS v4u*)(L + xw) = pk;
  CBAR();
  const v4u a = *(const LAS v4u*)(L + xr);
  const float ms = untag(sv[0]), ls = untag(sv[1]);
  float M = ms;
  M = fmx(M, dpp<XOR1>(M)); M = fmx(M, dpp<XOR2>(M)); M = fmx(M, dpp<HMIR>(M));
  const float f = __builtin_amdgcn_exp2f(ms - M);
  float Ls = f * ls;
  Ls += dpp<XOR1>(Ls); Ls += dpp<XOR2>(Ls); Ls += dpp<HMIR>(Ls);
  const float cs = f * __builtin_amdgcn_rcpf(Ls);
  f4 d[8];
#pragma unroll
  for (int i = 0; i < 8; ++i) d[i] = mfma(a, wf[i], f4{0.f, 0.f, 0.f, 0.f});
  const int bp = (lane >> 4) * 16;
  const float cr0 = asf(__builtin_amdgcn_ds_bpermute(bp, asu(cs))), cr1 = asf(__builtin_amdgcn_ds_bpermute(bp + 4, asu(cs)));
  const float cr2 = asf(__builtin_amdgcn_ds_bpermute(bp + 8, asu(cs))), cr3 = asf(__builtin_amdgcn_ds_bpermute(bp + 12, asu(cs)));
  const f2 c01 = {cr0, cr1}, c23 = {cr2, cr3};
  float e[8];
#pragma unroll
  for (int i = 0; i < 8; ++i) { const f2 t = f2{d[i][2], d[i][3]} * c23 + f2{d[i][0], d[i][1]} * c01; e[i] = t[0] + t[1]; }
  f4 v;
#pragma unroll
  for (int m = 0; m < 4; ++m) { auto t = __builtin_amdgcn_permlane16_swap(asu(e[m]), asu(e[m + 4]), false, false); v[m] = asf(t[0]) + asf(t[1]); }
  if ((lane >> 5) == ((lane >> 3) & 1)) *(LAS f4*)(L + wraddr) = v;
  BAR();
  if (tid < 64) {
    const f4 r = (*(const LAS f4*)(L + rdaddr) + *(const LAS f4*)(L + rdaddr + 4 * 320)) + (*(const LAS f4*)(L + rdaddr + 8 * 320) + *(const LAS f4*)(L + rdaddr + 12 * 320));
    st32s(tagf(quad_reduce4(r, lane), tag), rs, poff);
  }
  hook2();
}

template <int DLY, class Hook>
DI f4 mixer(Hook&& hook, float& xres, LAS unsigned char* L, int tid, int w, int lane, int j, __amdgpu_buffer_rsrc_t rs, int pbase, u32 tag, float b2, int& budget, int sb, float& mean_out) {
  float pw1[7], pb1[7], pw2[7];
  const int fl = tid & 255, q = tid >> 8;
#pragma unroll
  for (int i = 0; i < 7; ++i) {
    pw1[i] = asf((u32) * (const LAS u16*)(L + L_PAR + (fl + 256 * i) * 2) << 16);
    pb1[i] = asf((u32) * (const LAS u16*)(L + L_PAR + 3584 + (fl + 256 * i) * 2) << 16);
    pw2[i] = asf((u32) * (const LAS u16*)(L + L_PAR + 7168 + (fl + 256 * i) * 2) << 16);
  }
#pragma unroll
  for (int i = 0; i < 7; ++i) asm volatile("" : "+v"(pw1[i]), "+v"(pb1[i]), "+v"(pw2[i]));
  int po = pbase + tid * 4, xa = L_XL + (8 * j + 4 * q) * 4, xw = L_XL + tid * 4;
  asm volatile("" : "+v"(po), "+v"(xa), "+v"(xw));
  if (DLY) __builtin_amdgcn_s_sleep(DLY);
  if (sb == 30) STAMP(35);
  u32 p[8];
  for (;;) {
    CBAR();
#pragma unroll
    for (int i = 0; i < 8; ++i) p[i] = ld32s(rs, po + i * 2048) ^ tag;
    const u32 d = (p[0] | p[1] | p[2]) | (p[3] | p[4] | p[5]) | (p[6] | p[7]);
    if (__all((d & TMASK) == 0) || --budget <= 0) break;
    if (RS_CD) __builtin_amdgcn_s_sleep(RS_CD);
  }
  STAMP(sb); STAMPV(sb + 1, budget); if (sb == 30) STAMPW(112);
  hook();
  const float x1 = xres + (((asf(p[0]) + asf(p[1])) + (asf(p[2]) + asf(p[3]))) + ((asf(p[4]) + asf(p[5])) + (asf(p[6]) + asf(p[7]))));
  xres = x1;
  *(LAS float*)(L + xw) = x1;
  float mean, var;
  ln_stats(x1, L, w, lane, mean, var);
  mean_out = mean;
  const f4 xv = *(const LAS f4*)(L + xa);
  const float r1 = __builtin_amdgcn_rsqf(var + EPS);
  const float r2 = __builtin_amdgcn_rsqf(var * r1 * r1 + EPS);
  const float rr = r1 * r2, mr = -mean * rr;
  STAMP(sb + 2); if (sb == 22) STAMPW(88);
  const f2 hA = {fmaf(xv[0], rr, mr), fmaf(xv[1], rr, mr)}, hB = {fmaf(xv[2], rr, mr), fmaf(xv[3], rr, mr)};
  f2 aA = {0.f, 0.f}, aB = {0.f, 0.f};
#pragma unroll
  for (int i = 0; i < 7; ++i) {
    const f2 w1 = {pw1[i], pw1[i]}, b1 = {pb1[i], pb1[i]}, w2 = {pw2[i], pw2[i]};
    const f2 k0 = {GK0, GK0}, k1 = {GK1, GK1}, one = {1.f, 1.f};
    {
      const f2 u = hA * w1 + b1, z = u * (u * u * k1 + k0);
      const f2 e = {__builtin_amdgcn_exp2f(z[0]), __builtin_amdgcn_exp2f(z[1])}, dd = e + one;
      const f2 rc = {__builtin_amdgcn_rcpf(dd[0]), __builtin_amdgcn_rcpf(dd[1])};
      aA += (u * rc) * w2;
    }
    {
      const f2 u = hB * w1 + b1, z = u * (u * u * k1 + k0);
      const f2 e = {__builtin_amdgcn_exp2f(z[0]), __builtin_amdgcn_exp2f(z[1])}, dd = e + one;
      const f2 rc = {__builtin_amdgcn_rcpf(dd[0]), __builtin_amdgcn_rcpf(dd[1])};
      aB += (u * rc) * w2;
    }
  }
  float t0 = (lane & 1) ? aA[1] : aA[0], t1 = (lane & 1) ? aA[0] : aA[1];
  t0 += dpp<XOR1>(t1);
  float t2 = (lane & 1) ? aB[1] : aB[0], t3 = (lane & 1) ? aB[0] : aB[1];
  t2 += dpp<XOR1>(t3);
  float s0 = (lane & 2) ? t2 : t0, s1 = (lane & 2) ? t0 : t2;
  s0 += dpp<XOR2>(s1);
  s0 += dpp<ROR4>(s0); s0 += dpp<ROR8>(s0);
  s0 = xrow_sum(s0);
  STAMP(sb + 3); if (sb == 22) STAMPW(96);
  if (lane < 4) *(LAS float*)(L + L_R2 + (w * 4 + lane) * 4) = s0;
  BAR();
  f4 res = {0.f, 0.f, 0.f, 0.f};
  if (w == 0) {
    const int qq = lane & 1;
    const f4 a = (*(const LAS f4*)(L + L_R2 + (qq * 4 + 0) * 16) + *(const LAS f4*)(L + L_R2 + (qq * 4 + 1) * 16)) +
                 (*(const LAS f4*)(L + L_R2 + (qq * 4 + 2) * 16) + *(const LAS f4*)(L + L_R2 + (qq * 4 + 3) * 16));
    const f4 xr = *(const LAS f4*)(L + L_XL + (8 * j + 4 * qq) * 4);
    res = xr + a + f4{b2, b2, b2, b2};
  }
  return res;
}

__global__ void __launch_bounds__(512, 2) fused_decode_kernel(Params p) {
  __shared__ __attribute__((aligned(1024))) unsigned char smem[L_END];
  LAS unsigned char* L = (LAS unsigned char*)smem;
  const int tid = threadIdx.x, lane = tid & 63, w = __builtin_amdgcn_readfirstlane(tid >> 6);
  const int bid = blockIdx.x;
  v4u dummy = {0u, 0u, 0u, 0u};
  {
    const int piece = (bid >> 3) * 8 + w;
    if (piece < CODE_KB) {
      u64 pc; asm volatile("s_getpc_b64 %0" : "=s"(pc));
      const u64 a = (pc & ~(u64)255) + (u64)(piece * 1024 + lane * 16);
      asm volatile("global_load_dwordx4 %0, %1, off" : "+v"(dummy) : "v"(a) : "memory");
    }
  }
  const int kvh = bid & 7, b = bid >> 6, s = (bid >> 3) & 7;
  const int j = kvh * 8 + s;
  const int g = lane >> 4, c = lane & 15;
  STAMPR(36); STAMP(0);

  const size_t slab = ((size_t)(b * 8 + kvh) * SK + (SK - WIN) + s * 512) * 256;
  const __amdgpu_buffer_rsrc_t rK = __builtin_amdgcn_make_buffer_rsrc((void*)(p.K + slab), 0, 512 * 256, 0x00020000);
  const __amdgpu_buffer_rsrc_t rV = __builtin_amdgcn_make_buffer_rsrc((void*)(p.V + slab), 0, 512 * 256, 0x00020000);
  const __amdgpu_buffer_rsrc_t rQ = __builtin_amdgcn_make_buffer_rsrc((void*)p.wq, 0, 512 * 4096 * 2, 0x00020000);
  const __amdgpu_buffer_rsrc_t rO = __builtin_amdgcn_make_buffer_rsrc((void*)p.wo, 0, 512 * 4096 * 2, 0x00020000);
  const __amdgpu_buffer_rsrc_t rs = __builtin_amdgcn_make_buffer_rsrc((void*)p.ws, 0, WS_END, 0x00020000);
  const __amdgpu_buffer_rsrc_t rX = __builtin_amdgcn_make_buffer_rsrc((void*)p.x, 0, 4096, 0x00020000);
  const u32 ldsb = (u32)(size_t)L;

  u32 ev, xraw, b2raw, dummy2 = 0, dummy3 = 0;
  asm volatile("s_nop 4\n\tbuffer_load_dword %0, %1, %2, 0 offen sc1" : "=v"(ev) : "v"(WS_EP + b * 256), "s"(rs) : "memory");
  asm volatile("buffer_load_ushort %0, %1, %2, 0 offen" : "=v"(xraw) : "v"((b * 512 + tid) * 2), "s"(rX) : "memory");
  asm volatile("global_load_ushort %0, %1, %2" : "=v"(b2raw) : "v"(0), "s"(p.b2) : "memory");
  const int rot = __builtin_amdgcn_readfirstlane((int)(((u32)(bid * 8 + w) * 2654435761u) >> 28));
  const int cg = s >> 1, kh = s & 1;
  const int voq = ((256 * kh + 32 * w + 8 * g) * 4096 + kvh * 512 + 128 * cg + 8 * c) * 2;
  const int so_ = (s + OROT) & 7;
  const int voo = ((kvh * 512 + (2 * (w >> 2) + (c >> 3)) * 128 + 32 * (w & 3) + 4 * g) * 512 + 64 * so_ + 8 * (c & 7)) * 2;
  int vok[4], vov[2];
#pragma unroll
  for (int i = 0; i < 4; ++i) vok[i] = (64 * w + g) * 256 + ((c ^ (4 * i + g)) * 16);
#pragma unroll
  for (int i = 0; i < 2; ++i) vov[i] = (64 * w + g) * 256 + ((((c >> 1) ^ (g | (i << 2))) * 32) + (c & 1) * 16);
  v4u wqr[8], wor[8], kf[16];
#define LDQ(jj) asm volatile("buffer_load_dwordx4 %0, %1, %2, %3 offen" WPOL : "=v"(wqr[jj]) : "v"(voq), "s"(rQ), "s"((jj) * 8192) : "memory")
#define LDO(jj) asm volatile("buffer_load_dwordx4 %0, %1, %2, %3 offen" WPOL : "=v"(wor[jj]) : "v"(voo), "s"(rO), "s"((16 * ((jj) >> 2) + ((jj) & 3)) * 1024) : "memory")
#define LDK(i) asm volatile("s_mov_b32 m0, %0\n\ts_nop 0\n\tbuffer_load_dwordx4 %1, %2, %3 offen" KVPOL " lds" ::"s"(ldsb + L_KV + w * 16384 + (i) * 1024), "v"(vok[(i) & 3]), "s"(rK), "s"((((i) + rot) & 15) * 1024) : "memory")
#define LDV(i) asm volatile("s_mov_b32 m0, %0\n\ts_nop 0\n\tbuffer_load_dwordx4 %1, %2, %3 offen" KVPOL " lds" ::"s"(ldsb + L_KV + w * 16384 + (i) * 1024), "v"(vov[((i) >> 1) & 1]), "s"(rV), "s"((((i) + rot) & 15) * 1024) : "memory")
#define GW(d) asm volatile("s_waitcnt vmcnt(%0)" ::"n"((d) - 1) : "memory")
#define G() GW(GWIN)
#define GWIN WD
#define EV(n, i) asm volatile("s_waitcnt vmcnt(%0)" ::"n"((n) - (i) - 1) : "memory")
#define KFRAG(t)                                                                                                     \
  _Pragma("unroll") for (int st = 0; st < 4; ++st)                                                                   \
      kf[(t) * 4 + st] = *(const LAS v4u*)(L + L_KV + w * 16384 + (16 * (t) + c) * 256 + (((4 * st + g) ^ c) * 16)); \
  asm volatile("" : "+v"(kf[(t) * 4]), "+v"(kf[(t) * 4 + 1]), "+v"(kf[(t) * 4 + 2]), "+v"(kf[(t) * 4 + 3])::"memory")
  LDQ(0); LDK(0); LDQ(1); LDK(1); LDQ(2); LDK(2);
  STAMP(38);
  if (w == 7) {
    const int me = (b * 8 + kvh) * 8 + s;
    int a_ = lane < 16 ? WS_OB + me * 2048 + lane * 128
           : lane < 32 ? WS_OB + (1 << 19) + me * 2048 + (lane - 16) * 128
           : lane < 34 ? WS_OS + (lane - 32) * 32768 + me * 128
           : lane < 42 ? WS_QB + ((lane - 34) >> 2) * (4 * 8 * 4096) + ((b * 8 + kvh) * 2 + (s & 1)) * 2048 + (s >> 1) * 512 + ((lane - 34) & 3) * 128
           : lane < 46 ? WS_PB + ((lane - 42) >> 1) * 65536 + (b * 8 + kvh) * 2048 + ((s + OROT) & 7) * 256 + ((lane - 42) & 1) * 128
           : WS_XB + (b * 64 + j) * XBS;
    asm volatile("buffer_load_dword %0, %1, %2, 0 offen" : "+v"(dummy2) : "v"(a_), "s"(rs) : "memory");
  }
  if (w == 6) {
    const unsigned char* a_ = lane == 0 ? p.V + slab : lane == 1 ? p.V + slab + 131068 : lane == 2 ? p.wo + (size_t)kvh * 524288 : lane == 3 ? p.wo + (size_t)kvh * 524288 + 524284
                            : lane == 4 ? (const unsigned char*)p.w1 : lane == 5 ? (const unsigned char*)p.b1 : lane == 6 ? (const unsigned char*)p.w2 : lane == 7 ? p.K + slab + 131068
                            : p.wq + (size_t)(s & 1) * 2097152 + 2097148;
    asm volatile("global_load_dword %0, %1, off" : "+v"(dummy3) : "v"(a_) : "memory");
  }
  G(); LDQ(3); G(); LDK(3);
  asm volatile("s_waitcnt vmcnt(8)" : "+v"(ev), "+v"(xraw), "+v"(b2raw)::"memory");
  const u32 T0 = __builtin_amdgcn_readfirstlane(ev) + 1u;
  const u32 tag = T0 & TMASK;
  float xres = asf(xraw << 16);
  const int xqa = L_XL + (256 * kh + 32 * w + 8 * g) * 4;
  v4u qa;
  {
    *(LAS float*)(L + L_XL + tid * 4) = xres;
    float mean, var;
    ln_stats(xres, L, w, lane, mean, var);
    qa = q_operand(L, xqa, mean, var);
    PIN1(qa);
  }
  G(); LDQ(4); G(); LDK(4); G(); LDQ(5); G(); LDK(5);
  G(); LDQ(6); G(); LDK(6); G(); LDQ(7); G(); LDK(7);
  EV(16, 7); STAMPW(48); KFRAG(0);
  G(); LDK(8); G(); LDK(9); G(); LDK(10); G(); LDK(11); G(); LDK(12);
  EV(21, 14);
  asm volatile("" : "+v"(wqr[0]), "+v"(wqr[1]), "+v"(wqr[2]), "+v"(wqr[3]), "+v"(wqr[4]), "+v"(wqr[5]), "+v"(wqr[6]), "+v"(wqr[7]), "+v"(dummy), "+v"(dummy2), "+v"(dummy3)::"memory");
  STAMP(1); STAMPW(40);
  LDK(13); LDK(14); LDK(15);
  {
#pragma unroll
    for (int k = 0; k < 2; ++k) {
      const int piece = k == 0 ? w : 8 + (w % 3);
      int gr = piece * 64 + lane; gr = gr < 672 ? gr : 671;
      const int arr = gr / 224, e = gr - arr * 224;
      const unsigned char* src = (const unsigned char*)(arr == 0 ? p.w1 : arr == 1 ? p.b1 : p.w2) + e * 16;
      asm volatile("s_mov_b32 m0, %0\n\ts_nop 0\n\tglobal_load_lds_dwordx4 %1, off" ::"s"(ldsb + L_PAR + piece * 1024), "v"(src) : "memory");
    }
  }
  LDV(0); LDV(1);
  v4u wqf[8];
  transpose8(wqr, wqf);
  const int qwr = L_RED + w * 576 + (8 * c + 4 * (g & 1)) * 4, qrd = L_RED + (tid & 3) * 576 + (tid >> 2) * 16;
  const int qpub = WS_QB + ((b * 8 + kvh) * 2 + kh) * 2048 + cg * 512 + tid * 4;
  q_gemv(wqf, qa, L, tid, lane, qwr, qrd, rs, qpub, tag);
  STAMP(2);
#undef GWIN
#define GWIN WD2
  EV(28, 15); KFRAG(1);
  G(); LDV(2); G(); LDV(3);
  EV(30, 19); KFRAG(2);
#define WAITN(n) asm volatile("s_waitcnt vmcnt(%0)" ::"n"(n) : "memory")
  G(); LDO(0); G(); LDV(4); G(); LDV(5);
  STAMP(3);
  u32 q0, q1;
  const int qo = WS_QB + (b * 8 + kvh) * 4096 + tid * 4;
  G(); asm volatile("buffer_load_dword %0, %1, %2, 0 offen sc1" : "=v"(q0) : "v"(qo), "s"(rs) : "memory");
  asm volatile("buffer_load_dword %0, %1, %2, 0 offen offset:2048 sc1" : "=v"(q1) : "v"(qo), "s"(rs) : "memory");
  WAITN(11); KFRAG(3);
  STAMP(4); STAMPW(56);
  G(); LDO(1); G(); LDV(6); G(); LDV(7); G(); LDO(2); G(); LDV(8); G(); LDV(9);
  WAITN(6);
  asm volatile("" : "+v"(q0), "+v"(q1)::"memory");
  STAMP(5); STAMPW(64);
  LDO(3); LDV(10); LDV(11); LDO(4); LDV(12); LDV(13);
  int budget = 100000;
  if (!__all((((q0 ^ tag) | (q1 ^ tag)) & TMASK) == 0)) {
    _Pragma("nounroll") do {
      asm volatile("buffer_load_dword %0, %2, %3, 0 offen sc1\n\tbuffer_load_dword %1, %2, %3, 0 offen offset:2048 sc1\n\ts_waitcnt vmcnt(0)" : "=&v"(q0), "=&v"(q1) : "v"(qo), "s"(rs) : "memory");
    } while (!__all((((q0 ^ tag) | (q1 ^ tag)) & TMASK) == 0) && --budget > 0);
  }
  STAMPV(19, budget);
  const int vbase = L_KV + (g >> 1) * 16384 + (8 * (g & 1) + (c >> 2)) * 256 + ((w ^ ((c >> 2) | ((g & 1) << 2))) * 32) + (c & 3) * 8;
  const int a_qwr = L_QL + (tid >> 7) * QSTR + (tid & 127) * 2, a_qrd = L_QL + (c & 3) * QSTR + g * 16;
  const int a_mwr = L_MB + ((lane & 3) * 8 + w) * 4, a_mrd = L_MB + (c & 3) * 32;
  const int a_pwr = L_PL + (c >> 2) * PSTR + ((2 * (w & 1) + (g >> 1)) * 16 + (w >> 1) * 4 + (c & 3)) * 16 + (g & 1) * 8;
  const int a_lwr = L_LB + (w * 16 + g * 4 + (c & 3)) * 4;
  f4 acc[4];
  float M = attn_scores(asf(q0 ^ tag) + asf(q1 ^ tag), kf, L, a_qwr, a_qrd, a_mwr, a_mrd, a_pwr, a_lwr, lane);
  STAMP(6);
#undef GWIN
#define GWIN WD3
  G(); LDO(5); G(); LDO(6); G(); LDO(7); G(); LDV(14); G(); LDV(15);
  WAITN(8);
  BAR();
  const int pub_o = ((b * 8 + kvh) * 8 + s) * 2048 + w * 256 + lane * 4, pub_s = ((b * 8 + kvh) * 8 + s) * 128 + (lane & 7) * 16;
  attn_publish_stats(M, L, w, lane, rs, WS_OS + pub_s, tag, T0);
#pragma unroll
  for (int u = 0; u < 4; ++u) acc[u] = f4{0.f, 0.f, 0.f, 0.f};
  pv_chunk(0, acc, L, vbase, lane);
  pv_chunk(1, acc, L, vbase, lane);
  pv_chunk(2, acc, L, vbase, lane);
  v4u pf3 = *(const LAS v4u*)(L + L_PL + 3 * PSTR + lane * 16);
  PIN1(pf3);
  SB();
  WAITN(0);
  STAMP(7); STAMPW(72);
  BAR();
  {
    v4u vf3[4];
#pragma unroll
    for (int u = 0; u < 4; ++u) vf3[u] = v_frag(3, u, L, vbase);
    SB();
#pragma unroll
    for (int u = 0; u < 4; ++u) acc[u] = mfma(pf3, vf3[u], acc[u]);
  }
  int po1 = WS_OB + pub_o;
  attn_publish(acc, lane, rs, po1, T0);
  STAMP(8);
  v4u wof[8];
  const float b2 = asf(b2raw << 16);
  int g_oo = (b * 8 + kvh) * 8 * 2048 + (lane >> 3) * 2048 + 2 * (w & 3) * 256 + (w >> 2) * 128 + (lane & 7) * 16;
  int g_so = (b * 8 + kvh) * 8 * 128 + (lane & 7) * 128 + (2 * (w >> 2) + ((lane >> 3) & 1)) * 16;
  int o_xw = L_SC2 + w * 1024 + (lane ^ (lane >> 4)) * 16, o_xr = L_SC2 + w * 1024 + (((c & 7) * 8 + 4 * (c >> 3) + g) ^ ((c & 7) >> 1)) * 16;
  int o_wr = L_RED + (2 * w + (g >> 1)) * 320 + (8 * (c & 7) + 4 * (g & 1)) * 4, o_rd = L_RED + (tid & 3) * 320 + (tid >> 2) * 16;
  int o_pub = WS_PB + (b * 8 + kvh) * 2048 + so_ * 256 + tid * 4;
  g_oo += WS_OB; g_so += WS_OS;
  PIN4(g_oo, g_so, o_wr, o_rd); PIN1(o_pub); PIN2(o_xw, o_xr);
  BAR();

  if (DLY_B) __builtin_amdgcn_s_sleep(DLY_B);
  o_gemv<true>([&]() { q_gemv1(wqf, v4u{0x3f803f80u, 0x3f803f80u, 0x3f803f80u, 0x3f803f80u}, L, lane, L_PL + w * 544 + (8 * c + 4 * (g & 1)) * 4); },
               [&]() { if (w == 2 || w == 3) q_gemv2<544>(L, lane, L_PL + (lane & 3) * 544 + ((w - 2) * 16 + (lane >> 2)) * 16, rs, qpub - WS_QB + WS_CS - 128 * 4, tag); }, wof, wor, L, tid, lane, rs, g_oo, g_so, o_xw, o_xr, o_wr, o_rd, o_pub, tag, T0, budget, w, 20);
  STAMP(10);
  u32 cs0, cs1;
  const int cso = WS_CS + (b * 8 + kvh) * 4096 + tid * 4;
  float mprev, csum;
  {
    const f4 r = mixer<DLY_CD>([&]() { cs0 = ld32s(rs, cso); cs1 = ld32s(rs, cso + 2048); }, xres, L, tid, w, lane, j, rs, WS_PB + b * 8 * 2048, tag, b2, budget, 22, mprev);
    cs0 ^= tag; cs1 ^= tag;
    if (!__all(((cs0 | cs1) & TMASK) == 0)) {
      do { CBAR(); cs0 = ld32s(rs, cso) ^ tag; cs1 = ld32s(rs, cso + 2048) ^ tag; } while (!__all(((cs0 | cs1) & TMASK) == 0) && --budget > 0);
    }
    csum = asf(cs0) + asf(cs1);
    PIN1(csum);
    if (tid < XBS / 16) { v4u o = {tagf(r[0], tag), tagf(r[1], tag), tagf(r[2], tag), tagf(r[3], tag)}; st128s(o, rs, WS_XB + (b * 64 + j) * XBS + tid * 16); }
  }
  STAMP(11);
  float rstd2, dmean;
  int qo2 = qo + 4 * 8 * 4096, b0 = a_qwr, b1 = a_qrd, b2_ = a_mwr, b3 = a_mrd, b4 = a_pwr, b5 = a_lwr, pr = L_PL + lane * 16, po2 = WS_OB + (1 << 19) + pub_o, ps2 = WS_OS + 32768 + pub_s;
  PIN4(qo2, b0, b1, b2_); PIN4(b3, b4, b5, pr); PIN2(po2, ps2);
  {
    int xo_ = WS_XB + (b * 64 + (tid >> 3)) * XBS + (tid & 7) * 4, ao = WS_XB + (b * 64 + 32 * kh + 4 * w + g) * XBS + (c & 1) * 16, qw = qwr, qr = qrd, qp = qpub + 4 * 8 * 4096;
    PIN4(xo_, ao, qw, qr); PIN1(qp);
    if (DLY_E) __builtin_amdgcn_s_sleep(DLY_E);
    v4u a0;
    u32 xv;
    for (;;) {
      CBAR();
      a0 = v4u{tag, tag, tag, tag};
      if (c < 2) a0 = ld128s(rs, ao);
      xv = ld32s(rs, xo_) ^ tag;
#pragma unroll
      for (int i = 0; i < 4; ++i) a0[i] ^= tag;
      if (__all((((a0[0] | a0[1] | a0[2]) | (a0[3] | xv)) & TMASK) == 0) || --budget <= 0) break;
    }
    STAMP(26); STAMPV(27, budget); STAMPW(120);
    const u32 p0 = cvtpk(asf(a0[0]) - mprev, asf(a0[1]) - mprev), p1 = cvtpk(asf(a0[2]) - mprev, asf(a0[3]) - mprev);
    const v4u qa2 = {rowbc<0>(p0), rowbc<0>(p1), rowbc<1>(p0), rowbc<1>(p1)};
    STAMP(12);
    q_gemv(wqf, qa2, L, tid, lane, qw, qr, rs, qp, tag);
    STAMP(13);
    if (DLY_A) __builtin_amdgcn_s_sleep(DLY_A);
    CBAR();
    q0 = ld32s(rs, qo2); q1 = ld32s(rs, qo2 + 2048);
    CBAR();
    xres = asf(xv);
    float mean, var;
    ln_stats(xres, L, w, lane, mean, var);
    rstd2 = __builtin_amdgcn_rsqf(var + EPS);
    dmean = (mean - mprev) * csum;
    PIN2(rstd2, dmean);
  }
  {
    v4u vf[16];
#pragma unroll
    for (int t = 0; t < 4; ++t)
#pragma unroll
      for (int u = 0; u < 4; ++u) vf[t * 4 + u] = v_frag(t, u, L, vbase);
    PIN8(vf); { v4u* v2 = vf + 8; PIN8(v2); }
    q0 ^= tag; q1 ^= tag;
    while (!__all(((q0 | q1) & TMASK) == 0) && --budget > 0) { CBAR(); q0 = ld32s(rs, qo2) ^ tag; q1 = ld32s(rs, qo2 + 2048) ^ tag; }
    STAMP(14); STAMPV(18, budget);
    M = attn_scores(rstd2 * ((asf(q0) + asf(q1)) - dmean), kf, L, b0, b1, b2_, b3, b4, b5, lane);
    BAR();
    v4u pf[4];
#pragma unroll
    for (int t = 0; t < 4; ++t) pf[t] = *(const LAS v4u*)(L + pr + t * PSTR);
    SB();
#pragma unroll
    for (int t = 0; t < 4; ++t)
#pragma unroll
      for (int u = 0; u < 4; ++u) acc[u] = mfma(pf[t], vf[t * 4 + u], t == 0 ? f4{0.f, 0.f, 0.f, 0.f} : acc[u]);
    attn_publish_stats(M, L, w, lane, rs, ps2, tag, T0);
    attn_publish(acc, lane, rs, po2, T0);
  }
  STAMP(15);
  g_oo += 1 << 19; g_so += 32768; o_pub += 65536;
  PIN2(g_oo, g_so); PIN1(o_pub);
  if (DLY_B2) __builtin_amdgcn_s_sleep(DLY_B2);
  o_gemv<false>([]() {}, []() {}, wof, wor, L, tid, lane, rs, g_oo, g_so, o_xw, o_xr, o_wr, o_rd, o_pub, tag, T0, budget, w, 28);
  STAMP(16);
  {
    const f4 r = mixer<DLY_CD2>([]() {}, xres, L, tid, w, lane, j, rs, WS_PB + 65536 + b * 8 * 2048, tag, b2, budget, 30, mprev);
    if (w == 0) {
      const u32 lo = cvtpk(r[0], r[1]), hi = cvtpk(r[2], r[3]);
      if (lane < 2) { v2u o = {lo, hi}; *(GAS v2u*)((GAS unsigned char*)p.out + (b * 512 + 8 * j + 4 * lane) * 2) = o; }
      if (lane == 0 && j == 0) __builtin_amdgcn_raw_buffer_store_b32(T0, rs, WS_EP + b * 256, 0, 16);
    }
  }
  STAMP(17); STAMPR(37);
}

extern "C" void kernel_launch(void* const* d_in, const int* in_sizes, int n_in, void* d_out, int out_size, void* d_ws, size_t ws_size, hipStream_t stream) {
  Params p;
  p.x = (const u16*)d_in[0]; p.K = (const unsigned char*)d_in[1]; p.V = (const unsigned char*)d_in[2];
  p.wq = (const unsigned char*)d_in[3]; p.wo = (const unsigned char*)d_in[4];
  p.w1 = (const u16*)d_in[6]; p.b1 = (const u16*)d_in[7]; p.w2 = (const u16*)d_in[8]; p.b2 = (const u16*)d_in[9];
  p.out = (u16*)d_out; p.ws = (unsigned char*)d_ws;
  hipLaunchKernelGGL(fused_decode_kernel, dim3(256), dim3(512), 0, stream, p);
}
```

```cpp
#include <hip/hip_runtime.h>

typedef unsigned int u32;
typedef unsigned short u16;
typedef unsigned long long u64;
typedef u32 v4u __attribute__((ext_vector_type(4)));
typedef u32 v2u __attribute__((ext_vector_type(2)));
typedef float f4 __attribute__((ext_vector_type(4)));
typedef float f2 __attribute__((ext_vector_type(2)));
typedef short s8v __attribute__((ext_vector_type(8)));
typedef short s4v __attribute__((ext_vector_type(4)));
typedef __bf16 bf2v __attribute__((ext_vector_type(2)));
#define LAS __attribute__((address_space(3)))
#define GAS __attribute__((address_space(1)))
#define DI __device__ __forceinline__

#define STAMPS 0
#define DLY_B 10
#define DLY_CD 8
#define DLY_E 16
#define DLY_A 6
#define STW 0
#define KVPOL " nt"
#define WPOL " nt"
#define PRETOUCH 1
#define OROT 4
#define XBS 128
#define BAR_B2 0
#define DLY_B2 13
#define DLY_CD2 7
#define RS_CD 0
#define RS_B 0
#define WD2 5
#define WD3 5
#define WD 5
#define CODE_KB 17

constexpr int SK = 16384, WIN = 4096;
constexpr float EPS = 1e-5f;
constexpr float SCL = 0.08838834764831845f * 1.4426950408889634f;
constexpr float GK0 = -2.0f * 0.7978845608028654f * 1.4426950408889634f;
constexpr float GK1 = GK0 * 0.044715f;

constexpr int WS_EP = 0;
constexpr int WS_QB = 4096;
constexpr int WS_CS = WS_QB + 2 * 4 * 8 * 2 * 2048;
constexpr int WS_OB = WS_CS + 4 * 8 * 2 * 2048;
constexpr int WS_OS = WS_OB + 2 * 4 * 8 * 8 * 2048;
constexpr int WS_PB = WS_OS + 2 * 4 * 8 * 8 * 128;
constexpr int WS_XB = WS_PB + 2 * 4 * 8 * 2048;
constexpr int WS_END = WS_XB + 4 * 64 * XBS;
constexpr int WS_STAMP = 4 << 20;

constexpr int L_KV = 0;
constexpr int L_PAR = 131072;
constexpr int PSTR = 1088;
constexpr int L_PL = L_PAR + 11264;
constexpr int QSTR = 288;
constexpr int L_RED = L_PL + 4 * PSTR;
constexpr int L_ST = L_RED + 5120;
constexpr int L_MB = L_ST + 64;
constexpr int L_LB = L_MB + 128;
constexpr int L_R2 = L_LB + 512;
constexpr int L_QL = L_R2 + 128;
constexpr int L_XL = L_QL + 4 * QSTR;
constexpr int L_SC2 = L_QL;
constexpr int L_STAMP = L_SC2 + 8192;
constexpr int L_END = L_STAMP + (STAMPS ? 1024 + 64 : 0);
static_assert(L_END <= 163840, "LDS");

struct Params {
  const u16* x; const unsigned char* K; const unsigned char* V; const unsigned char* wq; const unsigned char* wo;
  const u16* w1; const u16* b1; const u16* w2; const u16* b2; u16* out; unsigned char* ws;
};

template <int CTRL> DI float dpp(float x) {
  return __builtin_bit_cast(float, __builtin_amdgcn_update_dpp(0, __builtin_bit_cast(int, x), CTRL, 0xf, 0xf, true));
}
constexpr int XOR1 = 0xB1, XOR2 = 0x4E, HMIR = 0x141, MIR = 0x140, ROR4 = 0x124, ROR8 = 0x128;
DI float asf(u32 x) { return __builtin_bit_cast(float, x); }
DI u32 asu(float x) { return __builtin_bit_cast(u32, x); }
DI u32 cvtpk(float lo, float hi) { f2 f = {lo, hi}; return __builtin_bit_cast(u32, __builtin_convertvector(f, bf2v)); }
DI f4 mfma(v4u a, v4u b, f4 c) {
  return __builtin_amdgcn_mfma_f32_16x16x32_bf16(__builtin_bit_cast(s8v, a), __builtin_bit_cast(s8v, b), c, 0, 0, 0);
}
DI float fmx(float a, float b) { return __builtin_elementwise_maximum(a, b); }
DI float xrow_max(float x) {
  auto s = __builtin_amdgcn_permlane16_swap(asu(x), asu(x), false, false);
  x = fmx(asf(s[0]), asf(s[1]));
  auto t = __builtin_amdgcn_permlane32_swap(asu(x), asu(x), false, false);
  return fmx(asf(t[0]), asf(t[1]));
}
DI float quad_reduce4(f4 r, int lane) {
  float t0 = (lane & 1) ? r[1] : r[0], t1 = (lane & 1) ? r[0] : r[1];
  t0 += dpp<XOR1>(t1);
  float t2 = (lane & 1) ? r[3] : r[2], t3 = (lane & 1) ? r[2] : r[3];
  t2 += dpp<XOR1>(t3);
  float s0 = (lane & 2) ? t2 : t0; const float s1 = (lane & 2) ? t0 : t2;
  s0 += dpp<XOR2>(s1);
  return s0;
}
DI void st32s(u32 v, __amdgpu_buffer_rsrc_t rs, int off) { __builtin_amdgcn_raw_buffer_store_b32(v, rs, off, 0, 16); }
template <int N> DI u32 rowbc(u32 x) { return (u32)__builtin_amdgcn_update_dpp(0, (int)x, 0x150 + N, 0xf, 0xf, false); }
DI u32 xo(u32 a, u32 t, u32 acc) { return __builtin_amdgcn_bitop3_b32(a, t, acc, 0xBE); }
#define PIN1(a) asm volatile("" : "+v"(a))
#define PIN2(a, b) asm volatile("" : "+v"(a), "+v"(b))
#define PIN4(a, b, c, d) asm volatile("" : "+v"(a), "+v"(b), "+v"(c), "+v"(d))
#define PIN8(x) asm volatile("" : "+v"(x[0]), "+v"(x[1]), "+v"(x[2]), "+v"(x[3]), "+v"(x[4]), "+v"(x[5]), "+v"(x[6]), "+v"(x[7]))
DI float xrow_sum(float x) {
  auto s = __builtin_amdgcn_permlane16_swap(asu(x), asu(x), false, false);
  x = asf(s[0]) + asf(s[1]);
  auto t = __builtin_amdgcn_permlane32_swap(asu(x), asu(x), false, false);
  return asf(t[0]) + asf(t[1]);
}
#define BAR() asm volatile("s_waitcnt lgkmcnt(0)\n\ts_barrier" ::: "memory")
#define CBAR() asm volatile("" ::: "memory")
#define SB() __builtin_amdgcn_sched_barrier(0)

#define STAMP(i)
#define STAMPR(i)
#define STAMPV(i, v)
#define STAMPW(i)

constexpr u32 TMASK = 0x3ffu;
DI u32 tagf(float v, u32 tag) { return __builtin_amdgcn_bitop3_b32(asu(v) + 0x200u, TMASK, tag, 0xBA); }
DI float untag(u32 v) { return asf(v & ~TMASK); }

DI u32 ld32s(__amdgpu_buffer_rsrc_t rs, int off) { return __builtin_amdgcn_raw_buffer_load_b32(rs, off, 0, 16); }
DI v4u ld128s(__amdgpu_buffer_rsrc_t rs, int off) { return __builtin_amdgcn_raw_buffer_load_b128(rs, off, 0, 16); }
DI void st128s(v4u v, __amdgpu_buffer_rsrc_t rs, int off) { __builtin_amdgcn_raw_buffer_store_b128(v, rs, off, 0, 16); }

DI void ln_stats(float x, LAS unsigned char* L, int w, int lane, float& mean, float& var) {
  const float c = asf(__builtin_amdgcn_readfirstlane(asu(x)));
  const float d = x - c;
  float s1 = d, s2 = d * d;
  s1 += dpp<XOR1>(s1); s2 += dpp<XOR1>(s2);
  s1 += dpp<XOR2>(s1); s2 += dpp<XOR2>(s2);
  s1 += dpp<HMIR>(s1); s2 += dpp<HMIR>(s2);
  s1 += dpp<MIR>(s1); s2 += dpp<MIR>(s2);
  asm volatile("s_nop 1\n\tv_add_f32_dpp %0, %0, %0 row_bcast:15 row_mask:0xa bank_mask:0xf\n\tv_add_f32_dpp %1, %1, %1 row_bcast:15 row_mask:0xa bank_mask:0xf\n\t"
               "s_nop 0\n\tv_add_f32_dpp %0, %0, %0 row_bcast:31 row_mask:0xc bank_mask:0xf\n\tv_add_f32_dpp %1, %1, %1 row_bcast:31 row_mask:0xc bank_mask:0xf"
               : "+v"(s1), "+v"(s2));
  const f2 pv = {fmaf(s1, 1.0f / 64, c), fmaf(s1 * s1, -1.0f / 64, s2)};
  if (lane == 63) *(LAS f2*)(L + L_ST + w * 8) = pv;
  BAR();
  const f2 v = *(const LAS f2*)(L + L_ST + (lane & 7) * 8);
  float m = v.x;
  m += dpp<XOR1>(m); m += dpp<XOR2>(m); m += dpp<HMIR>(m);
  mean = m * 0.125f;
  const float dv = v.x - mean;
  float t = fmaf(64.0f * dv, dv, v.y);
  t += dpp<XOR1>(t); t += dpp<XOR2>(t); t += dpp<HMIR>(t);
  var = t * (1.0f / 512);
}

DI void transpose8(const v4u (&raw)[8], v4u (&fr)[8]) {
#pragma unroll
  for (int i = 0; i < 8; ++i) {
    const u32 sel = (i & 1) ? 0x07060302u : 0x05040100u;
#pragma unroll
    for (int m = 0; m < 4; ++m) fr[i][m] = __builtin_amdgcn_perm(raw[2 * m + 1][i >> 1], raw[2 * m][i >> 1], sel);
  }
}

DI v4u q_operand(LAS unsigned char* L, int xaddr, float mean, float var) {
  const f4 a = *(const LAS f4*)(L + xaddr), b = *(const LAS f4*)(L + xaddr + 16);
  const float rs_ = __builtin_amdgcn_rsqf(var + EPS), mr = -mean * rs_;
  return v4u{cvtpk(fmaf(a[0], rs_, mr), fmaf(a[1], rs_, mr)), cvtpk(fmaf(a[2], rs_, mr), fmaf(a[3], rs_, mr)),
             cvtpk(fmaf(b[0], rs_, mr), fmaf(b[1], rs_, mr)), cvtpk(fmaf(b[2], rs_, mr), fmaf(b[3], rs_, mr))};
}

DI void q_gemv1(const v4u (&wf)[8], v4u a, LAS unsigned char* L, int lane, int wraddr) {
  f4 d[8];
#pragma unroll
  for (int i = 0; i < 8; ++i) d[i] = mfma(a, wf[i], f4{0.f, 0.f, 0.f, 0.f});
  f4 v;
#pragma unroll
  for (int m = 0; m < 4; ++m) v[m] = (lane & 16) ? d[4 + m][0] : d[m][0];
  if (lane < 32) *(LAS f4*)(L + wraddr) = v;
}
template <int STR>
DI void q_gemv2(LAS unsigned char* L, int lane, int rdaddr, __amdgpu_buffer_rsrc_t rs, int qoff, u32 tag) {
  const f4 r = *(const LAS f4*)(L + rdaddr) + *(const LAS f4*)(L + rdaddr + 4 * STR);
  st32s(tagf(quad_reduce4(r, lane), tag), rs, qoff);
}
DI void q_gemv(const v4u (&wf)[8], v4u a, LAS unsigned char* L, int tid, int lane, int wraddr, int rdaddr, __amdgpu_buffer_rsrc_t rs, int qoff, u32 tag) {
  q_gemv1(wf, a, L, lane, wraddr);
  BAR();
  if (tid < 128) q_gemv2<576>(L, lane, rdaddr, rs, qoff, tag);
}

DI float attn_scores(float qv, const v4u (&kf)[16], LAS unsigned char* L, int qwr, int qrd, int mwr, int mrd, int pwr, int lwr, int lane) {
  *(LAS u16*)(L + qwr) = (u16)(cvtpk(qv, qv) & 0xffffu);
  BAR();
  v4u qf[4];
#pragma unroll
  for (int st = 0; st < 4; ++st) qf[st] = *(const LAS v4u*)(L + qrd + st * 64);
  SB();
  f4 d[4];
#pragma unroll
  for (int st = 0; st < 4; ++st)
#pragma unroll
    for (int t = 0; t < 4; ++t) d[t] = mfma(kf[t * 4 + st], qf[st], st == 0 ? f4{0.f, 0.f, 0.f, 0.f} : d[t]);
  float sc[4];
#pragma unroll
  for (int i = 0; i < 4; ++i) {
    const float a = (lane & 4) ? d[1][i] : d[0][i], bq = (lane & 4) ? d[3][i] : d[2][i];
    sc[i] = (lane & 8) ? bq : a;
  }
  float m = fmx(fmx(sc[0], sc[1]), fmx(sc[2], sc[3]));
  m = fmx(m, dpp<ROR4>(m)); m = fmx(m, dpp<ROR8>(m));
  m = xrow_max(m);
  if (lane < 4) *(LAS float*)(L + mwr) = m;
  BAR();
  const f4 m0 = *(const LAS f4*)(L + mrd), m1 = *(const LAS f4*)(L + mrd + 16);
  float M = fmx(fmx(fmx(m0[0], m0[1]), fmx(m0[2], m0[3])), fmx(fmx(m1[0], m1[1]), fmx(m1[2], m1[3])));
  M = asf(asu(M * SCL) & ~TMASK);
  float p[4];
#pragma unroll
  for (int i = 0; i < 4; ++i) p[i] = __builtin_amdgcn_exp2f(fmaf(sc[i], SCL, -M));
  v2u pk = {cvtpk(p[0], p[1]), cvtpk(p[2], p[3])};
  *(LAS v2u*)(L + pwr) = pk;
  float l = (p[0] + p[1]) + (p[2] + p[3]);
  l += dpp<ROR4>(l); l += dpp<ROR8>(l);
  if ((lane & 12) == 0) *(LAS float*)(L + lwr) = l;
  return M;
}

DI v4u v_frag(int t, int u, LAS unsigned char* L, int vbase) {
  const s4v lo = __builtin_amdgcn_ds_read_tr16_b64_v4i16((LAS s4v*)(L + vbase + u * 32768 + t * 4096));
  const s4v hi = __builtin_amdgcn_ds_read_tr16_b64_v4i16((LAS s4v*)(L + vbase + u * 32768 + t * 4096 + 1024));
  const v2u l2 = __builtin_bit_cast(v2u, lo), h2 = __builtin_bit_cast(v2u, hi);
  return v4u{l2[0], l2[1], h2[0], h2[1]};
}
DI void pv_chunk(int t, f4 (&acc)[4], LAS unsigned char* L, int vbase, int lane) {
  const v4u pf = *(const LAS v4u*)(L + L_PL + t * PSTR + lane * 16);
#pragma unroll
  for (int u = 0; u < 4; ++u) acc[u] = mfma(pf, v_frag(t, u, L, vbase), acc[u]);
}

DI void attn_publish_stats(float M, LAS unsigned char* L, int w, int lane, __amdgpu_buffer_rsrc_t rs, int saddr, u32 tag, u32 T0) {
  if (w == 7) {
    float l = *(const LAS float*)(L + L_LB + lane * 4) + *(const LAS float*)(L + L_LB + (lane + 64) * 4);
    l += dpp<ROR4>(l); l += dpp<ROR8>(l);
    l = xrow_sum(l);
    if (lane < 8) { v4u sv = {asu(M) | tag, tagf(l, tag), T0, T0}; st128s(sv, rs, saddr); }
  }
}
template <int RM> DI float rowput(float old, float x) {
  return __builtin_bit_cast(float, __builtin_amdgcn_update_dpp(__builtin_bit_cast(int, old), __builtin_bit_cast(int, x), 0xE4, RM, 0xf, false));
}
DI void attn_publish(const f4 (&acc)[4], int lane, __amdgpu_buffer_rsrc_t rs, int oaddr, u32 T0) {
  float y[4];
#pragma unroll
  for (int h = 0; h < 4; ++h) y[h] = rowput<0x8>(rowput<0x4>(rowput<0x2>(acc[0][h], acc[1][h]), acc[2][h]), acc[3][h]);
  auto s01 = __builtin_amdgcn_permlane16_swap(asu(y[0]), asu(y[1]), false, false);
  auto s23 = __builtin_amdgcn_permlane16_swap(asu(y[2]), asu(y[3]), false, false);
  const float z01 = asf(s01[0]) + asf(s01[1]), z23 = asf(s23[0]) + asf(s23[1]);
  auto sf = __builtin_amdgcn_permlane32_swap(asu(z01), asu(z23), false, false);
  const float v = asf(sf[0]) + asf(sf[1]);
  st32s(__builtin_amdgcn_perm(cvtpk(v, v), T0, 0x05040100u), rs, oaddr);
}

DI bool o_check(const v4u& x0, const v4u& x1, const v4u& sv, u32 T0) {
  u32 d = xo(x0[0], T0, 0u);
  d = xo(x0[1], T0, d); d = xo(x0[2], T0, d); d = xo(x0[3], T0, d);
  d = xo(x1[0], T0, d); d = xo(x1[1], T0, d); d = xo(x1[2], T0, d); d = xo(x1[3], T0, d);
  d = xo(sv[0], T0, d); d = xo(sv[1], T0, d);
  d = xo(sv[3], T0, xo(sv[2], T0, d & TMASK));
  return __all(d == 0);
}
template <bool FIRST, class Hook, class Hook2>
DI void o_gemv(Hook&& hook, Hook2&& hook2, v4u (&wf)[8], v4u (&wraw)[8], LAS unsigned char* L, int tid, int lane, __amdgpu_buffer_rsrc_t rs, int oo, int so, int xw, int xr, int wraddr, int rdaddr, int poff, u32 tag, u32 T0, int& budget, int w, int sb) {
  v4u x0, x1, sv = {T0, T0, T0, T0};
  if (sb == 28) STAMP(39);
  CBAR();
  x0 = ld128s(rs, oo); x1 = ld128s(rs, oo + 256);
  if (lane < 16) sv = ld128s(rs, so);
  if (FIRST) {
    asm volatile("" : "+v"(wraw[0]), "+v"(wraw[1]), "+v"(wraw[2]), "+v"(wraw[3]), "+v"(wraw[4]), "+v"(wraw[5]), "+v"(wraw[6]), "+v"(wraw[7])::"memory");
    STAMP(9); STAMPW(80);
    transpose8(wraw, wf);
    PIN8(wf);
    hook();
  }
  while (!o_check(x0, x1, sv, T0) && --budget > 0) {
    if (RS_B) __builtin_amdgcn_s_sleep(RS_B);
    CBAR();
    x0 = ld128s(rs, oo); x1 = ld128s(rs, oo + 256);
    if (lane < 16) sv = ld128s(rs, so);
  }
  STAMP(sb); STAMPV(sb + 1, budget); if (sb == 28) STAMPW(104);
  const v4u pk = {__builtin_amdgcn_perm(x0[1], x0[0], 0x07060302u), __builtin_amdgcn_perm(x0[3], x0[2], 0x07060302u),
                  __builtin_amdgcn_perm(x1[1], x1[0], 0x07060302u), __builtin_amdgcn_perm(x1[3], x1[2], 0x07060302u)};
  *(LAS v4u*)(L + xw) = pk;
  CBAR();
  const v4u a = *(const LAS v4u*)(L + xr);
  const float ms = untag(sv[0]), ls = untag(sv[1]);
  float M = ms;
  M = fmx(M, dpp<XOR1>(M)); M = fmx(M, dpp<XOR2>(M)); M = fmx(M, dpp<HMIR>(M));
  const float f = __builtin_amdgcn_exp2f(ms - M);
  float Ls = f * ls;
  Ls += dpp<XOR1>(Ls); Ls += dpp<XOR2>(Ls); Ls += dpp<HMIR>(Ls);
  const float cs = f * __builtin_amdgcn_rcpf(Ls);
  f4 d[8];
#pragma unroll
  for (int i = 0; i < 8; ++i) d[i] = mfma(a, wf[i], f4{0.f, 0.f, 0.f, 0.f});
  const int bp = (lane >> 4) * 16;
  const float cr0 = asf(__builtin_amdgcn_ds_bpermute(bp, asu(cs))), cr1 = asf(__builtin_amdgcn_ds_bpermute(bp + 4, asu(cs)));
  const float cr2 = asf(__builtin_amdgcn_ds_bpermute(bp + 8, asu(cs))), cr3 = asf(__builtin_amdgcn_ds_bpermute(bp + 12, asu(cs)));
  const f2 c01 = {cr0, cr1}, c23 = {cr2, cr3};
  float e[8];
#pragma unroll
  for (int i = 0; i < 8; ++i) { const f2 t = f2{d[i][2], d[i][3]} * c23 + f2{d[i][0], d[i][1]} * c01; e[i] = t[0] + t[1]; }
  f4 v;
#pragma unroll
  for (int m = 0; m < 4; ++m) { auto t = __builtin_amdgcn_permlane16_swap(asu(e[m]), asu(e[m + 4]), false, false); v[m] = asf(t[0]) + asf(t[1]); }
  if ((lane >> 5) == ((lane >> 3) & 1)) *(LAS f4*)(L + wraddr) = v;
  BAR();
  if (tid < 64) {
    const f4 r = (*(const LAS f4*)(L + rdaddr) + *(const LAS f4*)(L + rdaddr + 4 * 320)) + (*(const LAS f4*)(L + rdaddr + 8 * 320) + *(const LAS f4*)(L + rdaddr + 12 * 320));
    st32s(tagf(quad_reduce4(r, lane), tag), rs, poff);
  }
  hook2();
}

template <int DLY, class Hook>
DI f4 mixer(Hook&& hook, float& xres, LAS unsigned char* L, int tid, int w, int lane, int j, __amdgpu_buffer_rsrc_t rs, int pbase, u32 tag, float b2, int& budget, int sb, float& mean_out) {
  float pw1[7], pb1[7], pw2[7];
  const int fl = tid & 255, q = tid >> 8;
#pragma unroll
  for (int i = 0; i < 7; ++i) {
    pw1[i] = asf((u32) * (const LAS u16*)(L + L_PAR + (fl + 256 * i) * 2) << 16);
    pb1[i] = asf((u32) * (const LAS u16*)(L + L_PAR + 3584 + (fl + 256 * i) * 2) << 16);
    pw2[i] = asf((u32) * (const LAS u16*)(L + L_PAR + 7168 + (fl + 256 * i) * 2) << 16);
  }
#pragma unroll
  for (int i = 0; i < 7; ++i) asm volatile("" : "+v"(pw1[i]), "+v"(pb1[i]), "+v"(pw2[i]));
  int po = pbase + tid * 4, xa = L_XL + (8 * j + 4 * q) * 4, xw = L_XL + tid * 4;
  asm volatile("" : "+v"(po), "+v"(xa), "+v"(xw));
  if (DLY) __builtin_amdgcn_s_sleep(DLY);
  if (sb == 30) STAMP(35);
  u32 p[8];
  for (;;) {
    CBAR();
#pragma unroll
    for (int i = 0; i < 8; ++i) p[i] = ld32s(rs, po + i * 2048) ^ tag;
    const u32 d = (p[0] | p[1] | p[2]) | (p[3] | p[4] | p[5]) | (p[6] | p[7]);
    if (__all((d & TMASK) == 0) || --budget <= 0) break;
    if (RS_CD) __builtin_amdgcn_s_sleep(RS_CD);
  }
  STAMP(sb); STAMPV(sb + 1, budget); if (sb == 30) STAMPW(112);
  hook();
  const float x1 = xres + (((asf(p[0]) + asf(p[1])) + (asf(p[2]) + asf(p[3]))) + ((asf(p[4]) + asf(p[5])) + (asf(p[6]) + asf(p[7]))));
  xres = x1;
  *(LAS float*)(L + xw) = x1;
  float mean, var;
  ln_stats(x1, L, w, lane, mean, var);
  mean_out = mean;
  const f4 xv = *(const LAS f4*)(L + xa);
  const float r1 = __builtin_amdgcn_rsqf(var + EPS);
  const float r2 = __builtin_amdgcn_rsqf(var * r1 * r1 + EPS);
  const float rr = r1 * r2, mr = -mean * rr;
  STAMP(sb + 2); if (sb == 22) STAMPW(88);
  const f2 hA = {fmaf(xv[0], rr, mr), fmaf(xv[1], rr, mr)}, hB = {fmaf(xv[2], rr, mr), fmaf(xv[3], rr, mr)};
  f2 aA = {0.f, 0.f}, aB = {0.f, 0.f};
#pragma unroll
  for (int i = 0; i < 7; ++i) {
    const f2 w1 = {pw1[i], pw1[i]}, b1 = {pb1[i], pb1[i]}, w2 = {pw2[i], pw2[i]};
    const f2 k0 = {GK0, GK0}, k1 = {GK1, GK1}, one = {1.f, 1.f};
    {
      const f2 u = hA * w1 + b1, z = u * (u * u * k1 + k0);
      const f2 e = {__builtin_amdgcn_exp2f(z[0]), __builtin_amdgcn_exp2f(z[1])}, dd = e + one;
      const f2 rc = {__builtin_amdgcn_rcpf(dd[0]), __builtin_amdgcn_rcpf(dd[1])};
      aA += (u * rc) * w2;
    }
    {
      const f2 u = hB * w1 + b1, z = u * (u * u * k1 + k0);
      const f2 e = {__builtin_amdgcn_exp2f(z[0]), __builtin_amdgcn_exp2f(z[1])}, dd = e + one;
      const f2 rc = {__builtin_amdgcn_rcpf(dd[0]), __builtin_amdgcn_rcpf(dd[1])};
      aB += (u * rc) * w2;
    }
  }
  float t0 = (lane & 1) ? aA[1] : aA[0], t1 = (lane & 1) ? aA[0] : aA[1];
  t0 += dpp<XOR1>(t1);
  float t2 = (lane & 1) ? aB[1] : aB[0], t3 = (lane & 1) ? aB[0] : aB[1];
  t2 += dpp<XOR1>(t3);
  float s0 = (lane & 2) ? t2 : t0, s1 = (lane & 2) ? t0 : t2;
  s0 += dpp<XOR2>(s1);
  s0 += dpp<ROR4>(s0); s0 += dpp<ROR8>(s0);
  s0 = xrow_sum(s0);
  STAMP(sb + 3); if (sb == 22) STAMPW(96);
  if (lane < 4) *(LAS float*)(L + L_R2 + (w * 4 + lane) * 4) = s0;
  BAR();
  f4 res = {0.f, 0.f, 0.f, 0.f};
  if (w == 0) {
    const int qq = lane & 1;
    const f4 a = (*(const LAS f4*)(L + L_R2 + (qq * 4 + 0) * 16) + *(const LAS f4*)(L + L_R2 + (qq * 4 + 1) * 16)) +
                 (*(const LAS f4*)(L + L_R2 + (qq * 4 + 2) * 16) + *(const LAS f4*)(L + L_R2 + (qq * 4 + 3) * 16));
    const f4 xr = *(const LAS f4*)(L + L_XL + (8 * j + 4 * qq) * 4);
    res = xr + a + f4{b2, b2, b2, b2};
  }
  return res;
}

__global__ void __launch_bounds__(512, 2) fused_decode_kernel(Params p) {
  __shared__ __attribute__((aligned(1024))) unsigned char smem[L_END];
  LAS unsigned char* L = (LAS unsigned char*)smem;
  const int tid = threadIdx.x, lane = tid & 63, w = __builtin_amdgcn_readfirstlane(tid >> 6);
  const int bid = blockIdx.x;
  v4u dummy = {0u, 0u, 0u, 0u};
  {
    const int piece = (bid >> 3) * 8 + w;
    if (piece < CODE_KB) {
      u64 pc; asm volatile("s_getpc_b64 %0" : "=s"(pc));
      const u64 a = (pc & ~(u64)255) + (u64)(piece * 1024 + lane * 16);
      asm volatile("global_load_dwordx4 %0, %1, off" : "+v"(dummy) : "v"(a) : "memory");
    }
  }
  const int kvh = bid & 7, b = bid >> 6, s = (bid >> 3) & 7;
  const int j = kvh * 8 + s;
  const int g = lane >> 4, c = lane & 15;
  STAMPR(36); STAMP(0);

  const size_t slab = ((size_t)(b * 8 + kvh) * SK + (SK - WIN) + s * 512) * 256;
  const __amdgpu_buffer_rsrc_t rK = __builtin_amdgcn_make_buffer_rsrc((void*)(p.K + slab), 0, 512 * 256, 0x00020000);
  const __amdgpu_buffer_rsrc_t rV = __builtin_amdgcn_make_buffer_rsrc((void*)(p.V + slab), 0, 512 * 256, 0x00020000);
  const __amdgpu_buffer_rsrc_t rQ = __builtin_amdgcn_make_buffer_rsrc((void*)p.wq, 0, 512 * 4096 * 2, 0x00020000);
  const __amdgpu_buffer_rsrc_t rO = __builtin_amdgcn_make_buffer_rsrc((void*)p.wo, 0, 512 * 4096 * 2, 0x00020000);
  const __amdgpu_buffer_rsrc_t rs = __builtin_amdgcn_make_buffer_rsrc((void*)p.ws, 0, WS_END, 0x00020000);
  const __amdgpu_buffer_rsrc_t rX = __builtin_amdgcn_make_buffer_rsrc((void*)p.x, 0, 4096, 0x00020000);
  const u32 ldsb = (u32)(size_t)L;

  u32 ev, xraw, b2raw, dummy2 = 0;
  asm volatile("s_nop 4\n\tbuffer_load_dword %0, %1, %2, 0 offen sc1" : "=v"(ev) : "v"(WS_EP + b * 256), "s"(rs) : "memory");
  asm volatile("buffer_load_ushort %0, %1, %2, 0 offen" : "=v"(xraw) : "v"((b * 512 + tid) * 2), "s"(rX) : "memory");
  asm volatile("global_load_ushort %0, %1, %2" : "=v"(b2raw) : "v"(0), "s"(p.b2) : "memory");
  const int rot = __builtin_amdgcn_readfirstlane((int)(((u32)(bid * 8 + w) * 2654435761u) >> 28));
  const int cg = s >> 1, kh = s & 1;
  const int voq = ((256 * kh + 32 * w + 8 * g) * 4096 + kvh * 512 + 128 * cg + 8 * c) * 2;
  const int so_ = (s + OROT) & 7;
  const int voo = ((kvh * 512 + (2 * (w >> 2) + (c >> 3)) * 128 + 32 * (w & 3) + 4 * g) * 512 + 64 * so_ + 8 * (c & 7)) * 2;
  int vok[4], vov[2];
#pragma unroll
  for (int i = 0; i < 4; ++i) vok[i] = (64 * w + g) * 256 + ((c ^ (4 * i + g)) * 16);
#pragma unroll
  for (int i = 0; i < 2; ++i) vov[i] = (64 * w + g) * 256 + ((((c >> 1) ^ (g | (i << 2))) * 32) + (c & 1) * 16);
  v4u wqr[8], wor[8], kf[16];
#define LDQ(jj) asm volatile("buffer_load_dwordx4 %0, %1, %2, %3 offen" WPOL : "=v"(wqr[jj]) : "v"(voq), "s"(rQ), "s"((jj) * 8192) : "memory")
#define LDO(jj) asm volatile("buffer_load_dwordx4 %0, %1, %2, %3 offen" WPOL : "=v"(wor[jj]) : "v"(voo), "s"(rO), "s"((16 * ((jj) >> 2) + ((jj) & 3)) * 1024) : "memory")
#define LDK(i) asm volatile("s_mov_b32 m0, %0\n\ts_nop 0\n\tbuffer_load_dwordx4 %1, %2, %3 offen" KVPOL " lds" ::"s"(ldsb + L_KV + w * 16384 + (i) * 1024), "v"(vok[(i) & 3]), "s"(rK), "s"((((i) + rot) & 15) * 1024) : "memory")
#define LDV(i) asm volatile("s_mov_b32 m0, %0\n\ts_nop 0\n\tbuffer_load_dwordx4 %1, %2, %3 offen" KVPOL " lds" ::"s"(ldsb + L_KV + w * 16384 + (i) * 1024), "v"(vov[((i) >> 1) & 1]), "s"(rV), "s"((((i) + rot) & 15) * 1024) : "memory")
#define GW(d) asm volatile("s_waitcnt vmcnt(%0)" ::"n"((d) - 1) : "memory")
#define G() GW(GWIN)
#define GWIN WD
#define EV(n, i) asm volatile("s_waitcnt vmcnt(%0)" ::"n"((n) - (i) - 1) : "memory")
#define KFRAG(t)                                                                                                     \
  _Pragma("unroll") for (int st = 0; st < 4; ++st)                                                                   \
      kf[(t) * 4 + st] = *(const LAS v4u*)(L + L_KV + w * 16384 + (16 * (t) + c) * 256 + (((4 * st + g) ^ c) * 16)); \
  asm volatile("" : "+v"(kf[(t) * 4]), "+v"(kf[(t) * 4 + 1]), "+v"(kf[(t) * 4 + 2]), "+v"(kf[(t) * 4 + 3])::"memory")
  LDQ(0); LDK(0); LDQ(1); LDK(1); LDQ(2); LDK(2);
  STAMP(38);
  if (w == 7) {
    const int me = (b * 8 + kvh) * 8 + s;
    int a_ = lane < 16 ? WS_OB + me * 2048 + lane * 128
           : lane < 32 ? WS_OB + (1 << 19) + me * 2048 + (lane - 16) * 128
           : lane < 34 ? WS_OS + (lane - 32) * 32768 + me * 128
           : lane < 42 ? WS_QB + ((lane - 34) >> 2) * (4 * 8 * 4096) + ((b * 8 + kvh) * 2 + (s & 1)) * 2048 + (s >> 1) * 512 + ((lane - 34) & 3) * 128
           : lane < 46 ? WS_PB + ((lane - 42) >> 1) * 65536 + (b * 8 + kvh) * 2048 + ((s + OROT) & 7) * 256 + ((lane - 42) & 1) * 128
           : WS_XB + (b * 64 + j) * XBS;
    asm volatile("buffer_load_dword %0, %1, %2, 0 offen" : "+v"(dummy2) : "v"(a_), "s"(rs) : "memory");
  }
  G(); LDQ(3); G(); LDK(3);
  asm volatile("s_waitcnt vmcnt(8)" : "+v"(ev), "+v"(xraw), "+v"(b2raw)::"memory");
  const u32 T0 = __builtin_amdgcn_readfirstlane(ev) + 1u;
  const u32 tag = T0 & TMASK;
  float xres = asf(xraw << 16);
  const int xqa = L_XL + (256 * kh + 32 * w + 8 * g) * 4;
  v4u qa;
  {
    *(LAS float*)(L + L_XL + tid * 4) = xres;
    float mean, var;
    ln_stats(xres, L, w, lane, mean, var);
    qa = q_operand(L, xqa, mean, var);
    PIN1(qa);
  }
  G(); LDQ(4); G(); LDK(4); G(); LDQ(5); G(); LDK(5);
  G(); LDQ(6); G(); LDK(6); G(); LDQ(7); G(); LDK(7);
  EV(16, 7); STAMPW(48); KFRAG(0);
  G(); LDK(8); G(); LDK(9); G(); LDK(10); G(); LDK(11); G(); LDK(12);
  EV(21, 14);
  asm volatile("" : "+v"(wqr[0]), "+v"(wqr[1]), "+v"(wqr[2]), "+v"(wqr[3]), "+v"(wqr[4]), "+v"(wqr[5]), "+v"(wqr[6]), "+v"(wqr[7]), "+v"(dummy), "+v"(dummy2)::"memory");
  STAMP(1); STAMPW(40);
  LDK(13); LDK(14); LDK(15);
  {
#pragma unroll
    for (int k = 0; k < 2; ++k) {
      const int piece = k == 0 ? w : 8 + (w % 3);
      int gr = piece * 64 + lane; gr = gr < 672 ? gr : 671;
      const int arr = gr / 224, e = gr - arr * 224;
      const unsigned char* src = (const unsigned char*)(arr == 0 ? p.w1 : arr == 1 ? p.b1 : p.w2) + e * 16;
      asm volatile("s_mov_b32 m0, %0\n\ts_nop 0\n\tglobal_load_lds_dwordx4 %1, off" ::"s"(ldsb + L_PAR + piece * 1024), "v"(src) : "memory");
    }
  }
  LDV(0); LDV(1);
  v4u wqf[8];
  transpose8(wqr, wqf);
  const int qwr = L_RED + w * 576 + (8 * c + 4 * (g & 1)) * 4, qrd = L_RED + (tid & 3) * 576 + (tid >> 2) * 16;
  const int qpub = WS_QB + ((b * 8 + kvh) * 2 + kh) * 2048 + cg * 512 + tid * 4;
  q_gemv(wqf, qa, L, tid, lane, qwr, qrd, rs, qpub, tag);
  STAMP(2);
#undef GWIN
#define GWIN WD2
  EV(28, 15); KFRAG(1);
  G(); LDV(2); G(); LDV(3);
  EV(30, 19); KFRAG(2);
#define WAITN(n) asm volatile("s_waitcnt vmcnt(%0)" ::"n"(n) : "memory")
  G(); LDO(0); G(); LDV(4); G(); LDV(5);
  STAMP(3);
  u32 q0, q1;
  const int qo = WS_QB + (b * 8 + kvh) * 4096 + tid * 4;
  G(); asm volatile("buffer_load_dword %0, %1, %2, 0 offen sc1" : "=v"(q0) : "v"(qo), "s"(rs) : "memory");
  asm volatile("buffer_load_dword %0, %1, %2, 0 offen offset:2048 sc1" : "=v"(q1) : "v"(qo), "s"(rs) : "memory");
  WAITN(11); KFRAG(3);
  STAMP(4); STAMPW(56);
  G(); LDO(1); G(); LDV(6); G(); LDV(7); G(); LDO(2); G(); LDV(8); G(); LDV(9);
  WAITN(6);
  asm volatile("" : "+v"(q0), "+v"(q1)::"memory");
  STAMP(5); STAMPW(64);
  LDO(3); LDV(10); LDV(11); LDO(4); LDV(12); LDV(13);
  int budget = 100000;
  if (!__all((((q0 ^ tag) | (q1 ^ tag)) & TMASK) == 0)) {
    _Pragma("nounroll") do {
      asm volatile("buffer_load_dword %0, %2, %3, 0 offen sc1\n\tbuffer_load_dword %1, %2, %3, 0 offen offset:2048 sc1\n\ts_waitcnt vmcnt(0)" : "=&v"(q0), "=&v"(q1) : "v"(qo), "s"(rs) : "memory");
    } while (!__all((((q0 ^ tag) | (q1 ^ tag)) & TMASK) == 0) && --budget > 0);
  }
  STAMPV(19, budget);
  const int vbase = L_KV + (g >> 1) * 16384 + (8 * (g & 1) + (c >> 2)) * 256 + ((w ^ ((c >> 2) | ((g & 1) << 2))) * 32) + (c & 3) * 8;
  const int a_qwr = L_QL + (tid >> 7) * QSTR + (tid & 127) * 2, a_qrd = L_QL + (c & 3) * QSTR + g * 16;
  const int a_mwr = L_MB + ((lane & 3) * 8 + w) * 4, a_mrd = L_MB + (c & 3) * 32;
  const int a_pwr = L_PL + (c >> 2) * PSTR + ((2 * (w & 1) + (g >> 1)) * 16 + (w >> 1) * 4 + (c & 3)) * 16 + (g & 1) * 8;
  const int a_lwr = L_LB + (w * 16 + g * 4 + (c & 3)) * 4;
  f4 acc[4];
  float M = attn_scores(asf(q0 ^ tag) + asf(q1 ^ tag), kf, L, a_qwr, a_qrd, a_mwr, a_mrd, a_pwr, a_lwr, lane);
  STAMP(6);
#undef GWIN
#define GWIN WD3
  G(); LDO(5); G(); LDO(6); G(); LDO(7); G(); LDV(14); G(); LDV(15);
  WAITN(8);
  BAR();
  const int pub_o = ((b * 8 + kvh) * 8 + s) * 2048 + w * 256 + lane * 4, pub_s = ((b * 8 + kvh) * 8 + s) * 128 + (lane & 7) * 16;
  attn_publish_stats(M, L, w, lane, rs, WS_OS + pub_s, tag, T0);
#pragma unroll
  for (int u = 0; u < 4; ++u) acc[u] = f4{0.f, 0.f, 0.f, 0.f};
  pv_chunk(0, acc, L, vbase, lane);
  pv_chunk(1, acc, L, vbase, lane);
  pv_chunk(2, acc, L, vbase, lane);
  v4u pf3 = *(const LAS v4u*)(L + L_PL + 3 * PSTR + lane * 16);
  PIN1(pf3);
  SB();
  WAITN(0);
  STAMP(7); STAMPW(72);
  BAR();
  {
    v4u vf3[4];
#pragma unroll
    for (int u = 0; u < 4; ++u) vf3[u] = v_frag(3, u, L, vbase);
    SB();
#pragma unroll
    for (int u = 0; u < 4; ++u) acc[u] = mfma(pf3, vf3[u], acc[u]);
  }
  int po1 = WS_OB + pub_o;
  attn_publish(acc, lane, rs, po1, T0);
  STAMP(8);
  v4u wof[8];
  const float b2 = asf(b2raw << 16);
  int g_oo = (b * 8 + kvh) * 8 * 2048 + (lane >> 3) * 2048 + 2 * (w & 3) * 256 + (w >> 2) * 128 + (lane & 7) * 16;
  int g_so = (b * 8 + kvh) * 8 * 128 + (lane & 7) * 128 + (2 * (w >> 2) + ((lane >> 3) & 1)) * 16;
  int o_xw = L_SC2 + w * 1024 + (lane ^ (lane >> 4)) * 16, o_xr = L_SC2 + w * 1024 + (((c & 7) * 8 + 4 * (c >> 3) + g) ^ ((c & 7) >> 1)) * 16;
  int o_wr = L_RED + (2 * w + (g >> 1)) * 320 + (8 * (c & 7) + 4 * (g & 1)) * 4, o_rd = L_RED + (tid & 3) * 320 + (tid >> 2) * 16;
  int o_pub = WS_PB + (b * 8 + kvh) * 2048 + so_ * 256 + tid * 4;
  g_oo += WS_OB; g_so += WS_OS;
  PIN4(g_oo, g_so, o_wr, o_rd); PIN1(o_pub); PIN2(o_xw, o_xr);
  BAR();

  if (DLY_B) __builtin_amdgcn_s_sleep(DLY_B);
  o_gemv<true>([&]() { q_gemv1(wqf, v4u{0x3f803f80u, 0x3f803f80u, 0x3f803f80u, 0x3f803f80u}, L, lane, L_PL + w * 544 + (8 * c + 4 * (g & 1)) * 4); },
               [&]() { if (w == 2 || w == 3) q_gemv2<544>(L, lane, L_PL + (lane & 3) * 544 + ((w - 2) * 16 + (lane >> 2)) * 16, rs, qpub - WS_QB + WS_CS - 128 * 4, tag); }, wof, wor, L, tid, lane, rs, g_oo, g_so, o_xw, o_xr, o_wr, o_rd, o_pub, tag, T0, budget, w, 20);
  STAMP(10);
  u32 cs0, cs1;
  const int cso = WS_CS + (b * 8 + kvh) * 4096 + tid * 4;
  float mprev, csum;
  {
    const f4 r = mixer<DLY_CD>([&]() { cs0 = ld32s(rs, cso); cs1 = ld32s(rs, cso + 2048); }, xres, L, tid, w, lane, j, rs, WS_PB + b * 8 * 2048, tag, b2, budget, 22, mprev);
    cs0 ^= tag; cs1 ^= tag;
    if (!__all(((cs0 | cs1) & TMASK) == 0)) {
      do { CBAR(); cs0 = ld32s(rs, cso) ^ tag; cs1 = ld32s(rs, cso + 2048) ^ tag; } while (!__all(((cs0 | cs1) & TMASK) == 0) && --budget > 0);
    }
    csum = asf(cs0) + asf(cs1);
    PIN1(csum);
    if (tid < XBS / 16) { v4u o = {tagf(r[0], tag), tagf(r[1], tag), tagf(r[2], tag), tagf(r[3], tag)}; st128s(o, rs, WS_XB + (b * 64 + j) * XBS + tid * 16); }
  }
  STAMP(11);
  float rstd2, dmean;
  int qo2 = qo + 4 * 8 * 4096, b0 = a_qwr, b1 = a_qrd, b2_ = a_mwr, b3 = a_mrd, b4 = a_pwr, b5 = a_lwr, pr = L_PL + lane * 16, po2 = WS_OB + (1 << 19) + pub_o, ps2 = WS_OS + 32768 + pub_s;
  PIN4(qo2, b0, b1, b2_); PIN4(b3, b4, b5, pr); PIN2(po2, ps2);
  {
    int xo_ = WS_XB + (b * 64 + (tid >> 3)) * XBS + (tid & 7) * 4, ao = WS_XB + (b * 64 + 32 * kh + 4 * w + g) * XBS + (c & 1) * 16, qw = qwr, qr = qrd, qp = qpub + 4 * 8 * 4096;
    PIN4(xo_, ao, qw, qr); PIN1(qp);
    if (DLY_E) __builtin_amdgcn_s_sleep(DLY_E);
    v4u a0;
    u32 xv;
    for (;;) {
      CBAR();
      a0 = v4u{tag, tag, tag, tag};
      if (c < 2) a0 = ld128s(rs, ao);
      xv = ld32s(rs, xo_) ^ tag;
#pragma unroll
      for (int i = 0; i < 4; ++i) a0[i] ^= tag;
      if (__all((((a0[0] | a0[1] | a0[2]) | (a0[3] | xv)) & TMASK) == 0) || --budget <= 0) break;
    }
    STAMP(26); STAMPV(27, budget); STAMPW(120);
    const u32 p0 = cvtpk(asf(a0[0]) - mprev, asf(a0[1]) - mprev), p1 = cvtpk(asf(a0[2]) - mprev, asf(a0[3]) - mprev);
    const v4u qa2 = {rowbc<0>(p0), rowbc<0>(p1), rowbc<1>(p0), rowbc<1>(p1)};
    STAMP(12);
    q_gemv(wqf, qa2, L, tid, lane, qw, qr, rs, qp, tag);
    STAMP(13);
    if (DLY_A) __builtin_amdgcn_s_sleep(DLY_A);
    CBAR();
    q0 = ld32s(rs, qo2); q1 = ld32s(rs, qo2 + 2048);
    CBAR();
    xres = asf(xv);
    float mean, var;
    ln_stats(xres, L, w, lane, mean, var);
    rstd2 = __builtin_amdgcn_rsqf(var + EPS);
    dmean = (mean - mprev) * csum;
    PIN2(rstd2, dmean);
  }
  {
    v4u vf[16];
#pragma unroll
    for (int t = 0; t < 4; ++t)
#pragma unroll
      for (int u = 0; u < 4; ++u) vf[t * 4 + u] = v_frag(t, u, L, vbase);
    PIN8(vf); { v4u* v2 = vf + 8; PIN8(v2); }
    q0 ^= tag; q1 ^= tag;
    while (!__all(((q0 | q1) & TMASK) == 0) && --budget > 0) { CBAR(); q0 = ld32s(rs, qo2) ^ tag; q1 = ld32s(rs, qo2 + 2048) ^ tag; }
    STAMP(14); STAMPV(18, budget);
    M = attn_scores(rstd2 * ((asf(q0) + asf(q1)) - dmean), kf, L, b0, b1, b2_, b3, b4, b5, lane);
    BAR();
    v4u pf[4];
#pragma unroll
    for (int t = 0; t < 4; ++t) pf[t] = *(const LAS v4u*)(L + pr + t * PSTR);
    SB();
#pragma unroll
    for (int t = 0; t < 4; ++t)
#pragma unroll
      for (int u = 0; u < 4; ++u) acc[u] = mfma(pf[t], vf[t * 4 + u], t == 0 ? f4{0.f, 0.f, 0.f, 0.f} : acc[u]);
    attn_publish_stats(M, L, w, lane, rs, ps2, tag, T0);
    attn_publish(acc, lane, rs, po2, T0);
  }
  STAMP(15);
  g_oo += 1 << 19; g_so += 32768; o_pub += 65536;
  PIN2(g_oo, g_so); PIN1(o_pub);
  if (DLY_B2) __builtin_amdgcn_s_sleep(DLY_B2);
  o_gemv<false>([]() {}, []() {}, wof, wor, L, tid, lane, rs, g_oo, g_so, o_xw, o_xr, o_wr, o_rd, o_pub, tag, T0, budget, w, 28);
  STAMP(16);
  {
    const f4 r = mixer<DLY_CD2>([]() {}, xres, L, tid, w, lane, j, rs, WS_PB + 65536 + b * 8 * 2048, tag, b2, budget, 30, mprev);
    if (w == 0) {
      const u32 lo = cvtpk(r[0], r[1]), hi = cvtpk(r[2], r[3]);
      if (lane < 2) { v2u o = {lo, hi}; *(GAS v2u*)((GAS unsigned char*)p.out + (b * 512 + 8 * j + 4 * lane) * 2) = o; }
      if (lane == 0 && j == 0) __builtin_amdgcn_raw_buffer_store_b32(T0, rs, WS_EP + b * 256, 0, 16);
    }
  }
  STAMP(17); STAMPR(37);
}

extern "C" void kernel_launch(void* const* d_in, const int* in_sizes, int n_in, void* d_out, int out_size, void* d_ws, size_t ws_size, hipStream_t stream) {
  Params p;
  p.x = (const u16*)d_in[0]; p.K = (const unsigned char*)d_in[1]; p.V = (const unsigned char*)d_in[2];
  p.wq = (const unsigned char*)d_in[3]; p.wo = (const unsigned char*)d_in[4];
  p.w1 = (const u16*)d_in[6]; p.b1 = (const u16*)d_in[7]; p.w2 = (const u16*)d_in[8]; p.b2 = (const u16*)d_in[9];
  p.out = (u16*)d_out; p.ws = (unsigned char*)d_ws;
  hipLaunchKernelGGL(fused_decode_kernel, dim3(256), dim3(512), 0, stream, p);
}
```

```cpp
#include <hip/hip_runtime.h>

typedef unsigned int u32;
typedef unsigned short u16;
typedef unsigned long long u64;
typedef u32 v4u __attribute__((ext_vector_type(4)));
typedef u32 v2u __attribute__((ext_vector_type(2)));
typedef float f4 __attribute__((ext_vector_type(4)));
typedef float f2 __attribute__((ext_vector_type(2)));
typedef short s8v __attribute__((ext_vector_type(8)));
typedef short s4v __attribute__((ext_vector_type(4)));
typedef __bf16 bf2v __attribute__((ext_vector_type(2)));
#define LAS __attribute__((address_space(3)))
#define GAS __attribute__((address_space(1)))
#define DI __device__ __forceinline__

#define STAMPS 0
#define DLY_B 10
#define DLY_CD 8
#define DLY_E 16
#define DLY_A 6
#define STW 0
#define KVPOL " nt"
#define WPOL " nt"
#define PRETOUCH 1
#define OROT 4
#define XBS 128
#define BAR_B2 0
#define DLY_B2 13
#define DLY_CD2 7
#define RS_CD 0
#define RS_B 0
#define WD2 5
#define WD3 5
#define WD 5
#define CODE_KB 17

constexpr int SK = 16384, WIN = 4096;
constexpr float EPS = 1e-5f;
constexpr float SCL = 0.08838834764831845f * 1.4426950408889634f;
constexpr float GK0 = -2.0f * 0.7978845608028654f * 1.4426950408889634f;
constexpr float GK1 = GK0 * 0.044715f;

constexpr int WS_EP = 0;
constexpr int WS_QB = 4096;
constexpr int WS_CS = WS_QB + 2 * 4 * 8 * 2 * 2048;
constexpr int WS_OB = WS_CS + 4 * 8 * 2 * 2048;
constexpr int WS_OS = WS_OB + 2 * 4 * 8 * 8 * 2048;
constexpr int WS_PB = WS_OS + 2 * 4 * 8 * 8 * 128;
constexpr int WS_XB = WS_PB + 2 * 4 * 8 * 2048;
constexpr int WS_END = WS_XB + 4 * 64 * XBS;
constexpr int WS_STAMP = 4 << 20;

constexpr int L_KV = 0;
constexpr int L_PAR = 131072;
constexpr int PSTR = 1088;
constexpr int L_PL = L_PAR + 11264;
constexpr int QSTR = 288;
constexpr int L_RED = L_PL + 4 * PSTR;
constexpr int L_ST = L_RED + 5120;
constexpr int L_MB = L_ST + 64;
constexpr int L_LB = L_MB + 128;
constexpr int L_R2 = L_LB + 512;
constexpr int L_QL = L_R2 + 128;
constexpr int L_XL = L_QL + 4 * QSTR;
constexpr int L_SC2 = L_QL;
constexpr int L_STAMP = L_SC2 + 8192;
constexpr int L_END = L_STAMP + (STAMPS ? 1024 + 64 : 0);
static_assert(L_END <= 163840, "LDS");

struct Params {
  const u16* x; const unsigned char* K; const unsigned char* V; const unsigned char* wq; const unsigned char* wo;
  const u16* w1; const u16* b1; const u16* w2; const u16* b2; u16* out; unsigned char* ws;
};

template <int CTRL> DI float dpp(float x) {
  return __builtin_bit_cast(float, __builtin_amdgcn_update_dpp(0, __builtin_bit_cast(int, x), CTRL, 0xf, 0xf, true));
}
constexpr int XOR1 = 0xB1, XOR2 = 0x4E, HMIR = 0x141, MIR = 0x140, ROR4 = 0x124, ROR8 = 0x128;
DI float asf(u32 x) { return __builtin_bit_cast(float, x); }
DI u32 asu(float x) { return __builtin_bit_cast(u32, x); }
DI u32 cvtpk(float lo, float hi) { f2 f = {lo, hi}; return __builtin_bit_cast(u32, __builtin_convertvector(f, bf2v)); }
DI f4 mfma(v4u a, v4u b, f4 c) {
  return __builtin_amdgcn_mfma_f32_16x16x32_bf16(__builtin_bit_cast(s8v, a), __builtin_bit_cast(s8v, b), c, 0, 0, 0);
}
DI float fmx(float a, float b) { return __builtin_elementwise_maximum(a, b); }
DI float xrow_max(float x) {
  auto s = __builtin_amdgcn_permlane16_swap(asu(x), asu(x), false, false);
  x = fmx(asf(s[0]), asf(s[1]));
  auto t = __builtin_amdgcn_permlane32_swap(asu(x), asu(x), false, false);
  return fmx(asf(t[0]), asf(t[1]));
}
DI float quad_reduce4(f4 r, int lane) {
  float t0 = (lane & 1) ? r[1] : r[0], t1 = (lane & 1) ? r[0] : r[1];
  t0 += dpp<XOR1>(t1);
  float t2 = (lane & 1) ? r[3] : r[2], t3 = (lane & 1) ? r[2] : r[3];
  t2 += dpp<XOR1>(t3);
  float s0 = (lane & 2) ? t2 : t0; const float s1 = (lane & 2) ? t0 : t2;
  s0 += dpp<XOR2>(s1);
  return s0;
}
DI void st32s(u32 v, __amdgpu_buffer_rsrc_t rs, int off) { __builtin_amdgcn_raw_buffer_store_b32(v, rs, off, 0, 16); }
template <int N> DI u32 rowbc(u32 x) { return (u32)__builtin_amdgcn_update_dpp(0, (int)x, 0x150 + N, 0xf, 0xf, false); }
DI u32 xo(u32 a, u32 t, u32 acc) { return __builtin_amdgcn_bitop3_b32(a, t, acc, 0xBE); }
#define PIN1(a) asm volatile("" : "+v"(a))
#define PIN2(a, b) asm volatile("" : "+v"(a), "+v"(b))
#define PIN4(a, b, c, d) asm volatile("" : "+v"(a), "+v"(b), "+v"(c), "+v"(d))
#define PIN8(x) asm volatile("" : "+v"(x[0]), "+v"(x[1]), "+v"(x[2]), "+v"(x[3]), "+v"(x[4]), "+v"(x[5]), "+v"(x[6]), "+v"(x[7]))
DI float xrow_sum(float x) {
  auto s = __builtin_amdgcn_permlane16_swap(asu(x), asu(x), false, false);
  x = asf(s[0]) + asf(s[1]);
  auto t = __builtin_amdgcn_permlane32_swap(asu(x), asu(x), false, false);
  return asf(t[0]) + asf(t[1]);
}
#define BAR() asm volatile("s_waitcnt lgkmcnt(0)\n\ts_barrier" ::: "memory")
#define CBAR() asm volatile("" ::: "memory")
#define SB() __builtin_amdgcn_sched_barrier(0)

#define STAMP(i)
#define STAMPR(i)
#define STAMPV(i, v)
#define STAMPW(i)

constexpr u32 TMASK = 0x3ffu;
DI u32 tagf(float v, u32 tag) { return __builtin_amdgcn_bitop3_b32(asu(v) + 0x200u, TMASK, tag, 0xBA); }
DI float untag(u32 v) { return asf(v & ~TMASK); }

DI u32 ld32s(__amdgpu_buffer_rsrc_t rs, int off) { return __builtin_amdgcn_raw_buffer_load_b32(rs, off, 0, 16); }
DI v4u ld128s(__amdgpu_buffer_rsrc_t rs, int off) { return __builtin_amdgcn_raw_buffer_load_b128(rs, off, 0, 16); }
DI void st128s(v4u v, __amdgpu_buffer_rsrc_t rs, int off) { __builtin_amdgcn_raw_buffer_store_b128(v, rs, off, 0, 16); }

DI void ln_stats(float x, LAS unsigned char* L, int w, int lane, float& mean, float& var) {
  const float c = asf(__builtin_amdgcn_readfirstlane(asu(x)));
  const float d = x - c;
  float s1 = d, s2 = d * d;
  s1 += dpp<XOR1>(s1); s2 += dpp<XOR1>(s2);
  s1 += dpp<XOR2>(s1); s2 += dpp<XOR2>(s2);
  s1 += dpp<HMIR>(s1); s2 += dpp<HMIR>(s2);
  s1 += dpp<MIR>(s1); s2 += dpp<MIR>(s2);
  asm volatile("s_nop 1\n\tv_add_f32_dpp %0, %0, %0 row_bcast:15 row_mask:0xa bank_mask:0xf\n\tv_add_f32_dpp %1, %1, %1 row_bcast:15 row_mask:0xa bank_mask:0xf\n\t"
               "s_nop 0\n\tv_add_f32_dpp %0, %0, %0 row_bcast:31 row_mask:0xc bank_mask:0xf\n\tv_add_f32_dpp %1, %1, %1 row_bcast:31 row_mask:0xc bank_mask:0xf"
               : "+v"(s1), "+v"(s2));
  const f2 pv = {fmaf(s1, 1.0f / 64, c), fmaf(s1 * s1, -1.0f / 64, s2)};
  if (lane == 63) *(LAS f2*)(L + L_ST + w * 8) = pv;
  BAR();
  const f2 v = *(const LAS f2*)(L + L_ST + (lane & 7) * 8);
  float m = v.x;
  m += dpp<XOR1>(m); m += dpp<XOR2>(m); m += dpp<HMIR>(m);
  mean = m * 0.125f;
  const float dv = v.x - mean;
  float t = fmaf(64.0f * dv, dv, v.y);
  t += dpp<XOR1>(t); t += dpp<XOR2>(t); t += dpp<HMIR>(t);
  var = t * (1.0f / 512);
}

DI void transpose8(const v4u (&raw)[8], v4u (&fr)[8]) {
#pragma unroll
  for (int i = 0; i < 8; ++i) {
    const u32 sel = (i & 1) ? 0x07060302u : 0x05040100u;
#pragma unroll
    for (int m = 0; m < 4; ++m) fr[i][m] = __builtin_amdgcn_perm(raw[2 * m + 1][i >> 1], raw[2 * m][i >> 1], sel);
  }
}

DI v4u q_operand(LAS unsigned char* L, int xaddr, float mean, float var) {
  const f4 a = *(const LAS f4*)(L + xaddr), b = *(const LAS f4*)(L + xaddr + 16);
  const float rs_ = __builtin_amdgcn_rsqf(var + EPS), mr = -mean * rs_;
  return v4u{cvtpk(fmaf(a[0], rs_, mr), fmaf(a[1], rs_, mr)), cvtpk(fmaf(a[2], rs_, mr), fmaf(a[3], rs_, mr)),
             cvtpk(fmaf(b[0], rs_, mr), fmaf(b[1], rs_, mr)), cvtpk(fmaf(b[2], rs_, mr), fmaf(b[3], rs_, mr))};
}

DI void q_gemv1(const v4u (&wf)[8], v4u a, LAS unsigned char* L, int lane, int wraddr) {
  f4 d[8];
#pragma unroll
  for (int i = 0; i < 8; ++i) d[i] = mfma(a, wf[i], f4{0.f, 0.f, 0.f, 0.f});
  f4 v;
#pragma unroll
  for (int m = 0; m < 4; ++m) v[m] = (lane & 16) ? d[4 + m][0] : d[m][0];
  if (lane < 32) *(LAS f4*)(L + wraddr) = v;
}
template <int STR>
DI void q_gemv2(LAS unsigned char* L, int lane, int rdaddr, __amdgpu_buffer_rsrc_t rs, int qoff, u32 tag) {
  const f4 r = *(const LAS f4*)(L + rdaddr) + *(const LAS f4*)(L + rdaddr + 4 * STR);
  st32s(tagf(quad_reduce4(r, lane), tag), rs, qoff);
}
DI void q_gemv(const v4u (&wf)[8], v4u a, LAS unsigned char* L, int tid, int lane, int wraddr, int rdaddr, __amdgpu_buffer_rsrc_t rs, int qoff, u32 tag) {
  q_gemv1(wf, a, L, lane, wraddr);
  BAR();
  if (tid < 128) q_gemv2<576>(L, lane, rdaddr, rs, qoff, tag);
}

DI float attn_scores(float qv, const v4u (&kf)[16], LAS unsigned char* L, int qwr, int qrd, int mwr, int mrd, int pwr, int lwr, int lane) {
  *(LAS u16*)(L + qwr) = (u16)(cvtpk(qv, qv) & 0xffffu);
  BAR();
  v4u qf[4];
#pragma unroll
  for (int st = 0; st < 4; ++st) qf[st] = *(const LAS v4u*)(L + qrd + st * 64);
  SB();
  f4 d[4];
#pragma unroll
  for (int st = 0; st < 4; ++st)
#pragma unroll
    for (int t = 0; t < 4; ++t) d[t] = mfma(kf[t * 4 + st], qf[st], st == 0 ? f4{0.f, 0.f, 0.f, 0.f} : d[t]);
  float sc[4];
#pragma unroll
  for (int i = 0; i < 4; ++i) {
    const float a = (lane & 4) ? d[1][i] : d[0][i], bq = (lane & 4) ? d[3][i] : d[2][i];
    sc[i] = (lane & 8) ? bq : a;
  }
  float m = fmx(fmx(sc[0], sc[1]), fmx(sc[2], sc[3]));
  m = fmx(m, dpp<ROR4>(m)); m = fmx(m, dpp<ROR8>(m));
  m = xrow_max(m);
  if (lane < 4) *(LAS float*)(L + mwr) = m;
  BAR();
  const f4 m0 = *(const LAS f4*)(L + mrd), m1 = *(const LAS f4*)(L + mrd + 16);
  float M = fmx(fmx(fmx(m0[0], m0[1]), fmx(m0[2], m0[3])), fmx(fmx(m1[0], m1[1]), fmx(m1[2], m1[3])));
  M = asf(asu(M * SCL) & ~TMASK);
  float p[4];
#pragma unroll
  for (int i = 0; i < 4; ++i) p[i] = __builtin_amdgcn_exp2f(fmaf(sc[i], SCL, -M));
  v2u pk = {cvtpk(p[0], p[1]), cvtpk(p[2], p[3])};
  *(LAS v2u*)(L + pwr) = pk;
  float l = (p[0] + p[1]) + (p[2] + p[3]);
  l += dpp<ROR4>(l); l += dpp<ROR8>(l);
  if ((lane & 12) == 0) *(LAS float*)(L + lwr) = l;
  return M;
}

DI v4u v_frag(int t, int u, LAS unsigned char* L, int vbase) {
  const s4v lo = __builtin_amdgcn_ds_read_tr16_b64_v4i16((LAS s4v*)(L + vbase + u * 32768 + t * 4096));
  const s4v hi = __builtin_amdgcn_ds_read_tr16_b64_v4i16((LAS s4v*)(L + vbase + u * 32768 + t * 4096 + 1024));
  const v2u l2 = __builtin_bit_cast(v2u, lo), h2 = __builtin_bit_cast(v2u, hi);
  return v4u{l2[0], l2[1], h2[0], h2[1]};
}
DI void pv_chunk(int t, f4 (&acc)[4], LAS unsigned char* L, int vbase, int lane) {
  const v4u pf = *(const LAS v4u*)(L + L_PL + t * PSTR + lane * 16);
#pragma unroll
  for (int u = 0; u < 4; ++u) acc[u] = mfma(pf, v_frag(t, u, L, vbase), acc[u]);
}

DI void attn_publish_stats(float M, LAS unsigned char* L, int w, int lane, __amdgpu_buffer_rsrc_t rs, int saddr, u32 tag, u32 T0) {
  if (w == 7) {
    float l = *(const LAS float*)(L + L_LB + lane * 4) + *(const LAS float*)(L + L_LB + (lane + 64) * 4);
    l += dpp<ROR4>(l); l += dpp<ROR8>(l);
    l = xrow_sum(l);
    if (lane < 8) { v4u sv = {asu(M) | tag, tagf(l, tag), T0, T0}; st128s(sv, rs, saddr); }
  }
}
template <int RM> DI float rowput(float old, float x) {
  return __builtin_bit_cast(float, __builtin_amdgcn_update_dpp(__builtin_bit_cast(int, old), __builtin_bit_cast(int, x), 0xE4, RM, 0xf, false));
}
DI void attn_publish(const f4 (&acc)[4], int lane, __amdgpu_buffer_rsrc_t rs, int oaddr, u32 T0) {
  float y[4];
#pragma unroll
  for (int h = 0; h < 4; ++h) y[h] = rowput<0x8>(rowput<0x4>(rowput<0x2>(acc[0][h], acc[1][h]), acc[2][h]), acc[3][h]);
  auto s01 = __builtin_amdgcn_permlane16_swap(asu(y[0]), asu(y[1]), false, false);
  auto s23 = __builtin_amdgcn_permlane16_swap(asu(y[2]), asu(y[3]), false, false);
  const float z01 = asf(s01[0]) + asf(s01[1]), z23 = asf(s23[0]) + asf(s23[1]);
  auto sf = __builtin_amdgcn_permlane32_swap(asu(z01), asu(z23), false, false);
  const float v = asf(sf[0]) + asf(sf[1]);
  st32s(__builtin_amdgcn_perm(cvtpk(v, v), T0, 0x05040100u), rs, oaddr);
}

DI bool o_check(const v4u& x0, const v4u& x1, const v4u& sv, u32 T0) {
  u32 d = xo(x0[0], T0, 0u);
  d = xo(x0[1], T0, d); d = xo(x0[2], T0, d); d = xo(x0[3], T0, d);
  d = xo(x1[0], T0, d); d = xo(x1[1], T0, d); d = xo(x1[2], T0, d); d = xo(x1[3], T0, d);
  d = xo(sv[0], T0, d); d = xo(sv[1], T0, d);
  d = xo(sv[3], T0, xo(sv[2], T0, d & TMASK));
  return __all(d == 0);
}
template <bool FIRST, class Hook, class Hook2>
DI void o_gemv(Hook&& hook, Hook2&& hook2, v4u (&wf)[8], v4u (&wraw)[8], LAS unsigned char* L, int tid, int lane, __amdgpu_buffer_rsrc_t rs, int oo, int so, int xw, int xr, int wraddr, int rdaddr, int poff, u32 tag, u32 T0, int& budget, int w, int sb) {
  v4u x0, x1, sv = {T0, T0, T0, T0};
  if (sb == 28) STAMP(39);
  CBAR();
  x0 = ld128s(rs, oo); x1 = ld128s(rs, oo + 256);
  if (lane < 16) sv = ld128s(rs, so);
  if (FIRST) {
    asm volatile("" : "+v"(wraw[0]), "+v"(wraw[1]), "+v"(wraw[2]), "+v"(wraw[3]), "+v"(wraw[4]), "+v"(wraw[5]), "+v"(wraw[6]), "+v"(wraw[7])::"memory");
    STAMP(9); STAMPW(80);
    transpose8(wraw, wf);
    PIN8(wf);
    hook();
  }
  while (!o_check(x0, x1, sv, T0) && --budget > 0) {
    if (RS_B) __builtin_amdgcn_s_sleep(RS_B);
    CBAR();
    x0 = ld128s(rs, oo); x1 = ld128s(rs, oo + 256);
    if (lane < 16) sv = ld128s(rs, so);
  }
  STAMP(sb); STAMPV(sb + 1, budget); if (sb == 28) STAMPW(104);
  const v4u pk = {__builtin_amdgcn_perm(x0[1], x0[0], 0x07060302u), __builtin_amdgcn_perm(x0[3], x0[2], 0x07060302u),
                  __builtin_amdgcn_perm(x1[1], x1[0], 0x07060302u), __builtin_amdgcn_perm(x1[3], x1[2], 0x07060302u)};
  *(LAS v4u*)(L + xw) = pk;
  CBAR();
  const v4u a = *(const LAS v4u*)(L + xr);
  const float ms = untag(sv[0]), ls = untag(sv[1]);
  float M = ms;
  M = fmx(M, dpp<XOR1>(M)); M = fmx(M, dpp<XOR2>(M)); M = fmx(M, dpp<HMIR>(M));
  const float f = __builtin_amdgcn_exp2f(ms - M);
  float Ls = f * ls;
  Ls += dpp<XOR1>(Ls); Ls += dpp<XOR2>(Ls); Ls += dpp<HMIR>(Ls);
  const float cs = f * __builtin_amdgcn_rcpf(Ls);
  f4 d[8];
#pragma unroll
  for (int i = 0; i < 8; ++i) d[i] = mfma(a, wf[i], f4{0.f, 0.f, 0.f, 0.f});
  const int bp = (lane >> 4) * 16;
  const float cr0 = asf(__builtin_amdgcn_ds_bpermute(bp, asu(cs))), cr1 = asf(__builtin_amdgcn_ds_bpermute(bp + 4, asu(cs)));
  const float cr2 = asf(__builtin_amdgcn_ds_bpermute(bp + 8, asu(cs))), cr3 = asf(__builtin_amdgcn_ds_bpermute(bp + 12, asu(cs)));
  const f2 c01 = {cr0, cr1}, c23 = {cr2, cr3};
  float e[8];
#pragma unroll
  for (int i = 0; i < 8; ++i) { const f2 t = f2{d[i][2], d[i][3]} * c23 + f2{d[i][0], d[i][1]} * c01; e[i] = t[0] + t[1]; }
  f4 v;
#pragma unroll
  for (int m = 0; m < 4; ++m) { auto t = __builtin_amdgcn_permlane16_swap(asu(e[m]), asu(e[m + 4]), false, false); v[m] = asf(t[0]) + asf(t[1]); }
  if ((lane >> 5) == ((lane >> 3) & 1)) *(LAS f4*)(L + wraddr) = v;
  BAR();
  if (tid < 64) {
    const f4 r = (*(const LAS f4*)(L + rdaddr) + *(const LAS f4*)(L + rdaddr + 4 * 320)) + (*(const LAS f4*)(L + rdaddr + 8 * 320) + *(const LAS f4*)(L + rdaddr + 12 * 320));
    st32s(tagf(quad_reduce4(r, lane), tag), rs, poff);
  }
  hook2();
}

template <int DLY, class Hook>
DI f4 mixer(Hook&& hook, float& xres, LAS unsigned char* L, int tid, int w, int lane, int j, __amdgpu_buffer_rsrc_t rs, int pbase, u32 tag, float b2, int& budget, int sb, float& mean_out) {
  float pw1[7], pb1[7], pw2[7];
  const int fl = tid & 255, q = tid >> 8;
#pragma unroll
  for (int i = 0; i < 7; ++i) {
    pw1[i] = asf((u32) * (const LAS u16*)(L + L_PAR + (fl + 256 * i) * 2) << 16);
    pb1[i] = asf((u32) * (const LAS u16*)(L + L_PAR + 3584 + (fl + 256 * i) * 2) << 16);
    pw2[i] = asf((u32) * (const LAS u16*)(L + L_PAR + 7168 + (fl + 256 * i) * 2) << 16);
  }
#pragma unroll
  for (int i = 0; i < 7; ++i) asm volatile("" : "+v"(pw1[i]), "+v"(pb1[i]), "+v"(pw2[i]));
  int po = pbase + tid * 4, xa = L_XL + (8 * j + 4 * q) * 4, xw = L_XL + tid * 4;
  asm volatile("" : "+v"(po), "+v"(xa), "+v"(xw));
  if (DLY) __builtin_amdgcn_s_sleep(DLY);
  if (sb == 30) STAMP(35);
  u32 p[8];
  for (;;) {
    CBAR();
#pragma unroll
    for (int i = 0; i < 8; ++i) p[i] = ld32s(rs, po + i * 2048) ^ tag;
    const u32 d = (p[0] | p[1] | p[2]) | (p[3] | p[4] | p[5]) | (p[6] | p[7]);
    if (__all((d & TMASK) == 0) || --budget <= 0) break;
    if (RS_CD) __builtin_amdgcn_s_sleep(RS_CD);
  }
  STAMP(sb); STAMPV(sb + 1, budget); if (sb == 30) STAMPW(112);
  hook();
  const float x1 = xres + (((asf(p[0]) + asf(p[1])) + (asf(p[2]) + asf(p[3]))) + ((asf(p[4]) + asf(p[5])) + (asf(p[6]) + asf(p[7]))));
  xres = x1;
  *(LAS float*)(L + xw) = x1;
  float mean, var;
  ln_stats(x1, L, w, lane, mean, var);
  mean_out = mean;
  const f4 xv = *(const LAS f4*)(L + xa);
  const float r1 = __builtin_amdgcn_rsqf(var + EPS);
  const float r2 = __builtin_amdgcn_rsqf(var * r1 * r1 + EPS);
  const float rr = r1 * r2, mr = -mean * rr;
  STAMP(sb + 2); if (sb == 22) STAMPW(88);
  const f2 hA = {fmaf(xv[0], rr, mr), fmaf(xv[1], rr, mr)}, hB = {fmaf(xv[2], rr, mr), fmaf(xv[3], rr, mr)};
  f2 aA = {0.f, 0.f}, aB = {0.f, 0.f};
#pragma unroll
  for (int i = 0; i < 7; ++i) {
    const f2 w1 = {pw1[i], pw1[i]}, b1 = {pb1[i], pb1[i]}, w2 = {pw2[i], pw2[i]};
    const f2 k0 = {GK0, GK0}, k1 = {GK1, GK1}, one = {1.f, 1.f};
    {
      const f2 u = hA * w1 + b1, z = u * (u * u * k1 + k0);
      const f2 e = {__builtin_amdgcn_exp2f(z[0]), __builtin_amdgcn_exp2f(z[1])}, dd = e + one;
      const f2 rc = {__builtin_amdgcn_rcpf(dd[0]), __builtin_amdgcn_rcpf(dd[1])};
      aA += (u * rc) * w2;
    }
    {
      const f2 u = hB * w1 + b1, z = u * (u * u * k1 + k0);
      const f2 e = {__builtin_amdgcn_exp2f(z[0]), __builtin_amdgcn_exp2f(z[1])}, dd = e + one;
      const f2 rc = {__builtin_amdgcn_rcpf(dd[0]), __builtin_amdgcn_rcpf(dd[1])};
      aB += (u * rc) * w2;
    }
  }
  float t0 = (lane & 1) ? aA[1] : aA[0], t1 = (lane & 1) ? aA[0] : aA[1];
  t0 += dpp<XOR1>(t1);
  float t2 = (lane & 1) ? aB[1] : aB[0], t3 = (lane & 1) ? aB[0] : aB[1];
  t2 += dpp<XOR1>(t3);
  float s0 = (lane & 2) ? t2 : t0, s1 = (lane & 2) ? t0 : t2;
  s0 += dpp<XOR2>(s1);
  s0 += dpp<ROR4>(s0); s0 += dpp<ROR8>(s0);
  s0 = xrow_sum(s0);
  STAMP(sb + 3); if (sb == 22) STAMPW(96);
  if (lane < 4) *(LAS float*)(L + L_R2 + (w * 4 + lane) * 4) = s0;
  BAR();
  f4 res = {0.f, 0.f, 0.f, 0.f};
  if (w == 0) {
    const int qq = lane & 1;
    const f4 a = (*(const LAS f4*)(L + L_R2 + (qq * 4 + 0) * 16) + *(const LAS f4*)(L + L_R2 + (qq * 4 + 1) * 16)) +
                 (*(const LAS f4*)(L + L_R2 + (qq * 4 + 2) * 16) + *(const LAS f4*)(L + L_R2 + (qq * 4 + 3) * 16));
    const f4 xr = *(const LAS f4*)(L + L_XL + (8 * j + 4 * qq) * 4);
    res = xr + a + f4{b2, b2, b2, b2};
  }
  return res;
}

__global__ void __launch_bounds__(512, 2) fused_decode_kernel(Params p) {
  __shared__ __attribute__((aligned(1024))) unsigned char smem[L_END];
  LAS unsigned char* L = (LAS unsigned char*)smem;
  const int tid = threadIdx.x, lane = tid & 63, w = __builtin_amdgcn_readfirstlane(tid >> 6);
  const int bid = blockIdx.x;
  v4u dummy = {0u, 0u, 0u, 0u};
  {
    const int piece = (bid >> 3) * 8 + w;
    if (piece < CODE_KB) {
      u64 pc; asm volatile("s_getpc_b64 %0" : "=s"(pc));
      const u64 a = (pc & ~(u64)255) + (u64)(piece * 1024 + lane * 16);
      asm volatile("global_load_dwordx4 %0, %1, off" : "+v"(dummy) : "v"(a) : "memory");
    }
  }
  const int kvh = bid & 7, b = bid >> 6, s = (bid >> 3) & 7;
  const int j = kvh * 8 + s;
  const int g = lane >> 4, c = lane & 15;
  STAMPR(36); STAMP(0);

  const size_t slab = ((size_t)(b * 8 + kvh) * SK + (SK - WIN) + s * 512) * 256;
  const __amdgpu_buffer_rsrc_t rK = __builtin_amdgcn_make_buffer_rsrc((void*)(p.K + slab), 0, 512 * 256, 0x00020000);
  const __amdgpu_buffer_rsrc_t rV = __builtin_amdgcn_make_buffer_rsrc((void*)(p.V + slab), 0, 512 * 256, 0x00020000);
  const __amdgpu_buffer_rsrc_t rQ = __builtin_amdgcn_make_buffer_rsrc((void*)p.wq, 0, 512 * 4096 * 2, 0x00020000);
  const __amdgpu_buffer_rsrc_t rO = __builtin_amdgcn_make_buffer_rsrc((void*)p.wo, 0, 512 * 4096 * 2, 0x00020000);
  const __amdgpu_buffer_rsrc_t rs = __builtin_amdgcn_make_buffer_rsrc((void*)p.ws, 0, WS_END, 0x00020000);
  const __amdgpu_buffer_rsrc_t rX = __builtin_amdgcn_make_buffer_rsrc((void*)p.x, 0, 4096, 0x00020000);
  const u32 ldsb = (u32)(size_t)L;

  const int rot = __builtin_amdgcn_readfirstlane((int)(((u32)(bid * 8 + w) * 2654435761u) >> 28));
  int vok[4], vov[2];
#pragma unroll
  for (int i = 0; i < 4; ++i) vok[i] = (64 * w + g) * 256 + ((c ^ (4 * i + g)) * 16);
#define LDK(i) asm volatile("s_mov_b32 m0, %0\n\ts_nop 0\n\tbuffer_load_dwordx4 %1, %2, %3 offen" KVPOL " lds" ::"s"(ldsb + L_KV + w * 16384 + (i) * 1024), "v"(vok[(i) & 3]), "s"(rK), "s"((((i) + rot) & 15) * 1024) : "memory")
  asm volatile("s_nop 4" ::: "memory");
  LDK(0); LDK(1); LDK(2);
  u32 ev, xraw, b2raw, dummy2 = 0;
  asm volatile("buffer_load_dword %0, %1, %2, 0 offen sc1" : "=v"(ev) : "v"(WS_EP + b * 256), "s"(rs) : "memory");
  asm volatile("buffer_load_ushort %0, %1, %2, 0 offen" : "=v"(xraw) : "v"((b * 512 + tid) * 2), "s"(rX) : "memory");
  asm volatile("global_load_ushort %0, %1, %2" : "=v"(b2raw) : "v"(0), "s"(p.b2) : "memory");
  const int cg = s >> 1, kh = s & 1;
  const int voq = ((256 * kh + 32 * w + 8 * g) * 4096 + kvh * 512 + 128 * cg + 8 * c) * 2;
  const int so_ = (s + OROT) & 7;
  const int voo = ((kvh * 512 + (2 * (w >> 2) + (c >> 3)) * 128 + 32 * (w & 3) + 4 * g) * 512 + 64 * so_ + 8 * (c & 7)) * 2;
#pragma unroll
  for (int i = 0; i < 2; ++i) vov[i] = (64 * w + g) * 256 + ((((c >> 1) ^ (g | (i << 2))) * 32) + (c & 1) * 16);
  v4u wqr[8], wor[8], kf[16];
#define LDQ(jj) asm volatile("buffer_load_dwordx4 %0, %1, %2, %3 offen" WPOL : "=v"(wqr[jj]) : "v"(voq), "s"(rQ), "s"((jj) * 8192) : "memory")
#define LDO(jj) asm volatile("buffer_load_dwordx4 %0, %1, %2, %3 offen" WPOL : "=v"(wor[jj]) : "v"(voo), "s"(rO), "s"((16 * ((jj) >> 2) + ((jj) & 3)) * 1024) : "memory")
#define LDV(i) asm volatile("s_mov_b32 m0, %0\n\ts_nop 0\n\tbuffer_load_dwordx4 %1, %2, %3 offen" KVPOL " lds" ::"s"(ldsb + L_KV + w * 16384 + (i) * 1024), "v"(vov[((i) >> 1) & 1]), "s"(rV), "s"((((i) + rot) & 15) * 1024) : "memory")
#define GW(d) asm volatile("s_waitcnt vmcnt(%0)" ::"n"((d) - 1) : "memory")
#define G() GW(GWIN)
#define GWIN WD
#define EV(n, i) asm volatile("s_waitcnt vmcnt(%0)" ::"n"((n) - (i) - 1) : "memory")
#define KFRAG(t)                                                                                                     \
  _Pragma("unroll") for (int st = 0; st < 4; ++st)                                                                   \
      kf[(t) * 4 + st] = *(const LAS v4u*)(L + L_KV + w * 16384 + (16 * (t) + c) * 256 + (((4 * st + g) ^ c) * 16)); \
  asm volatile("" : "+v"(kf[(t) * 4]), "+v"(kf[(t) * 4 + 1]), "+v"(kf[(t) * 4 + 2]), "+v"(kf[(t) * 4 + 3])::"memory")
  LDQ(0); LDQ(1); LDQ(2);
  STAMP(38);
  if (w == 7) {
    const int me = (b * 8 + kvh) * 8 + s;
    int a_ = lane < 16 ? WS_OB + me * 2048 + lane * 128
           : lane < 32 ? WS_OB + (1 << 19) + me * 2048 + (lane - 16) * 128
           : lane < 34 ? WS_OS + (lane - 32) * 32768 + me * 128
           : lane < 42 ? WS_QB + ((lane - 34) >> 2) * (4 * 8 * 4096) + ((b * 8 + kvh) * 2 + (s & 1)) * 2048 + (s >> 1) * 512 + ((lane - 34) & 3) * 128
           : lane < 46 ? WS_PB + ((lane - 42) >> 1) * 65536 + (b * 8 + kvh) * 2048 + ((s + OROT) & 7) * 256 + ((lane - 42) & 1) * 128
           : WS_XB + (b * 64 + j) * XBS;
    asm volatile("buffer_load_dword %0, %1, %2, 0 offen" : "+v"(dummy2) : "v"(a_), "s"(rs) : "memory");
  }
  G(); LDQ(3); G(); LDK(3);
  asm volatile("s_waitcnt vmcnt(5)" : "+v"(ev), "+v"(xraw), "+v"(b2raw)::"memory");
  const u32 T0 = __builtin_amdgcn_readfirstlane(ev) + 1u;
  const u32 tag = T0 & TMASK;
  float xres = asf(xraw << 16);
  const int xqa = L_XL + (256 * kh + 32 * w + 8 * g) * 4;
  v4u qa;
  {
    *(LAS float*)(L + L_XL + tid * 4) = xres;
    float mean, var;
    ln_stats(xres, L, w, lane, mean, var);
    qa = q_operand(L, xqa, mean, var);
    PIN1(qa);
  }
  G(); LDQ(4); G(); LDK(4); G(); LDQ(5); G(); LDK(5);
  G(); LDQ(6); G(); LDK(6); G(); LDQ(7); G(); LDK(7);
  EV(16, 7); STAMPW(48); KFRAG(0);
  G(); LDK(8); G(); LDK(9); G(); LDK(10); G(); LDK(11); G(); LDK(12);
  EV(21, 14);
  asm volatile("" : "+v"(wqr[0]), "+v"(wqr[1]), "+v"(wqr[2]), "+v"(wqr[3]), "+v"(wqr[4]), "+v"(wqr[5]), "+v"(wqr[6]), "+v"(wqr[7]), "+v"(dummy), "+v"(dummy2)::"memory");
  STAMP(1); STAMPW(40);
  LDK(13); LDK(14); LDK(15);
  {
#pragma unroll
    for (int k = 0; k < 2; ++k) {
      const int piece = k == 0 ? w : 8 + (w % 3);
      int gr = piece * 64 + lane; gr = gr < 672 ? gr : 671;
      const int arr = gr / 224, e = gr - arr * 224;
      const unsigned char* src = (const unsigned char*)(arr == 0 ? p.w1 : arr == 1 ? p.b1 : p.w2) + e * 16;
      asm volatile("s_mov_b32 m0, %0\n\ts_nop 0\n\tglobal_load_lds_dwordx4 %1, off" ::"s"(ldsb + L_PAR + piece * 1024), "v"(src) : "memory");
    }
  }
  LDV(0); LDV(1);
  v4u wqf[8];
  transpose8(wqr, wqf);
  const int qwr = L_RED + w * 576 + (8 * c + 4 * (g & 1)) * 4, qrd = L_RED + (tid & 3) * 576 + (tid >> 2) * 16;
  const int qpub = WS_QB + ((b * 8 + kvh) * 2 + kh) * 2048 + cg * 512 + tid * 4;
  q_gemv(wqf, qa, L, tid, lane, qwr, qrd, rs, qpub, tag);
  STAMP(2);
#undef GWIN
#define GWIN WD2
  EV(28, 15); KFRAG(1);
  G(); LDV(2); G(); LDV(3);
  EV(30, 19); KFRAG(2);
#define WAITN(n) asm volatile("s_waitcnt vmcnt(%0)" ::"n"(n) : "memory")
  G(); LDO(0); G(); LDV(4); G(); LDV(5);
  STAMP(3);
  u32 q0, q1;
  const int qo = WS_QB + (b * 8 + kvh) * 4096 + tid * 4;
  G(); asm volatile("buffer_load_dword %0, %1, %2, 0 offen sc1" : "=v"(q0) : "v"(qo), "s"(rs) : "memory");
  asm volatile("buffer_load_dword %0, %1, %2, 0 offen offset:2048 sc1" : "=v"(q1) : "v"(qo), "s"(rs) : "memory");
  WAITN(11); KFRAG(3);
  STAMP(4); STAMPW(56);
  G(); LDO(1); G(); LDV(6); G(); LDV(7); G(); LDO(2); G(); LDV(8); G(); LDV(9);
  WAITN(6);
  asm volatile("" : "+v"(q0), "+v"(q1)::"memory");
  STAMP(5); STAMPW(64);
  LDO(3); LDV(10); LDV(11); LDO(4); LDV(12); LDV(13);
  int budget = 100000;
  if (!__all((((q0 ^ tag) | (q1 ^ tag)) & TMASK) == 0)) {
    _Pragma("nounroll") do {
      asm volatile("buffer_load_dword %0, %2, %3, 0 offen sc1\n\tbuffer_load_dword %1, %2, %3, 0 offen offset:2048 sc1\n\ts_waitcnt vmcnt(0)" : "=&v"(q0), "=&v"(q1) : "v"(qo), "s"(rs) : "memory");
    } while (!__all((((q0 ^ tag) | (q1 ^ tag)) & TMASK) == 0) && --budget > 0);
  }
  STAMPV(19, budget);
  const int vbase = L_KV + (g >> 1) * 16384 + (8 * (g & 1) + (c >> 2)) * 256 + ((w ^ ((c >> 2) | ((g & 1) << 2))) * 32) + (c & 3) * 8;
  const int a_qwr = L_QL + (tid >> 7) * QSTR + (tid & 127) * 2, a_qrd = L_QL + (c & 3) * QSTR + g * 16;
  const int a_mwr = L_MB + ((lane & 3) * 8 + w) * 4, a_mrd = L_MB + (c & 3) * 32;
  const int a_pwr = L_PL + (c >> 2) * PSTR + ((2 * (w & 1) + (g >> 1)) * 16 + (w >> 1) * 4 + (c & 3)) * 16 + (g & 1) * 8;
  const int a_lwr = L_LB + (w * 16 + g * 4 + (c & 3)) * 4;
  f4 acc[4];
  float M = attn_scores(asf(q0 ^ tag) + asf(q1 ^ tag), kf, L, a_qwr, a_qrd, a_mwr, a_mrd, a_pwr, a_lwr, lane);
  STAMP(6);
#undef GWIN
#define GWIN WD3
  G(); LDO(5); G(); LDO(6); G(); LDO(7); G(); LDV(14); G(); LDV(15);
  WAITN(8);
  BAR();
  const int pub_o = ((b * 8 + kvh) * 8 + s) * 2048 + w * 256 + lane * 4, pub_s = ((b * 8 + kvh) * 8 + s) * 128 + (lane & 7) * 16;
  attn_publish_stats(M, L, w, lane, rs, WS_OS + pub_s, tag, T0);
#pragma unroll
  for (int u = 0; u < 4; ++u) acc[u] = f4{0.f, 0.f, 0.f, 0.f};
  pv_chunk(0, acc, L, vbase, lane);
  pv_chunk(1, acc, L, vbase, lane);
  pv_chunk(2, acc, L, vbase, lane);
  v4u pf3 = *(const LAS v4u*)(L + L_PL + 3 * PSTR + lane * 16);
  PIN1(pf3);
  SB();
  WAITN(0);
  STAMP(7); STAMPW(72);
  BAR();
  {
    v4u vf3[4];
#pragma unroll
    for (int u = 0; u < 4; ++u) vf3[u] = v_frag(3, u, L, vbase);
    SB();
#pragma unroll
    for (int u = 0; u < 4; ++u) acc[u] = mfma(pf3, vf3[u], acc[u]);
  }
  int po1 = WS_OB + pub_o;
  attn_publish(acc, lane, rs, po1, T0);
  STAMP(8);
  v4u wof[8];
  const float b2 = asf(b2raw << 16);
  int g_oo = (b * 8 + kvh) * 8 * 2048 + (lane >> 3) * 2048 + 2 * (w & 3) * 256 + (w >> 2) * 128 + (lane & 7) * 16;
  int g_so = (b * 8 + kvh) * 8 * 128 + (lane & 7) * 128 + (2 * (w >> 2) + ((lane >> 3) & 1)) * 16;
  int o_xw = L_SC2 + w * 1024 + (lane ^ (lane >> 4)) * 16, o_xr = L_SC2 + w * 1024 + (((c & 7) * 8 + 4 * (c >> 3) + g) ^ ((c & 7) >> 1)) * 16;
  int o_wr = L_RED + (2 * w + (g >> 1)) * 320 + (8 * (c & 7) + 4 * (g & 1)) * 4, o_rd = L_RED + (tid & 3) * 320 + (tid >> 2) * 16;
  int o_pub = WS_PB + (b * 8 + kvh) * 2048 + so_ * 256 + tid * 4;
  g_oo += WS_OB; g_so += WS_OS;
  PIN4(g_oo, g_so, o_wr, o_rd); PIN1(o_pub); PIN2(o_xw, o_xr);
  BAR();

  if (DLY_B) __builtin_amdgcn_s_sleep(DLY_B);
  o_gemv<true>([&]() { q_gemv1(wqf, v4u{0x3f803f80u, 0x3f803f80u, 0x3f803f80u, 0x3f803f80u}, L, lane, L_PL + w * 544 + (8 * c + 4 * (g & 1)) * 4); },
               [&]() { if (w == 2 || w == 3) q_gemv2<544>(L, lane, L_PL + (lane & 3) * 544 + ((w - 2) * 16 + (lane >> 2)) * 16, rs, qpub - WS_QB + WS_CS - 128 * 4, tag); }, wof, wor, L, tid, lane, rs, g_oo, g_so, o_xw, o_xr, o_wr, o_rd, o_pub, tag, T0, budget, w, 20);
  STAMP(10);
  u32 cs0, cs1;
  const int cso = WS_CS + (b * 8 + kvh) * 4096 + tid * 4;
  float mprev, csum;
  {
    const f4 r = mixer<DLY_CD>([&]() { cs0 = ld32s(rs, cso); cs1 = ld32s(rs, cso + 2048); }, xres, L, tid, w, lane, j, rs, WS_PB + b * 8 * 2048, tag, b2, budget, 22, mprev);
    cs0 ^= tag; cs1 ^= tag;
    if (!__all(((cs0 | cs1) & TMASK) == 0)) {
      do { CBAR(); cs0 = ld32s(rs, cso) ^ tag; cs1 = ld32s(rs, cso + 2048) ^ tag; } while (!__all(((cs0 | cs1) & TMASK) == 0) && --budget > 0);
    }
    csum = asf(cs0) + asf(cs1);
    PIN1(csum);
    if (tid < XBS / 16) { v4u o = {tagf(r[0], tag), tagf(r[1], tag), tagf(r[2], tag), tagf(r[3], tag)}; st128s(o, rs, WS_XB + (b * 64 + j) * XBS + tid * 16); }
  }
  STAMP(11);
  float rstd2, dmean;
  int qo2 = qo + 4 * 8 * 4096, b0 = a_qwr, b1 = a_qrd, b2_ = a_mwr, b3 = a_mrd, b4 = a_pwr, b5 = a_lwr, pr = L_PL + lane * 16, po2 = WS_OB + (1 << 19) + pub_o, ps2 = WS_OS + 32768 + pub_s;
  PIN4(qo2, b0, b1, b2_); PIN4(b3, b4, b5, pr); PIN2(po2, ps2);
  {
    int xo_ = WS_XB + (b * 64 + (tid >> 3)) * XBS + (tid & 7) * 4, ao = WS_XB + (b * 64 + 32 * kh + 4 * w + g) * XBS + (c & 1) * 16, qw = qwr, qr = qrd, qp = qpub + 4 * 8 * 4096;
    PIN4(xo_, ao, qw, qr); PIN1(qp);
    if (DLY_E) __builtin_amdgcn_s_sleep(DLY_E);
    v4u a0;
    u32 xv;
    for (;;) {
      CBAR();
      a0 = v4u{tag, tag, tag, tag};
      if (c < 2) a0 = ld128s(rs, ao);
      xv = ld32s(rs, xo_) ^ tag;
#pragma unroll
      for (int i = 0; i < 4; ++i) a0[i] ^= tag;
      if (__all((((a0[0] | a0[1] | a0[2]) | (a0[3] | xv)) & TMASK) == 0) || --budget <= 0) break;
    }
    STAMP(26); STAMPV(27, budget); STAMPW(120);
    const u32 p0 = cvtpk(asf(a0[0]) - mprev, asf(a0[1]) - mprev), p1 = cvtpk(asf(a0[2]) - mprev, asf(a0[3]) - mprev);
    const v4u qa2 = {rowbc<0>(p0), rowbc<0>(p1), rowbc<1>(p0), rowbc<1>(p1)};
    STAMP(12);
    q_gemv(wqf, qa2, L, tid, lane, qw, qr, rs, qp, tag);
    STAMP(13);
    if (DLY_A) __builtin_amdgcn_s_sleep(DLY_A);
    CBAR();
    q0 = ld32s(rs, qo2); q1 = ld32s(rs, qo2 + 2048);
    CBAR();
    xres = asf(xv);
    float mean, var;
    ln_stats(xres, L, w, lane, mean, var);
    rstd2 = __builtin_amdgcn_rsqf(var + EPS);
    dmean = (mean - mprev) * csum;
    PIN2(rstd2, dmean);
  }
  {
    v4u vf[16];
#pragma unroll
    for (int t = 0; t < 4; ++t)
#pragma unroll
      for (int u = 0; u < 4; ++u) vf[t * 4 + u] = v_frag(t, u, L, vbase);
    PIN8(vf); { v4u* v2 = vf + 8; PIN8(v2); }
    q0 ^= tag; q1 ^= tag;
    while (!__all(((q0 | q1) & TMASK) == 0) && --budget > 0) { CBAR(); q0 = ld32s(rs, qo2) ^ tag; q1 = ld32s(rs, qo2 + 2048) ^ tag; }
    STAMP(14); STAMPV(18, budget);
    M = attn_scores(rstd2 * ((asf(q0) + asf(q1)) - dmean), kf, L, b0, b1, b2_, b3, b4, b5, lane);
    BAR();
    v4u pf[4];
#pragma unroll
    for (int t = 0; t < 4; ++t) pf[t] = *(const LAS v4u*)(L + pr + t * PSTR);
    SB();
#pragma unroll
    for (int t = 0; t < 4; ++t)
#pragma unroll
      for (int u = 0; u < 4; ++u) acc[u] = mfma(pf[t], vf[t * 4 + u], t == 0 ? f4{0.f, 0.f, 0.f, 0.f} : acc[u]);
    attn_publish_stats(M, L, w, lane, rs, ps2, tag, T0);
    attn_publish(acc, lane, rs, po2, T0);
  }
  STAMP(15);
  g_oo += 1 << 19; g_so += 32768; o_pub += 65536;
  PIN2(g_oo, g_so); PIN1(o_pub);
  if (DLY_B2) __builtin_amdgcn_s_sleep(DLY_B2);
  o_gemv<false>([]() {}, []() {}, wof, wor, L, tid, lane, rs, g_oo, g_so, o_xw, o_xr, o_wr, o_rd, o_pub, tag, T0, budget, w, 28);
  STAMP(16);
  {
    const f4 r = mixer<DLY_CD2>([]() {}, xres, L, tid, w, lane, j, rs, WS_PB + 65536 + b * 8 * 2048, tag, b2, budget, 30, mprev);
    if (w == 0) {
      const u32 lo = cvtpk(r[0], r[1]), hi = cvtpk(r[2], r[3]);
      if (lane < 2) { v2u o = {lo, hi}; *(GAS v2u*)((GAS unsigned char*)p.out + (b * 512 + 8 * j + 4 * lane) * 2) = o; }
      if (lane == 0 && j == 0) __builtin_amdgcn_raw_buffer_store_b32(T0, rs, WS_EP + b * 256, 0, 16);
    }
  }
  STAMP(17); STAMPR(37);
}

extern "C" void kernel_launch(void* const* d_in, const int* in_sizes, int n_in, void* d_out, int out_size, void* d_ws, size_t ws_size, hipStream_t stream) {
  Params p;
  p.x = (const u16*)d_in[0]; p.K = (const unsigned char*)d_in[1]; p.V = (const unsigned char*)d_in[2];
  p.wq = (const unsigned char*)d_in[3]; p.wo = (const unsigned char*)d_in[4];
  p.w1 = (const u16*)d_in[6]; p.b1 = (const u16*)d_in[7]; p.w2 = (const u16*)d_in[8]; p.b2 = (const u16*)d_in[9];
  p.out = (u16*)d_out; p.ws = (unsigned char*)d_ws;
  hipLaunchKernelGGL(fused_decode_kernel, dim3(256), dim3(512), 0, stream, p);
}
```

```cpp
#include <hip/hip_runtime.h>

typedef unsigned int u32;
typedef unsigned short u16;
typedef unsigned long long u64;
typedef u32 v4u __attribute__((ext_vector_type(4)));
typedef u32 v2u __attribute__((ext_vector_type(2)));
typedef float f4 __attribute__((ext_vector_type(4)));
typedef float f2 __attribute__((ext_vector_type(2)));
typedef short s8v __attribute__((ext_vector_type(8)));
typedef short s4v __attribute__((ext_vector_type(4)));
typedef __bf16 bf2v __attribute__((ext_vector_type(2)));
#define LAS __attribute__((address_space(3)))
#define GAS __attribute__((address_space(1)))
#define DI __device__ __forceinline__

#define STAMPS 0
#define DLY_B 10
#define DLY_CD 8
#define DLY_E 16
#define DLY_A 6
#define STW 0
#define KVPOL " nt"
#define WPOL " nt"
#define PRETOUCH 1
#define OROT 4
#define XBS 128
#define BAR_B2 0
#define DLY_B2 13
#define DLY_CD2 7
#define RS_CD 0
#define RS_B 0
#define WD2 5
#define WD3 5
#define WD 5
#define CODE_KB 17

constexpr int SK = 16384, WIN = 4096;
constexpr float EPS = 1e-5f;
constexpr float SCL = 0.08838834764831845f * 1.4426950408889634f;
constexpr float GK0 = -2.0f * 0.7978845608028654f * 1.4426950408889634f;
constexpr float GK1 = GK0 * 0.044715f;

constexpr int WS_EP = 0;
constexpr int WS_QB = 4096;
constexpr int WS_CS = WS_QB + 2 * 4 * 8 * 2 * 2048;
constexpr int WS_OB = WS_CS + 4 * 8 * 2 * 2048;
constexpr int WS_OS = WS_OB + 2 * 4 * 8 * 8 * 2048;
constexpr int WS_PB = WS_OS + 2 * 4 * 8 * 8 * 128;
constexpr int WS_XB = WS_PB + 2 * 4 * 8 * 2048;
constexpr int WS_END = WS_XB + 4 * 64 * XBS;
constexpr int WS_STAMP = 4 << 20;

constexpr int L_KV = 0;
constexpr int L_PAR = 131072;
constexpr int PSTR = 1088;
constexpr int L_PL = L_PAR + 11264;
constexpr int QSTR = 288;
constexpr int L_RED = L_PL + 4 * PSTR;
constexpr int L_ST = L_RED + 5120;
constexpr int L_MB = L_ST + 64;
constexpr int L_LB = L_MB + 128;
constexpr int L_R2 = L_LB + 512;
constexpr int L_QL = L_R2 + 128;
constexpr int L_XL = L_QL + 4 * QSTR;
constexpr int L_SC2 = L_QL;
constexpr int L_STAMP = L_SC2 + 8192;
constexpr int L_END = L_STAMP + (STAMPS ? 1024 + 64 : 0);
static_assert(L_END <= 163840, "LDS");

struct Params {
  const u16* x; const unsigned char* K; const unsigned char* V; const unsigned char* wq; const unsigned char* wo;
  const u16* w1; const u16* b1; const u16* w2; const u16* b2; u16* out; unsigned char* ws;
};

template <int CTRL> DI float dpp(float x) {
  return __builtin_bit_cast(float, __builtin_amdgcn_update_dpp(0, __builtin_bit_cast(int, x), CTRL, 0xf, 0xf, true));
}
constexpr int XOR1 = 0xB1, XOR2 = 0x4E, HMIR = 0x141, MIR = 0x140, ROR4 = 0x124, ROR8 = 0x128;
DI float asf(u32 x) { return __builtin_bit_cast(float, x); }
DI u32 asu(float x) { return __builtin_bit_cast(u32, x); }
DI u32 cvtpk(float lo, float hi) { f2 f = {lo, hi}; return __builtin_bit_cast(u32, __builtin_convertvector(f, bf2v)); }
DI f4 mfma(v4u a, v4u b, f4 c) {
  return __builtin_amdgcn_mfma_f32_16x16x32_bf16(__builtin_bit_cast(s8v, a), __builtin_bit_cast(s8v, b), c, 0, 0, 0);
}
DI float fmx(float a, float b) { return __builtin_elementwise_maximum(a, b); }
DI float xrow_max(float x) {
  auto s = __builtin_amdgcn_permlane16_swap(asu(x), asu(x), false, false);
  x = fmx(asf(s[0]), asf(s[1]));
  auto t = __builtin_amdgcn_permlane32_swap(asu(x), asu(x), false, false);
  return fmx(asf(t[0]), asf(t[1]));
}
DI float quad_reduce4(f4 r, int lane) {
  float t0 = (lane & 1) ? r[1] : r[0], t1 = (lane & 1) ? r[0] : r[1];
  t0 += dpp<XOR1>(t1);
  float t2 = (lane & 1) ? r[3] : r[2], t3 = (lane & 1) ? r[2] : r[3];
  t2 += dpp<XOR1>(t3);
  float s0 = (lane & 2) ? t2 : t0; const float s1 = (lane & 2) ? t0 : t2;
  s0 += dpp<XOR2>(s1);
  return s0;
}
DI void st32s(u32 v, __amdgpu_buffer_rsrc_t rs, int off) { __builtin_amdgcn_raw_buffer_store_b32(v, rs, off, 0, 16); }
template <int N> DI u32 rowbc(u32 x) { return (u32)__builtin_amdgcn_update_dpp(0, (int)x, 0x150 + N, 0xf, 0xf, false); }
DI u32 xo(u32 a, u32 t, u32 acc) { return __builtin_amdgcn_bitop3_b32(a, t, acc, 0xBE); }
#define PIN1(a) asm volatile("" : "+v"(a))
#define PIN2(a, b) asm volatile("" : "+v"(a), "+v"(b))
#define PIN4(a, b, c, d) asm volatile("" : "+v"(a), "+v"(b), "+v"(c), "+v"(d))
#define PIN8(x) asm volatile("" : "+v"(x[0]), "+v"(x[1]), "+v"(x[2]), "+v"(x[3]), "+v"(x[4]), "+v"(x[5]), "+v"(x[6]), "+v"(x[7]))
DI float xrow_sum(float x) {
  auto s = __builtin_amdgcn_permlane16_swap(asu(x), asu(x), false, false);
  x = asf(s[0]) + asf(s[1]);
  auto t = __builtin_amdgcn_permlane32_swap(asu(x), asu(x), false, false);
  return asf(t[0]) + asf(t[1]);
}
#define BAR() asm volatile("s_waitcnt lgkmcnt(0)\n\ts_barrier" ::: "memory")
#define CBAR() asm volatile("" ::: "memory")
#define SB() __builtin_amdgcn_sched_barrier(0)

#define STAMP(i)
#define STAMPR(i)
#define STAMPV(i, v)
#define STAMPW(i)

constexpr u32 TMASK = 0x3ffu;
DI u32 tagf(float v, u32 tag) { return __builtin_amdgcn_bitop3_b32(asu(v) + 0x200u, TMASK, tag, 0xBA); }
DI float untag(u32 v) { return asf(v & ~TMASK); }

DI u32 ld32s(__amdgpu_buffer_rsrc_t rs, int off) { return __builtin_amdgcn_raw_buffer_load_b32(rs, off, 0, 16); }
DI v4u ld128s(__amdgpu_buffer_rsrc_t rs, int off) { return __builtin_amdgcn_raw_buffer_load_b128(rs, off, 0, 16); }
DI void st128s(v4u v, __amdgpu_buffer_rsrc_t rs, int off) { __builtin_amdgcn_raw_buffer_store_b128(v, rs, off, 0, 16); }

DI void ln_stats(float x, LAS unsigned char* L, int w, int lane, float& mean, float& var) {
  const float c = asf(__builtin_amdgcn_readfirstlane(asu(x)));
  const float d = x - c;
  float s1 = d, s2 = d * d;
  s1 += dpp<XOR1>(s1); s2 += dpp<XOR1>(s2);
  s1 += dpp<XOR2>(s1); s2 += dpp<XOR2>(s2);
  s1 += dpp<HMIR>(s1); s2 += dpp<HMIR>(s2);
  s1 += dpp<MIR>(s1); s2 += dpp<MIR>(s2);
  asm volatile("s_nop 1\n\tv_add_f32_dpp %0, %0, %0 row_bcast:15 row_mask:0xa bank_mask:0xf\n\tv_add_f32_dpp %1, %1, %1 row_bcast:15 row_mask:0xa bank_mask:0xf\n\t"
               "s_nop 0\n\tv_add_f32_dpp %0, %0, %0 row_bcast:31 row_mask:0xc bank_mask:0xf\n\tv_add_f32_dpp %1, %1, %1 row_bcast:31 row_mask:0xc bank_mask:0xf"
               : "+v"(s1), "+v"(s2));
  const f2 pv = {fmaf(s1, 1.0f / 64, c), fmaf(s1 * s1, -1.0f / 64, s2)};
  if (lane == 63) *(LAS f2*)(L + L_ST + w * 8) = pv;
  BAR();
  const f2 v = *(const LAS f2*)(L + L_ST + (lane & 7) * 8);
  float m = v.x;
  m += dpp<XOR1>(m); m += dpp<XOR2>(m); m += dpp<HMIR>(m);
  mean = m * 0.125f;
  const float dv = v.x - mean;
  float t = fmaf(64.0f * dv, dv, v.y);
  t += dpp<XOR1>(t); t += dpp<XOR2>(t); t += dpp<HMIR>(t);
  var = t * (1.0f / 512);
}

DI void transpose8(const v4u (&raw)[8], v4u (&fr)[8]) {
#pragma unroll
  for (int i = 0; i < 8; ++i) {
    const u32 sel = (i & 1) ? 0x07060302u : 0x05040100u;
#pragma unroll
    for (int m = 0; m < 4; ++m) fr[i][m] = __builtin_amdgcn_perm(raw[2 * m + 1][i >> 1], raw[2 * m][i >> 1], sel);
  }
}

DI v4u q_operand(LAS unsigned char* L, int xaddr, float mean, float var) {
  const f4 a = *(const LAS f4*)(L + xaddr), b = *(const LAS f4*)(L + xaddr + 16);
  const float rs_ = __builtin_amdgcn_rsqf(var + EPS), mr = -mean * rs_;
  return v4u{cvtpk(fmaf(a[0], rs_, mr), fmaf(a[1], rs_, mr)), cvtpk(fmaf(a[2], rs_, mr), fmaf(a[3], rs_, mr)),
             cvtpk(fmaf(b[0], rs_, mr), fmaf(b[1], rs_, mr)), cvtpk(fmaf(b[2], rs_, mr), fmaf(b[3], rs_, mr))};
}

DI void q_gemv1(const v4u (&wf)[8], v4u a, LAS unsigned char* L, int lane, int wraddr) {
  f4 d[8];
#pragma unroll
  for (int i = 0; i < 8; ++i) d[i] = mfma(a, wf[i], f4{0.f, 0.f, 0.f, 0.f});
  f4 v;
#pragma unroll
  for (int m = 0; m < 4; ++m) v[m] = (lane & 16) ? d[4 + m][0] : d[m][0];
  if (lane < 32) *(LAS f4*)(L + wraddr) = v;
}
template <int STR>
DI void q_gemv2(LAS unsigned char* L, int lane, int rdaddr, __amdgpu_buffer_rsrc_t rs, int qoff, u32 tag) {
  const f4 r = *(const LAS f4*)(L + rdaddr) + *(const LAS f4*)(L + rdaddr + 4 * STR);
  st32s(tagf(quad_reduce4(r, lane), tag), rs, qoff);
}
DI void q_gemv(const v4u (&wf)[8], v4u a, LAS unsigned char* L, int tid, int lane, int wraddr, int rdaddr, __amdgpu_buffer_rsrc_t rs, int qoff, u32 tag) {
  q_gemv1(wf, a, L, lane, wraddr);
  BAR();
  if (tid < 128) q_gemv2<576>(L, lane, rdaddr, rs, qoff, tag);
}

DI float attn_scores(float qv, const v4u (&kf)[16], LAS unsigned char* L, int qwr, int qrd, int mwr, int mrd, int pwr, int lwr, int lane) {
  *(LAS u16*)(L + qwr) = (u16)(cvtpk(qv, qv) & 0xffffu);
  BAR();
  v4u qf[4];
#pragma unroll
  for (int st = 0; st < 4; ++st) qf[st] = *(const LAS v4u*)(L + qrd + st * 64);
  SB();
  f4 d[4];
#pragma unroll
  for (int st = 0; st < 4; ++st)
#pragma unroll
    for (int t = 0; t < 4; ++t) d[t] = mfma(kf[t * 4 + st], qf[st], st == 0 ? f4{0.f, 0.f, 0.f, 0.f} : d[t]);
  float sc[4];
#pragma unroll
  for (int i = 0; i < 4; ++i) {
    const float a = (lane & 4) ? d[1][i] : d[0][i], bq = (lane & 4) ? d[3][i] : d[2][i];
    sc[i] = (lane & 8) ? bq : a;
  }
  float m = fmx(fmx(sc[0], sc[1]), fmx(sc[2], sc[3]));
  m = fmx(m, dpp<ROR4>(m)); m = fmx(m, dpp<ROR8>(m));
  m = xrow_max(m);
  if (lane < 4) *(LAS float*)(L + mwr) = m;
  BAR();
  const f4 m0 = *(const LAS f4*)(L + mrd), m1 = *(const LAS f4*)(L + mrd + 16);
  float M = fmx(fmx(fmx(m0[0], m0[1]), fmx(m0[2], m0[3])), fmx(fmx(m1[0], m1[1]), fmx(m1[2], m1[3])));
  M = asf(asu(M * SCL) & ~TMASK);
  float p[4];
#pragma unroll
  for (int i = 0; i < 4; ++i) p[i] = __builtin_amdgcn_exp2f(fmaf(sc[i], SCL, -M));
  v2u pk = {cvtpk(p[0], p[1]), cvtpk(p[2], p[3])};
  *(LAS v2u*)(L + pwr) = pk;
  float l = (p[0] + p[1]) + (p[2] + p[3]);
  l += dpp<ROR4>(l); l += dpp<ROR8>(l);
  if ((lane & 12) == 0) *(LAS float*)(L + lwr) = l;
  return M;
}

DI v4u v_frag(int t, int u, LAS unsigned char* L, int vbase) {
  const s4v lo = __builtin_amdgcn_ds_read_tr16_b64_v4i16((LAS s4v*)(L + vbase + u * 32768 + t * 4096));
  const s4v hi = __builtin_amdgcn_ds_read_tr16_b64_v4i16((LAS s4v*)(L + vbase + u * 32768 + t * 4096 + 1024));
  const v2u l2 = __builtin_bit_cast(v2u, lo), h2 = __builtin_bit_cast(v2u, hi);
  return v4u{l2[0], l2[1], h2[0], h2[1]};
}
DI void pv_chunk(int t, f4 (&acc)[4], LAS unsigned char* L, int vbase, int lane) {
  const v4u pf = *(const LAS v4u*)(L + L_PL + t * PSTR + lane * 16);
#pragma unroll
  for (int u = 0; u < 4; ++u) acc[u] = mfma(pf, v_frag(t, u, L, vbase), acc[u]);
}

DI void attn_publish_stats(float M, LAS unsigned char* L, int w, int lane, __amdgpu_buffer_rsrc_t rs, int saddr, u32 tag, u32 T0) {
  if (w == 7) {
    float l = *(const LAS float*)(L + L_LB + lane * 4) + *(const LAS float*)(L + L_LB + (lane + 64) * 4);
    l += dpp<ROR4>(l); l += dpp<ROR8>(l);
    l = xrow_sum(l);
    if (lane < 8) { v4u sv = {asu(M) | tag, tagf(l, tag), T0, T0}; st128s(sv, rs, saddr); }
  }
}
template <int RM> DI float rowput(float old, float x) {
  return __builtin_bit_cast(float, __builtin_amdgcn_update_dpp(__builtin_bit_cast(int, old), __builtin_bit_cast(int, x), 0xE4, RM, 0xf, false));
}
DI void attn_publish(const f4 (&acc)[4], int lane, __amdgpu_buffer_rsrc_t rs, int oaddr, u32 T0) {
  float y[4];
#pragma unroll
  for (int h = 0; h < 4; ++h) y[h] = rowput<0x8>(rowput<0x4>(rowput<0x2>(acc[0][h], acc[1][h]), acc[2][h]), acc[3][h]);
  auto s01 = __builtin_amdgcn_permlane16_swap(asu(y[0]), asu(y[1]), false, false);
  auto s23 = __builtin_amdgcn_permlane16_swap(asu(y[2]), asu(y[3]), false, false);
  const float z01 = asf(s01[0]) + asf(s01[1]), z23 = asf(s23[0]) + asf(s23[1]);
  auto sf = __builtin_amdgcn_permlane32_swap(asu(z01), asu(z23), false, false);
  const float v = asf(sf[0]) + asf(sf[1]);
  st32s(__builtin_amdgcn_perm(cvtpk(v, v), T0, 0x05040100u), rs, oaddr);
}

DI bool o_check(const v4u& x0, const v4u& x1, const v4u& sv, u32 T0) {
  u32 d = xo(x0[0], T0, 0u);
  d = xo(x0[1], T0, d); d = xo(x0[2], T0, d); d = xo(x0[3], T0, d);
  d = xo(x1[0], T0, d); d = xo(x1[1], T0, d); d = xo(x1[2], T0, d); d = xo(x1[3], T0, d);
  d = xo(sv[0], T0, d); d = xo(sv[1], T0, d);
  d = xo(sv[3], T0, xo(sv[2], T0, d & TMASK));
  return __all(d == 0);
}
template <bool FIRST, class Hook, class Hook2>
DI void o_gemv(Hook&& hook, Hook2&& hook2, v4u (&wf)[8], v4u (&wraw)[8], LAS unsigned char* L, int tid, int lane, __amdgpu_buffer_rsrc_t rs, int oo, int so, int xw, int xr, int wraddr, int rdaddr, int poff, u32 tag, u32 T0, int& budget, int w, int sb) {
  v4u x0, x1, sv = {T0, T0, T0, T0};
  if (sb == 28) STAMP(39);
  CBAR();
  x0 = ld128s(rs, oo); x1 = ld128s(rs, oo + 256);
  if (lane < 16) sv = ld128s(rs, so);
  if (FIRST) {
    asm volatile("" : "+v"(wraw[0]), "+v"(wraw[1]), "+v"(wraw[2]), "+v"(wraw[3]), "+v"(wraw[4]), "+v"(wraw[5]), "+v"(wraw[6]), "+v"(wraw[7])::"memory");
    STAMP(9); STAMPW(80);
    transpose8(wraw, wf);
    PIN8(wf);
    hook();
  }
  if (__builtin_expect(!o_check(x0, x1, sv, T0), 0)) {
    do {
      CBAR();
      x0 = ld128s(rs, oo); x1 = ld128s(rs, oo + 256);
      if (lane < 16) sv = ld128s(rs, so);
    } while (!o_check(x0, x1, sv, T0) && --budget > 0);
  }
  STAMP(sb); STAMPV(sb + 1, budget); if (sb == 28) STAMPW(104);
  const v4u pk = {__builtin_amdgcn_perm(x0[1], x0[0], 0x07060302u), __builtin_amdgcn_perm(x0[3], x0[2], 0x07060302u),
                  __builtin_amdgcn_perm(x1[1], x1[0], 0x07060302u), __builtin_amdgcn_perm(x1[3], x1[2], 0x07060302u)};
  *(LAS v4u*)(L + xw) = pk;
  CBAR();
  const v4u a = *(const LAS v4u*)(L + xr);
  const float ms = untag(sv[0]), ls = untag(sv[1]);
  float M = ms;
  M = fmx(M, dpp<XOR1>(M)); M = fmx(M, dpp<XOR2>(M)); M = fmx(M, dpp<HMIR>(M));
  const float f = __builtin_amdgcn_exp2f(ms - M);
  float Ls = f * ls;
  Ls += dpp<XOR1>(Ls); Ls += dpp<XOR2>(Ls); Ls += dpp<HMIR>(Ls);
  const float cs = f * __builtin_amdgcn_rcpf(Ls);
  f4 d[8];
#pragma unroll
  for (int i = 0; i < 8; ++i) d[i] = mfma(a, wf[i], f4{0.f, 0.f, 0.f, 0.f});
  const int bp = (lane >> 4) * 16;
  const float cr0 = asf(__builtin_amdgcn_ds_bpermute(bp, asu(cs))), cr1 = asf(__builtin_amdgcn_ds_bpermute(bp + 4, asu(cs)));
  const float cr2 = asf(__builtin_amdgcn_ds_bpermute(bp + 8, asu(cs))), cr3 = asf(__builtin_amdgcn_ds_bpermute(bp + 12, asu(cs)));
  const f2 c01 = {cr0, cr1}, c23 = {cr2, cr3};
  float e[8];
#pragma unroll
  for (int i = 0; i < 8; ++i) { const f2 t = f2{d[i][2], d[i][3]} * c23 + f2{d[i][0], d[i][1]} * c01; e[i] = t[0] + t[1]; }
  f4 v;
#pragma unroll
  for (int m = 0; m < 4; ++m) { auto t = __builtin_amdgcn_permlane16_swap(asu(e[m]), asu(e[m + 4]), false, false); v[m] = asf(t[0]) + asf(t[1]); }
  if ((lane >> 5) == ((lane >> 3) & 1)) *(LAS f4*)(L + wraddr) = v;
  BAR();
  if (tid < 64) {
    const f4 r = (*(const LAS f4*)(L + rdaddr) + *(const LAS f4*)(L + rdaddr + 4 * 320)) + (*(const LAS f4*)(L + rdaddr + 8 * 320) + *(const LAS f4*)(L + rdaddr + 12 * 320));
    st32s(tagf(quad_reduce4(r, lane), tag), rs, poff);
  }
  hook2();
}

template <int DLY, class Hook>
DI f4 mixer(Hook&& hook, float& xres, LAS unsigned char* L, int tid, int w, int lane, int j, __amdgpu_buffer_rsrc_t rs, int pbase, u32 tag, float b2, int& budget, int sb, float& mean_out) {
  float pw1[7], pb1[7], pw2[7];
  const int fl = tid & 255, q = tid >> 8;
#pragma unroll
  for (int i = 0; i < 7; ++i) {
    pw1[i] = asf((u32) * (const LAS u16*)(L + L_PAR + (fl + 256 * i) * 2) << 16);
    pb1[i] = asf((u32) * (const LAS u16*)(L + L_PAR + 3584 + (fl + 256 * i) * 2) << 16);
    pw2[i] = asf((u32) * (const LAS u16*)(L + L_PAR + 7168 + (fl + 256 * i) * 2) << 16);
  }
#pragma unroll
  for (int i = 0; i < 7; ++i) asm volatile("" : "+v"(pw1[i]), "+v"(pb1[i]), "+v"(pw2[i]));
  int po = pbase + tid * 4, xa = L_XL + (8 * j + 4 * q) * 4, xw = L_XL + tid * 4;
  asm volatile("" : "+v"(po), "+v"(xa), "+v"(xw));
  if (DLY) __builtin_amdgcn_s_sleep(DLY);
  if (sb == 30) STAMP(35);
  u32 p[8];
#define PBAD() !__all(((((p[0] | p[1] | p[2]) | (p[3] | p[4] | p[5])) | (p[6] | p[7])) & TMASK) == 0)
  CBAR();
#pragma unroll
  for (int i = 0; i < 8; ++i) p[i] = ld32s(rs, po + i * 2048) ^ tag;
  if (__builtin_expect(PBAD(), 0)) {
    do {
      CBAR();
#pragma unroll
      for (int i = 0; i < 8; ++i) p[i] = ld32s(rs, po + i * 2048) ^ tag;
    } while (PBAD() && --budget > 0);
  }
  STAMP(sb); STAMPV(sb + 1, budget); if (sb == 30) STAMPW(112);
  hook();
  const float x1 = xres + (((asf(p[0]) + asf(p[1])) + (asf(p[2]) + asf(p[3]))) + ((asf(p[4]) + asf(p[5])) + (asf(p[6]) + asf(p[7]))));
  xres = x1;
  *(LAS float*)(L + xw) = x1;
  float mean, var;
  ln_stats(x1, L, w, lane, mean, var);
  mean_out = mean;
  const f4 xv = *(const LAS f4*)(L + xa);
  const float r1 = __builtin_amdgcn_rsqf(var + EPS);
  const float r2 = __builtin_amdgcn_rsqf(var * r1 * r1 + EPS);
  const float rr = r1 * r2, mr = -mean * rr;
  STAMP(sb + 2); if (sb == 22) STAMPW(88);
  const f2 hA = {fmaf(xv[0], rr, mr), fmaf(xv[1], rr, mr)}, hB = {fmaf(xv[2], rr, mr), fmaf(xv[3], rr, mr)};
  f2 aA = {0.f, 0.f}, aB = {0.f, 0.f};
#pragma unroll
  for (int i = 0; i < 7; ++i) {
    const f2 w1 = {pw1[i], pw1[i]}, b1 = {pb1[i], pb1[i]}, w2 = {pw2[i], pw2[i]};
    const f2 k0 = {GK0, GK0}, k1 = {GK1, GK1}, one = {1.f, 1.f};
    {
      const f2 u = hA * w1 + b1, z = u * (u * u * k1 + k0);
      const f2 e = {__builtin_amdgcn_exp2f(z[0]), __builtin_amdgcn_exp2f(z[1])}, dd = e + one;
      const f2 rc = {__builtin_amdgcn_rcpf(dd[0]), __builtin_amdgcn_rcpf(dd[1])};
      aA += (u * rc) * w2;
    }
    {
      const f2 u = hB * w1 + b1, z = u * (u * u * k1 + k0);
      const f2 e = {__builtin_amdgcn_exp2f(z[0]), __builtin_amdgcn_exp2f(z[1])}, dd = e + one;
      const f2 rc = {__builtin_amdgcn_rcpf(dd[0]), __builtin_amdgcn_rcpf(dd[1])};
      aB += (u * rc) * w2;
    }
  }
  float t0 = (lane & 1) ? aA[1] : aA[0], t1 = (lane & 1) ? aA[0] : aA[1];
  t0 += dpp<XOR1>(t1);
  float t2 = (lane & 1) ? aB[1] : aB[0], t3 = (lane & 1) ? aB[0] : aB[1];
  t2 += dpp<XOR1>(t3);
  float s0 = (lane & 2) ? t2 : t0, s1 = (lane & 2) ? t0 : t2;
  s0 += dpp<XOR2>(s1);
  s0 += dpp<ROR4>(s0); s0 += dpp<ROR8>(s0);
  s0 = xrow_sum(s0);
  STAMP(sb + 3); if (sb == 22) STAMPW(96);
  if (lane < 4) *(LAS float*)(L + L_R2 + (w * 4 + lane) * 4) = s0;
  BAR();
  f4 res = {0.f, 0.f, 0.f, 0.f};
  if (w == 0) {
    const int qq = lane & 1;
    const f4 a = (*(const LAS f4*)(L + L_R2 + (qq * 4 + 0) * 16) + *(const LAS f4*)(L + L_R2 + (qq * 4 + 1) * 16)) +
                 (*(const LAS f4*)(L + L_R2 + (qq * 4 + 2) * 16) + *(const LAS f4*)(L + L_R2 + (qq * 4 + 3) * 16));
    const f4 xr = *(const LAS f4*)(L + L_XL + (8 * j + 4 * qq) * 4);
    res = xr + a + f4{b2, b2, b2, b2};
  }
  return res;
}

__global__ void __launch_bounds__(512, 2) fused_decode_kernel(Params p) {
  __shared__ __attribute__((aligned(1024))) unsigned char smem[L_END];
  LAS unsigned char* L = (LAS unsigned char*)smem;
  const int tid = threadIdx.x, lane = tid & 63, w = __builtin_amdgcn_readfirstlane(tid >> 6);
  const int bid = blockIdx.x;
  v4u dummy = {0u, 0u, 0u, 0u};
  {
    const int piece = (bid >> 3) * 8 + w;
    if (piece < CODE_KB) {
      u64 pc; asm volatile("s_getpc_b64 %0" : "=s"(pc));
      const u64 a = (pc & ~(u64)255) + (u64)(piece * 1024 + lane * 16);
      asm volatile("global_load_dwordx4 %0, %1, off" : "+v"(dummy) : "v"(a) : "memory");
    }
  }
  const int kvh = bid & 7, b = bid >> 6, s = (bid >> 3) & 7;
  const int j = kvh * 8 + s;
  const int g = lane >> 4, c = lane & 15;
  STAMPR(36); STAMP(0);

  const size_t slab = ((size_t)(b * 8 + kvh) * SK + (SK - WIN) + s * 512) * 256;
  const __amdgpu_buffer_rsrc_t rK = __builtin_amdgcn_make_buffer_rsrc((void*)(p.K + slab), 0, 512 * 256, 0x00020000);
  const __amdgpu_buffer_rsrc_t rV = __builtin_amdgcn_make_buffer_rsrc((void*)(p.V + slab), 0, 512 * 256, 0x00020000);
  const __amdgpu_buffer_rsrc_t rQ = __builtin_amdgcn_make_buffer_rsrc((void*)p.wq, 0, 512 * 4096 * 2, 0x00020000);
  const __amdgpu_buffer_rsrc_t rO = __builtin_amdgcn_make_buffer_rsrc((void*)p.wo, 0, 512 * 4096 * 2, 0x00020000);
  const __amdgpu_buffer_rsrc_t rs = __builtin_amdgcn_make_buffer_rsrc((void*)p.ws, 0, WS_END, 0x00020000);
  const __amdgpu_buffer_rsrc_t rX = __builtin_amdgcn_make_buffer_rsrc((void*)p.x, 0, 4096, 0x00020000);
  const u32 ldsb = (u32)(size_t)L;

  u32 ev, xraw, b2raw, dummy2 = 0;
  asm volatile("s_nop 4\n\tbuffer_load_dword %0, %1, %2, 0 offen sc1" : "=v"(ev) : "v"(WS_EP + b * 256), "s"(rs) : "memory");
  asm volatile("buffer_load_ushort %0, %1, %2, 0 offen" : "=v"(xraw) : "v"((b * 512 + tid) * 2), "s"(rX) : "memory");
  asm volatile("global_load_ushort %0, %1, %2" : "=v"(b2raw) : "v"(0), "s"(p.b2) : "memory");
  const int rot = __builtin_amdgcn_readfirstlane((int)(((u32)(bid * 8 + w) * 2654435761u) >> 28));
  const int cg = s >> 1, kh = s & 1;
  const int voq = ((256 * kh + 32 * w + 8 * g) * 4096 + kvh * 512 + 128 * cg + 8 * c) * 2;
  const int so_ = (s + OROT) & 7;
  const int voo = ((kvh * 512 + (2 * (w >> 2) + (c >> 3)) * 128 + 32 * (w & 3) + 4 * g) * 512 + 64 * so_ + 8 * (c & 7)) * 2;
  int vok[4], vov[2];
#pragma unroll
  for (int i = 0; i < 4; ++i) vok[i] = (64 * w + g) * 256 + ((c ^ (4 * i + g)) * 16);
#pragma unroll
  for (int i = 0; i < 2; ++i) vov[i] = (64 * w + g) * 256 + ((((c >> 1) ^ (g | (i << 2))) * 32) + (c & 1) * 16);
  v4u wqr[8], wor[8], kf[16];
#define LDQ(jj) asm volatile("buffer_load_dwordx4 %0, %1, %2, %3 offen" WPOL : "=v"(wqr[jj]) : "v"(voq), "s"(rQ), "s"((jj) * 8192) : "memory")
#define LDO(jj) asm volatile("buffer_load_dwordx4 %0, %1, %2, %3 offen" WPOL : "=v"(wor[jj]) : "v"(voo), "s"(rO), "s"((16 * ((jj) >> 2) + ((jj) & 3)) * 1024) : "memory")
#define LDK(i) asm volatile("s_mov_b32 m0, %0\n\ts_nop 0\n\tbuffer_load_dwordx4 %1, %2, %3 offen" KVPOL " lds" ::"s"(ldsb + L_KV + w * 16384 + (i) * 1024), "v"(vok[(i) & 3]), "s"(rK), "s"((((i) + rot) & 15) * 1024) : "memory")
#define LDV(i) asm volatile("s_mov_b32 m0, %0\n\ts_nop 0\n\tbuffer_load_dwordx4 %1, %2, %3 offen" KVPOL " lds" ::"s"(ldsb + L_KV + w * 16384 + (i) * 1024), "v"(vov[((i) >> 1) & 1]), "s"(rV), "s"((((i) + rot) & 15) * 1024) : "memory")
#define GW(d) asm volatile("s_waitcnt vmcnt(%0)" ::"n"((d) - 1) : "memory")
#define G() GW(GWIN)
#define GWIN WD
#define EV(n, i) asm volatile("s_waitcnt vmcnt(%0)" ::"n"((n) - (i) - 1) : "memory")
#define KFRAG(t)                                                                                                     \
  _Pragma("unroll") for (int st = 0; st < 4; ++st)                                                                   \
      kf[(t) * 4 + st] = *(const LAS v4u*)(L + L_KV + w * 16384 + (16 * (t) + c) * 256 + (((4 * st + g) ^ c) * 16)); \
  asm volatile("" : "+v"(kf[(t) * 4]), "+v"(kf[(t) * 4 + 1]), "+v"(kf[(t) * 4 + 2]), "+v"(kf[(t) * 4 + 3])::"memory")
  LDQ(0); LDK(0); LDQ(1); LDK(1); LDQ(2); LDK(2);
  STAMP(38);
  if (w == 7) {
    const int me = (b * 8 + kvh) * 8 + s;
    int a_ = lane < 16 ? WS_OB + me * 2048 + lane * 128
           : lane < 32 ? WS_OB + (1 << 19) + me * 2048 + (lane - 16) * 128
           : lane < 34 ? WS_OS + (lane - 32) * 32768 + me * 128
           : lane < 42 ? WS_QB + ((lane - 34) >> 2) * (4 * 8 * 4096) + ((b * 8 + kvh) * 2 + (s & 1)) * 2048 + (s >> 1) * 512 + ((lane - 34) & 3) * 128
           : lane < 46 ? WS_PB + ((lane - 42) >> 1) * 65536 + (b * 8 + kvh) * 2048 + ((s + OROT) & 7) * 256 + ((lane - 42) & 1) * 128
           : WS_XB + (b * 64 + j) * XBS;
    asm volatile("buffer_load_dword %0, %1, %2, 0 offen" : "+v"(dummy2) : "v"(a_), "s"(rs) : "memory");
  }
  G(); LDQ(3); G(); LDK(3);
  asm volatile("s_waitcnt vmcnt(8)" : "+v"(ev), "+v"(xraw), "+v"(b2raw)::"memory");
  const u32 T0 = __builtin_amdgcn_readfirstlane(ev) + 1u;
  const u32 tag = T0 & TMASK;
  float xres = asf(xraw << 16);
  const int xqa = L_XL + (256 * kh + 32 * w + 8 * g) * 4;
  v4u qa;
  {
    *(LAS float*)(L + L_XL + tid * 4) = xres;
    float mean, var;
    ln_stats(xres, L, w, lane, mean, var);
    qa = q_operand(L, xqa, mean, var);
    PIN1(qa);
  }
  G(); LDQ(4); G(); LDK(4); G(); LDQ(5); G(); LDK(5);
  G(); LDQ(6); G(); LDK(6); G(); LDQ(7); G(); LDK(7);
  EV(16, 7); STAMPW(48); KFRAG(0);
  G(); LDK(8); G(); LDK(9); G(); LDK(10); G(); LDK(11); G(); LDK(12);
  EV(21, 14);
  asm volatile("" : "+v"(wqr[0]), "+v"(wqr[1]), "+v"(wqr[2]), "+v"(wqr[3]), "+v"(wqr[4]), "+v"(wqr[5]), "+v"(wqr[6]), "+v"(wqr[7]), "+v"(dummy), "+v"(dummy2)::"memory");
  STAMP(1); STAMPW(40);
  LDK(13); LDK(14); LDK(15);
  {
#pragma unroll
    for (int k = 0; k < 2; ++k) {
      const int piece = k == 0 ? w : 8 + (w % 3);
      int gr = piece * 64 + lane; gr = gr < 672 ? gr : 671;
      const int arr = gr / 224, e = gr - arr * 224;
      const unsigned char* src = (const unsigned char*)(arr == 0 ? p.w1 : arr == 1 ? p.b1 : p.w2) + e * 16;
      asm volatile("s_mov_b32 m0, %0\n\ts_nop 0\n\tglobal_load_lds_dwordx4 %1, off" ::"s"(ldsb + L_PAR + piece * 1024), "v"(src) : "memory");
    }
  }
  LDV(0); LDV(1);
  v4u wqf[8];
  transpose8(wqr, wqf);
  const int qwr = L_RED + w * 576 + (8 * c + 4 * (g & 1)) * 4, qrd = L_RED + (tid & 3) * 576 + (tid >> 2) * 16;
  const int qpub = WS_QB + ((b * 8 + kvh) * 2 + kh) * 2048 + cg * 512 + tid * 4;
  q_gemv(wqf, qa, L, tid, lane, qwr, qrd, rs, qpub, tag);
  STAMP(2);
#undef GWIN
#define GWIN WD2
  EV(28, 15); KFRAG(1);
  G(); LDV(2); G(); LDV(3);
  EV(30, 19); KFRAG(2);
#define WAITN(n) asm volatile("s_waitcnt vmcnt(%0)" ::"n"(n) : "memory")
  G(); LDO(0); G(); LDV(4); G(); LDV(5);
  STAMP(3);
  u32 q0, q1;
  const int qo = WS_QB + (b * 8 + kvh) * 4096 + tid * 4;
  G(); asm volatile("buffer_load_dword %0, %1, %2, 0 offen sc1" : "=v"(q0) : "v"(qo), "s"(rs) : "memory");
  asm volatile("buffer_load_dword %0, %1, %2, 0 offen offset:2048 sc1" : "=v"(q1) : "v"(qo), "s"(rs) : "memory");
  WAITN(11); KFRAG(3);
  STAMP(4); STAMPW(56);
  G(); LDO(1); G(); LDV(6); G(); LDV(7); G(); LDO(2); G(); LDV(8); G(); LDV(9);
  WAITN(6);
  asm volatile("" : "+v"(q0), "+v"(q1)::"memory");
  STAMP(5); STAMPW(64);
  LDO(3); LDV(10); LDV(11); LDO(4); LDV(12); LDV(13);
  int budget = 100000;
  if (__builtin_expect(!__all((((q0 ^ tag) | (q1 ^ tag)) & TMASK) == 0), 0)) {
    _Pragma("nounroll") do {
      asm volatile("buffer_load_dword %0, %2, %3, 0 offen sc1\n\tbuffer_load_dword %1, %2, %3, 0 offen offset:2048 sc1\n\ts_waitcnt vmcnt(0)" : "=&v"(q0), "=&v"(q1) : "v"(qo), "s"(rs) : "memory");
    } while (!__all((((q0 ^ tag) | (q1 ^ tag)) & TMASK) == 0) && --budget > 0);
  }
  STAMPV(19, budget);
  const int vbase = L_KV + (g >> 1) * 16384 + (8 * (g & 1) + (c >> 2)) * 256 + ((w ^ ((c >> 2) | ((g & 1) << 2))) * 32) + (c & 3) * 8;
  const int a_qwr = L_QL + (tid >> 7) * QSTR + (tid & 127) * 2, a_qrd = L_QL + (c & 3) * QSTR + g * 16;
  const int a_mwr = L_MB + ((lane & 3) * 8 + w) * 4, a_mrd = L_MB + (c & 3) * 32;
  const int a_pwr = L_PL + (c >> 2) * PSTR + ((2 * (w & 1) + (g >> 1)) * 16 + (w >> 1) * 4 + (c & 3)) * 16 + (g & 1) * 8;
  const int a_lwr = L_LB + (w * 16 + g * 4 + (c & 3)) * 4;
  f4 acc[4];
  float M = attn_scores(asf(q0 ^ tag) + asf(q1 ^ tag), kf, L, a_qwr, a_qrd, a_mwr, a_mrd, a_pwr, a_lwr, lane);
  STAMP(6);
#undef GWIN
#define GWIN WD3
  G(); LDO(5); G(); LDO(6); G(); LDO(7); G(); LDV(14); G(); LDV(15);
  WAITN(8);
  BAR();
  const int pub_o = ((b * 8 + kvh) * 8 + s) * 2048 + w * 256 + lane * 4, pub_s = ((b * 8 + kvh) * 8 + s) * 128 + (lane & 7) * 16;
  attn_publish_stats(M, L, w, lane, rs, WS_OS + pub_s, tag, T0);
#pragma unroll
  for (int u = 0; u < 4; ++u) acc[u] = f4{0.f, 0.f, 0.f, 0.f};
  pv_chunk(0, acc, L, vbase, lane);
  pv_chunk(1, acc, L, vbase, lane);
  pv_chunk(2, acc, L, vbase, lane);
  v4u pf3 = *(const LAS v4u*)(L + L_PL + 3 * PSTR + lane * 16);
  PIN1(pf3);
  SB();
  WAITN(0);
  STAMP(7); STAMPW(72);
  BAR();
  {
    v4u vf3[4];
#pragma unroll
    for (int u = 0; u < 4; ++u) vf3[u] = v_frag(3, u, L, vbase);
    SB();
#pragma unroll
    for (int u = 0; u < 4; ++u) acc[u] = mfma(pf3, vf3[u], acc[u]);
  }
  int po1 = WS_OB + pub_o;
  attn_publish(acc, lane, rs, po1, T0);
  STAMP(8);
  v4u wof[8];
  const float b2 = asf(b2raw << 16);
  int g_oo = (b * 8 + kvh) * 8 * 2048 + (lane >> 3) * 2048 + 2 * (w & 3) * 256 + (w >> 2) * 128 + (lane & 7) * 16;
  int g_so = (b * 8 + kvh) * 8 * 128 + (lane & 7) * 128 + (2 * (w >> 2) + ((lane >> 3) & 1)) * 16;
  int o_xw = L_SC2 + w * 1024 + (lane ^ (lane >> 4)) * 16, o_xr = L_SC2 + w * 1024 + (((c & 7) * 8 + 4 * (c >> 3) + g) ^ ((c & 7) >> 1)) * 16;
  int o_wr = L_RED + (2 * w + (g >> 1)) * 320 + (8 * (c & 7) + 4 * (g & 1)) * 4, o_rd = L_RED + (tid & 3) * 320 + (tid >> 2) * 16;
  int o_pub = WS_PB + (b * 8 + kvh) * 2048 + so_ * 256 + tid * 4;
  g_oo += WS_OB; g_so += WS_OS;
  PIN4(g_oo, g_so, o_wr, o_rd); PIN1(o_pub); PIN2(o_xw, o_xr);
  BAR();

  if (DLY_B) __builtin_amdgcn_s_sleep(DLY_B);
  o_gemv<true>([&]() { q_gemv1(wqf, v4u{0x3f803f80u, 0x3f803f80u, 0x3f803f80u, 0x3f803f80u}, L, lane, L_PL + w * 544 + (8 * c + 4 * (g & 1)) * 4); },
               [&]() { if (w == 2 || w == 3) q_gemv2<544>(L, lane, L_PL + (lane & 3) * 544 + ((w - 2) * 16 + (lane >> 2)) * 16, rs, qpub - WS_QB + WS_CS - 128 * 4, tag); }, wof, wor, L, tid, lane, rs, g_oo, g_so, o_xw, o_xr, o_wr, o_rd, o_pub, tag, T0, budget, w, 20);
  STAMP(10);
  u32 cs0, cs1;
  const int cso = WS_CS + (b * 8 + kvh) * 4096 + tid * 4;
  float mprev, csum;
  {
    const f4 r = mixer<DLY_CD>([&]() { cs0 = ld32s(rs, cso); cs1 = ld32s(rs, cso + 2048); }, xres, L, tid, w, lane, j, rs, WS_PB + b * 8 * 2048, tag, b2, budget, 22, mprev);
    cs0 ^= tag; cs1 ^= tag;
    if (__builtin_expect(!__all(((cs0 | cs1) & TMASK) == 0), 0)) {
      do { CBAR(); cs0 = ld32s(rs, cso) ^ tag; cs1 = ld32s(rs, cso + 2048) ^ tag; } while (!__all(((cs0 | cs1) & TMASK) == 0) && --budget > 0);
    }
    csum = asf(cs0) + asf(cs1);
    PIN1(csum);
    if (tid < XBS / 16) { v4u o = {tagf(r[0], tag), tagf(r[1], tag), tagf(r[2], tag), tagf(r[3], tag)}; st128s(o, rs, WS_XB + (b * 64 + j) * XBS + tid * 16); }
  }
  STAMP(11);
  float rstd2, dmean;
  int qo2 = qo + 4 * 8 * 4096, b0 = a_qwr, b1 = a_qrd, b2_ = a_mwr, b3 = a_mrd, b4 = a_pwr, b5 = a_lwr, pr = L_PL + lane * 16, po2 = WS_OB + (1 << 19) + pub_o, ps2 = WS_OS + 32768 + pub_s;
  PIN4(qo2, b0, b1, b2_); PIN4(b3, b4, b5, pr); PIN2(po2, ps2);
  {
    int xo_ = WS_XB + (b * 64 + (tid >> 3)) * XBS + (tid & 7) * 4, ao = WS_XB + (b * 64 + 32 * kh + 4 * w + g) * XBS + (c & 1) * 16, qw = qwr, qr = qrd, qp = qpub + 4 * 8 * 4096;
    PIN4(xo_, ao, qw, qr); PIN1(qp);
    if (DLY_E) __builtin_amdgcn_s_sleep(DLY_E);
    v4u a0 = {tag, tag, tag, tag};
    u32 xv;
#define EBAD() !__all((((a0[0] | a0[1] | a0[2]) | (a0[3] | xv)) & TMASK) == 0)
    CBAR();
    if (c < 2) a0 = ld128s(rs, ao);
    xv = ld32s(rs, xo_) ^ tag;
#pragma unroll
    for (int i = 0; i < 4; ++i) a0[i] ^= tag;
    if (__builtin_expect(EBAD(), 0)) {
      do {
        CBAR();
        a0 = v4u{tag, tag, tag, tag};
        if (c < 2) a0 = ld128s(rs, ao);
        xv = ld32s(rs, xo_) ^ tag;
#pragma unroll
        for (int i = 0; i < 4; ++i) a0[i] ^= tag;
      } while (EBAD() && --budget > 0);
    }
    STAMP(26); STAMPV(27, budget); STAMPW(120);
    const u32 p0 = cvtpk(asf(a0[0]) - mprev, asf(a0[1]) - mprev), p1 = cvtpk(asf(a0[2]) - mprev, asf(a0[3]) - mprev);
    const v4u qa2 = {rowbc<0>(p0), rowbc<0>(p1), rowbc<1>(p0), rowbc<1>(p1)};
    STAMP(12);
    q_gemv(wqf, qa2, L, tid, lane, qw, qr, rs, qp, tag);
    STAMP(13);
    if (DLY_A) __builtin_amdgcn_s_sleep(DLY_A);
    CBAR();
    q0 = ld32s(rs, qo2); q1 = ld32s(rs, qo2 + 2048);
    CBAR();
    xres = asf(xv);
    float mean, var;
    ln_stats(xres, L, w, lane, mean, var);
    rstd2 = __builtin_amdgcn_rsqf(var + EPS);
    dmean = (mean - mprev) * csum;
    PIN2(rstd2, dmean);
  }
  {
    v4u vf[16];
#pragma unroll
    for (int t = 0; t < 4; ++t)
#pragma unroll
      for (int u = 0; u < 4; ++u) vf[t * 4 + u] = v_frag(t, u, L, vbase);
    PIN8(vf); { v4u* v2 = vf + 8; PIN8(v2); }
    q0 ^= tag; q1 ^= tag;
    if (__builtin_expect(!__all(((q0 | q1) & TMASK) == 0), 0)) {
      do { CBAR(); q0 = ld32s(rs, qo2) ^ tag; q1 = ld32s(rs, qo2 + 2048) ^ tag; } while (!__all(((q0 | q1) & TMASK) == 0) && --budget > 0);
    }
    STAMP(14); STAMPV(18, budget);
    M = attn_scores(rstd2 * ((asf(q0) + asf(q1)) - dmean), kf, L, b0, b1, b2_, b3, b4, b5, lane);
    BAR();
    v4u pf[4];
#pragma unroll
    for (int t = 0; t < 4; ++t) pf[t] = *(const LAS v4u*)(L + pr + t * PSTR);
    SB();
#pragma unroll
    for (int t = 0; t < 4; ++t)
#pragma unroll
      for (int u = 0; u < 4; ++u) acc[u] = mfma(pf[t], vf[t * 4 + u], t == 0 ? f4{0.f, 0.f, 0.f, 0.f} : acc[u]);
    attn_publish_stats(M, L, w, lane, rs, ps2, tag, T0);
    attn_publish(acc, lane, rs, po2, T0);
  }
  STAMP(15);
  g_oo += 1 << 19; g_so += 32768; o_pub += 65536;
  PIN2(g_oo, g_so); PIN1(o_pub);
  if (DLY_B2) __builtin_amdgcn_s_sleep(DLY_B2);
  o_gemv<false>([]() {}, []() {}, wof, wor, L, tid, lane, rs, g_oo, g_so, o_xw, o_xr, o_wr, o_rd, o_pub, tag, T0, budget, w, 28);
  STAMP(16);
  {
    const f4 r = mixer<DLY_CD2>([]() {}, xres, L, tid, w, lane, j, rs, WS_PB + 65536 + b * 8 * 2048, tag, b2, budget, 30, mprev);
    if (w == 0) {
      const u32 lo = cvtpk(r[0], r[1]), hi = cvtpk(r[2], r[3]);
      if (lane < 2) { v2u o = {lo, hi}; *(GAS v2u*)((GAS unsigned char*)p.out + (b * 512 + 8 * j + 4 * lane) * 2) = o; }
      if (lane == 0 && j == 0) __builtin_amdgcn_raw_buffer_store_b32(T0, rs, WS_EP + b * 256, 0, 16);
    }
  }
  STAMP(17); STAMPR(37);
}

extern "C" void kernel_launch(void* const* d_in, const int* in_sizes, int n_in, void* d_out, int out_size, void* d_ws, size_t ws_size, hipStream_t stream) {
  Params p;
  p.x = (const u16*)d_in[0]; p.K = (const unsigned char*)d_in[1]; p.V = (const unsigned char*)d_in[2];
  p.wq = (const unsigned char*)d_in[3]; p.wo = (const unsigned char*)d_in[4];
  p.w1 = (const u16*)d_in[6]; p.b1 = (const u16*)d_in[7]; p.w2 = (const u16*)d_in[8]; p.b2 = (const u16*)d_in[9];
  p.out = (u16*)d_out; p.ws = (unsigned char*)d_ws;
  hipLaunchKernelGGL(fused_decode_kernel, dim3(256), dim3(512), 0, stream, p);
}
```

```cpp
#include <hip/hip_runtime.h>

typedef unsigned int u32;
typedef unsigned short u16;
typedef unsigned long long u64;
typedef u32 v4u __attribute__((ext_vector_type(4)));
typedef u32 v2u __attribute__((ext_vector_type(2)));
typedef float f4 __attribute__((ext_vector_type(4)));
typedef float f2 __attribute__((ext_vector_type(2)));
typedef short s8v __attribute__((ext_vector_type(8)));
typedef short s4v __attribute__((ext_vector_type(4)));
typedef __bf16 bf2v __attribute__((ext_vector_type(2)));
#define LAS __attribute__((address_space(3)))
#define GAS __attribute__((address_space(1)))
#define DI __device__ __forceinline__

#define DLY_B 10
#define DLY_CD 8
#define DLY_E 16
#define DLY_A 6
#define KVPOL " nt"
#define WPOL " nt"
#define PRETOUCH 1
#define OROT 4
#define XBS 128
#define DLY_B2 13
#define DLY_CD2 7
#define WD2 5
#define WD3 5
#define WD 5
#define CODE_KB 18

constexpr int SK = 16384, WIN = 4096;
constexpr float EPS = 1e-5f;
constexpr float SCL = 0.08838834764831845f * 1.4426950408889634f;
constexpr float GK0 = -2.0f * 0.7978845608028654f * 1.4426950408889634f;
constexpr float GK1 = GK0 * 0.044715f;

constexpr int WS_EP = 0;
constexpr int WS_QB = 4096;
constexpr int WS_CS = WS_QB + 2 * 4 * 8 * 2 * 2048;
constexpr int WS_OB = WS_CS + 4 * 8 * 2 * 2048;
constexpr int WS_OS = WS_OB + 2 * 4 * 8 * 8 * 2048;
constexpr int WS_PB = WS_OS + 2 * 4 * 8 * 8 * 128;
constexpr int WS_XB = WS_PB + 2 * 4 * 8 * 2048;
constexpr int WS_END = WS_XB + 4 * 64 * XBS;

constexpr int L_KV = 0;
constexpr int L_PAR = 131072;
constexpr int PSTR = 1088;
constexpr int L_PL = L_PAR + 11264;
constexpr int QSTR = 288;
constexpr int L_RED = L_PL + 4 * PSTR;
constexpr int L_ST = L_RED + 5120;
constexpr int L_MB = L_ST + 64;
constexpr int L_LB = L_MB + 128;
constexpr int L_R2 = L_LB + 512;
constexpr int L_QL = L_R2 + 128;
constexpr int L_XL = L_QL + 4 * QSTR;
constexpr int L_SC2 = L_QL;
constexpr int L_END = L_SC2 + 8192;
static_assert(L_END <= 163840, "LDS");

struct Params {
  const u16* x; const unsigned char* K; const unsigned char* V; const unsigned char* wq; const unsigned char* wo;
  const u16* w1; const u16* b1; const u16* w2; const u16* b2; u16* out; unsigned char* ws;
};

template <int CTRL> DI float dpp(float x) {
  return __builtin_bit_cast(float, __builtin_amdgcn_update_dpp(0, __builtin_bit_cast(int, x), CTRL, 0xf, 0xf, true));
}
constexpr int XOR1 = 0xB1, XOR2 = 0x4E, HMIR = 0x141, MIR = 0x140, ROR4 = 0x124, ROR8 = 0x128;
DI float asf(u32 x) { return __builtin_bit_cast(float, x); }
DI u32 asu(float x) { return __builtin_bit_cast(u32, x); }
DI u32 cvtpk(float lo, float hi) { f2 f = {lo, hi}; return __builtin_bit_cast(u32, __builtin_convertvector(f, bf2v)); }
DI f4 mfma(v4u a, v4u b, f4 c) {
  return __builtin_amdgcn_mfma_f32_16x16x32_bf16(__builtin_bit_cast(s8v, a), __builtin_bit_cast(s8v, b), c, 0, 0, 0);
}
DI float fmx(float a, float b) { return __builtin_elementwise_maximum(a, b); }
DI float xrow_max(float x) {
  auto s = __builtin_amdgcn_permlane16_swap(asu(x), asu(x), false, false);
  x = fmx(asf(s[0]), asf(s[1]));
  auto t = __builtin_amdgcn_permlane32_swap(asu(x), asu(x), false, false);
  return fmx(asf(t[0]), asf(t[1]));
}
DI float quad_reduce4(f4 r, int lane) {
  float t0 = (lane & 1) ? r[1] : r[0], t1 = (lane & 1) ? r[0] : r[1];
  t0 += dpp<XOR1>(t1);
  float t2 = (lane & 1) ? r[3] : r[2], t3 = (lane & 1) ? r[2] : r[3];
  t2 += dpp<XOR1>(t3);
  float s0 = (lane & 2) ? t2 : t0; const float s1 = (lane & 2) ? t0 : t2;
  s0 += dpp<XOR2>(s1);
  return s0;
}
DI void st32s(u32 v, __amdgpu_buffer_rsrc_t rs, int off) { __builtin_amdgcn_raw_buffer_store_b32(v, rs, off, 0, 16); }
template <int N> DI u32 rowbc(u32 x) { return (u32)__builtin_amdgcn_update_dpp(0, (int)x, 0x150 + N, 0xf, 0xf, false); }
DI u32 xo(u32 a, u32 t, u32 acc) { return __builtin_amdgcn_bitop3_b32(a, t, acc, 0xBE); }
#define PIN1(a) asm volatile("" : "+v"(a))
#define PIN2(a, b) asm volatile("" : "+v"(a), "+v"(b))
#define PIN4(a, b, c, d) asm volatile("" : "+v"(a), "+v"(b), "+v"(c), "+v"(d))
#define PIN8(x) asm volatile("" : "+v"(x[0]), "+v"(x[1]), "+v"(x[2]), "+v"(x[3]), "+v"(x[4]), "+v"(x[5]), "+v"(x[6]), "+v"(x[7]))
DI float xrow_sum(float x) {
  auto s = __builtin_amdgcn_permlane16_swap(asu(x), asu(x), false, false);
  x = asf(s[0]) + asf(s[1]);
  auto t = __builtin_amdgcn_permlane32_swap(asu(x), asu(x), false, false);
  return asf(t[0]) + asf(t[1]);
}
#define BAR() asm volatile("s_waitcnt lgkmcnt(0)\n\ts_barrier" ::: "memory")
#define CBAR() asm volatile("" ::: "memory")
#define SB() __builtin_amdgcn_sched_barrier(0)

constexpr u32 TMASK = 0x3ffu;
DI u32 tagf(float v, u32 tag) { return __builtin_amdgcn_bitop3_b32(asu(v) + 0x200u, TMASK, tag, 0xBA); }
DI float untag(u32 v) { return asf(v & ~TMASK); }

DI u32 ld32s(__amdgpu_buffer_rsrc_t rs, int off) { return __builtin_amdgcn_raw_buffer_load_b32(rs, off, 0, 16); }
DI v4u ld128s(__amdgpu_buffer_rsrc_t rs, int off) { return __builtin_amdgcn_raw_buffer_load_b128(rs, off, 0, 16); }
DI void st128s(v4u v, __amdgpu_buffer_rsrc_t rs, int off) { __builtin_amdgcn_raw_buffer_store_b128(v, rs, off, 0, 16); }

DI void ln_stats(float x, LAS unsigned char* L, int w, int lane, float& mean, float& var) {
  const float c = asf(__builtin_amdgcn_readfirstlane(asu(x)));
  const float d = x - c;
  float s1 = d, s2 = d * d;
  s1 += dpp<XOR1>(s1); s2 += dpp<XOR1>(s2);
  s1 += dpp<XOR2>(s1); s2 += dpp<XOR2>(s2);
  s1 += dpp<HMIR>(s1); s2 += dpp<HMIR>(s2);
  s1 += dpp<MIR>(s1); s2 += dpp<MIR>(s2);
  asm volatile("s_nop 1\n\tv_add_f32_dpp %0, %0, %0 row_bcast:15 row_mask:0xa bank_mask:0xf\n\tv_add_f32_dpp %1, %1, %1 row_bcast:15 row_mask:0xa bank_mask:0xf\n\t"
               "s_nop 0\n\tv_add_f32_dpp %0, %0, %0 row_bcast:31 row_mask:0xc bank_mask:0xf\n\tv_add_f32_dpp %1, %1, %1 row_bcast:31 row_mask:0xc bank_mask:0xf"
               : "+v"(s1), "+v"(s2));
  const f2 pv = {fmaf(s1, 1.0f / 64, c), fmaf(s1 * s1, -1.0f / 64, s2)};
  if (lane == 63) *(LAS f2*)(L + L_ST + w * 8) = pv;
  BAR();
  const f2 v = *(const LAS f2*)(L + L_ST + (lane & 7) * 8);
  float m = v.x;
  m += dpp<XOR1>(m); m += dpp<XOR2>(m); m += dpp<HMIR>(m);
  mean = m * 0.125f;
  const float dv = v.x - mean;
  float t = fmaf(64.0f * dv, dv, v.y);
  t += dpp<XOR1>(t); t += dpp<XOR2>(t); t += dpp<HMIR>(t);
  var = t * (1.0f / 512);
}

DI void transpose8(const v4u (&raw)[8], v4u (&fr)[8]) {
#pragma unroll
  for (int i = 0; i < 8; ++i) {
    const u32 sel = (i & 1) ? 0x07060302u : 0x05040100u;
#pragma unroll
    for (int m = 0; m < 4; ++m) fr[i][m] = __builtin_amdgcn_perm(raw[2 * m + 1][i >> 1], raw[2 * m][i >> 1], sel);
  }
}

DI v4u q_operand(LAS unsigned char* L, int xaddr, float mean, float var) {
  const f4 a = *(const LAS f4*)(L + xaddr), b = *(const LAS f4*)(L + xaddr + 16);
  const float rs_ = __builtin_amdgcn_rsqf(var + EPS), mr = -mean * rs_;
  return v4u{cvtpk(fmaf(a[0], rs_, mr), fmaf(a[1], rs_, mr)), cvtpk(fmaf(a[2], rs_, mr), fmaf(a[3], rs_, mr)),
             cvtpk(fmaf(b[0], rs_, mr), fmaf(b[1], rs_, mr)), cvtpk(fmaf(b[2], rs_, mr), fmaf(b[3], rs_, mr))};
}

DI void q_gemv1(const v4u (&wf)[8], v4u a, LAS unsigned char* L, int lane, int wraddr) {
  f4 d[8];
#pragma unroll
  for (int i = 0; i < 8; ++i) d[i] = mfma(a, wf[i], f4{0.f, 0.f, 0.f, 0.f});
  f4 v;
#pragma unroll
  for (int m = 0; m < 4; ++m) v[m] = (lane & 16) ? d[4 + m][0] : d[m][0];
  if (lane < 32) *(LAS f4*)(L + wraddr) = v;
}
template <int STR>
DI void q_gemv2(LAS unsigned char* L, int lane, int rdaddr, __amdgpu_buffer_rsrc_t rs, int qoff, u32 tag) {
  const f4 r = *(const LAS f4*)(L + rdaddr) + *(const LAS f4*)(L + rdaddr + 4 * STR);
  st32s(tagf(quad_reduce4(r, lane), tag), rs, qoff);
}
DI void q_gemv(const v4u (&wf)[8], v4u a, LAS unsigned char* L, int tid, int lane, int wraddr, int rdaddr, __amdgpu_buffer_rsrc_t rs, int qoff, u32 tag) {
  q_gemv1(wf, a, L, lane, wraddr);
  BAR();
  if (tid < 128) q_gemv2<576>(L, lane, rdaddr, rs, qoff, tag);
}

DI float attn_scores(float qv, const v4u (&kf)[16], LAS unsigned char* L, int qwr, int qrd, int mwr, int mrd, int pwr, int lwr, int lane) {
  *(LAS u16*)(L + qwr) = (u16)(cvtpk(qv, qv) & 0xffffu);
  BAR();
  v4u qf[4];
#pragma unroll
  for (int st = 0; st < 4; ++st) qf[st] = *(const LAS v4u*)(L + qrd + st * 64);
  SB();
  f4 d[4];
#pragma unroll
  for (int st = 0; st < 4; ++st)
#pragma unroll
    for (int t = 0; t < 4; ++t) d[t] = mfma(kf[t * 4 + st], qf[st], st == 0 ? f4{0.f, 0.f, 0.f, 0.f} : d[t]);
  float sc[4];
#pragma unroll
  for (int i = 0; i < 4; ++i) {
    const float a = (lane & 4) ? d[1][i] : d[0][i], bq = (lane & 4) ? d[3][i] : d[2][i];
    sc[i] = (lane & 8) ? bq : a;
  }
  float m = fmx(fmx(sc[0], sc[1]), fmx(sc[2], sc[3]));
  m = fmx(m, dpp<ROR4>(m)); m = fmx(m, dpp<ROR8>(m));
  m = xrow_max(m);
  if (lane < 4) *(LAS float*)(L + mwr) = m;
  BAR();
  const f4 m0 = *(const LAS f4*)(L + mrd), m1 = *(const LAS f4*)(L + mrd + 16);
  float M = fmx(fmx(fmx(m0[0], m0[1]), fmx(m0[2], m0[3])), fmx(fmx(m1[0], m1[1]), fmx(m1[2], m1[3])));
  M = asf(asu(M * SCL) & ~TMASK);
  float p[4];
#pragma unroll
  for (int i = 0; i < 4; ++i) p[i] = __builtin_amdgcn_exp2f(fmaf(sc[i], SCL, -M));
  v2u pk = {cvtpk(p[0], p[1]), cvtpk(p[2], p[3])};
  *(LAS v2u*)(L + pwr) = pk;
  float l = (p[0] + p[1]) + (p[2] + p[3]);
  l += dpp<ROR4>(l); l += dpp<ROR8>(l);
  if ((lane & 12) == 0) *(LAS float*)(L + lwr) = l;
  return M;
}

DI v4u v_frag(int t, int u, LAS unsigned char* L, int vbase) {
  const s4v lo = __builtin_amdgcn_ds_read_tr16_b64_v4i16((LAS s4v*)(L + vbase + u * 32768 + t * 4096));
  const s4v hi = __builtin_amdgcn_ds_read_tr16_b64_v4i16((LAS s4v*)(L + vbase + u * 32768 + t * 4096 + 1024));
  const v2u l2 = __builtin_bit_cast(v2u, lo), h2 = __builtin_bit_cast(v2u, hi);
  return v4u{l2[0], l2[1], h2[0], h2[1]};
}
DI void pv_chunk(int t, f4 (&acc)[4], LAS unsigned char* L, int vbase, int lane) {
  const v4u pf = *(const LAS v4u*)(L + L_PL + t * PSTR + lane * 16);
#pragma unroll
  for (int u = 0; u < 4; ++u) acc[u] = mfma(pf, v_frag(t, u, L, vbase), acc[u]);
}

DI void attn_publish_stats(float M, LAS unsigned char* L, int w, int lane, __amdgpu_buffer_rsrc_t rs, int saddr, u32 tag, u32 T0) {
  if (w == 7) {
    float l = *(const LAS float*)(L + L_LB + lane * 4) + *(const LAS float*)(L + L_LB + (lane + 64) * 4);
    l += dpp<ROR4>(l); l += dpp<ROR8>(l);
    l = xrow_sum(l);
    if (lane < 8) { v4u sv = {asu(M) | tag, tagf(l, tag), T0, T0}; st128s(sv, rs, saddr); }
  }
}
template <int RM> DI float rowput(float old, float x) {
  return __builtin_bit_cast(float, __builtin_amdgcn_update_dpp(__builtin_bit_cast(int, old), __builtin_bit_cast(int, x), 0xE4, RM, 0xf, false));
}
DI void attn_publish(const f4 (&acc)[4], int lane, __amdgpu_buffer_rsrc_t rs, int oaddr, u32 T0) {
  float y[4];
#pragma unroll
  for (int h = 0; h < 4; ++h) y[h] = rowput<0x8>(rowput<0x4>(rowput<0x2>(acc[0][h], acc[1][h]), acc[2][h]), acc[3][h]);
  auto s01 = __builtin_amdgcn_permlane16_swap(asu(y[0]), asu(y[1]), false, false);
  auto s23 = __builtin_amdgcn_permlane16_swap(asu(y[2]), asu(y[3]), false, false);
  const float z01 = asf(s01[0]) + asf(s01[1]), z23 = asf(s23[0]) + asf(s23[1]);
  auto sf = __builtin_amdgcn_permlane32_swap(asu(z01), asu(z23), false, false);
  const float v = asf(sf[0]) + asf(sf[1]);
  st32s(__builtin_amdgcn_perm(cvtpk(v, v), T0, 0x05040100u), rs, oaddr);
}

DI bool o_check(const v4u& x0, const v4u& x1, const v4u& sv, u32 T0) {
  u32 d = xo(x0[0], T0, 0u);
  d = xo(x0[1], T0, d); d = xo(x0[2], T0, d); d = xo(x0[3], T0, d);
  d = xo(x1[0], T0, d); d = xo(x1[1], T0, d); d = xo(x1[2], T0, d); d = xo(x1[3], T0, d);
  d = xo(sv[0], T0, d); d = xo(sv[1], T0, d);
  d = xo(sv[3], T0, xo(sv[2], T0, d & TMASK));
  return __all(d == 0);
}
template <bool FIRST, class Hook, class Hook2>
DI void o_gemv(Hook&& hook, Hook2&& hook2, v4u (&wf)[8], v4u (&wraw)[8], LAS unsigned char* L, int tid, int lane, __amdgpu_buffer_rsrc_t rs, int oo, int so, int xw, int xr, int wraddr, int rdaddr, int poff, u32 tag, u32 T0, int& budget) {
  v4u x0, x1, sv = {T0, T0, T0, T0};
  CBAR();
  x0 = ld128s(rs, oo); x1 = ld128s(rs, oo + 256);
  if (lane < 16) sv = ld128s(rs, so);
  if (FIRST) {
    asm volatile("" : "+v"(wraw[0]), "+v"(wraw[1]), "+v"(wraw[2]), "+v"(wraw[3]), "+v"(wraw[4]), "+v"(wraw[5]), "+v"(wraw[6]), "+v"(wraw[7])::"memory");
    transpose8(wraw, wf);
    PIN8(wf);
    hook();
  }
  if (__builtin_expect(!o_check(x0, x1, sv, T0), 0)) {
    do {
      CBAR();
      x0 = ld128s(rs, oo); x1 = ld128s(rs, oo + 256);
      if (lane < 16) sv = ld128s(rs, so);
    } while (!o_check(x0, x1, sv, T0) && --budget > 0);
  }
  const v4u pk = {__builtin_amdgcn_perm(x0[1], x0[0], 0x07060302u), __builtin_amdgcn_perm(x0[3], x0[2], 0x07060302u),
                  __builtin_amdgcn_perm(x1[1], x1[0], 0x07060302u), __builtin_amdgcn_perm(x1[3], x1[2], 0x07060302u)};
  *(LAS v4u*)(L + xw) = pk;
  CBAR();
  const v4u a = *(const LAS v4u*)(L + xr);
  const float ms = untag(sv[0]), ls = untag(sv[1]);
  float M = ms;
  M = fmx(M, dpp<XOR1>(M)); M = fmx(M, dpp<XOR2>(M)); M = fmx(M, dpp<HMIR>(M));
  const float f = __builtin_amdgcn_exp2f(ms - M);
  float Ls = f * ls;
  Ls += dpp<XOR1>(Ls); Ls += dpp<XOR2>(Ls); Ls += dpp<HMIR>(Ls);
  const float cs = f * __builtin_amdgcn_rcpf(Ls);
  f4 d[8];
#pragma unroll
  for (int i = 0; i < 8; ++i) d[i] = mfma(a, wf[i], f4{0.f, 0.f, 0.f, 0.f});
  const int bp = (lane >> 4) * 16;
  const float cr0 = asf(__builtin_amdgcn_ds_bpermute(bp, asu(cs))), cr1 = asf(__builtin_amdgcn_ds_bpermute(bp + 4, asu(cs)));
  const float cr2 = asf(__builtin_amdgcn_ds_bpermute(bp + 8, asu(cs))), cr3 = asf(__builtin_amdgcn_ds_bpermute(bp + 12, asu(cs)));
  const f2 c01 = {cr0, cr1}, c23 = {cr2, cr3};
  float e[8];
#pragma unroll
  for (int i = 0; i < 8; ++i) { const f2 t = f2{d[i][2], d[i][3]} * c23 + f2{d[i][0], d[i][1]} * c01; e[i] = t[0] + t[1]; }
  f4 v;
#pragma unroll
  for (int m = 0; m < 4; ++m) { auto t = __builtin_amdgcn_permlane16_swap(asu(e[m]), asu(e[m + 4]), false, false); v[m] = asf(t[0]) + asf(t[1]); }
  if ((lane >> 5) == ((lane >> 3) & 1)) *(LAS f4*)(L + wraddr) = v;
  BAR();
  if (tid < 64) {
    const f4 r = (*(const LAS f4*)(L + rdaddr) + *(const LAS f4*)(L + rdaddr + 4 * 320)) + (*(const LAS f4*)(L + rdaddr + 8 * 320) + *(const LAS f4*)(L + rdaddr + 12 * 320));
    st32s(tagf(quad_reduce4(r, lane), tag), rs, poff);
  }
  hook2();
}

template <int DLY, class Hook>
DI f4 mixer(Hook&& hook, float& xres, LAS unsigned char* L, int tid, int w, int lane, int j, __amdgpu_buffer_rsrc_t rs, int pbase, u32 tag, float b2, int& budget, float& mean_out) {
  float pw1[7], pb1[7], pw2[7];
  const int fl = tid & 255, q = tid >> 8;
#pragma unroll
  for (int i = 0; i < 7; ++i) {
    pw1[i] = asf((u32) * (const LAS u16*)(L + L_PAR + (fl + 256 * i) * 2) << 16);
    pb1[i] = asf((u32) * (const LAS u16*)(L + L_PAR + 3584 + (fl + 256 * i) * 2) << 16);
    pw2[i] = asf((u32) * (const LAS u16*)(L + L_PAR + 7168 + (fl + 256 * i) * 2) << 16);
  }
#pragma unroll
  for (int i = 0; i < 7; ++i) asm volatile("" : "+v"(pw1[i]), "+v"(pb1[i]), "+v"(pw2[i]));
  int po = pbase + tid * 4, xa = L_XL + (8 * j + 4 * q) * 4, xw = L_XL + tid * 4;
  asm volatile("" : "+v"(po), "+v"(xa), "+v"(xw));
  if (DLY) __builtin_amdgcn_s_sleep(DLY);
  u32 p[8];
#define PBAD() !__all(((((p[0] | p[1] | p[2]) | (p[3] | p[4] | p[5])) | (p[6] | p[7])) & TMASK) == 0)
  CBAR();
#pragma unroll
  for (int i = 0; i < 8; ++i) p[i] = ld32s(rs, po + i * 2048) ^ tag;
  if (__builtin_expect(PBAD(), 0)) {
    do {
      CBAR();
#pragma unroll
      for (int i = 0; i < 8; ++i) p[i] = ld32s(rs, po + i * 2048) ^ tag;
    } while (PBAD() && --budget > 0);
  }
  hook();
  const float x1 = xres + (((asf(p[0]) + asf(p[1])) + (asf(p[2]) + asf(p[3]))) + ((asf(p[4]) + asf(p[5])) + (asf(p[6]) + asf(p[7]))));
  xres = x1;
  *(LAS float*)(L + xw) = x1;
  float mean, var;
  ln_stats(x1, L, w, lane, mean, var);
  mean_out = mean;
  const f4 xv = *(const LAS f4*)(L + xa);
  const float r1 = __builtin_amdgcn_rsqf(var + EPS);
  const float r2 = __builtin_amdgcn_rsqf(var * r1 * r1 + EPS);
  const float rr = r1 * r2, mr = -mean * rr;
  const f2 hA = {fmaf(xv[0], rr, mr), fmaf(xv[1], rr, mr)}, hB = {fmaf(xv[2], rr, mr), fmaf(xv[3], rr, mr)};
  f2 aA = {0.f, 0.f}, aB = {0.f, 0.f};
#pragma unroll
  for (int i = 0; i < 7; ++i) {
    const f2 w1 = {pw1[i], pw1[i]}, b1 = {pb1[i], pb1[i]}, w2 = {pw2[i], pw2[i]};
    const f2 k0 = {GK0, GK0}, k1 = {GK1, GK1}, one = {1.f, 1.f};
    {
      const f2 u = hA * w1 + b1, z = u * (u * u * k1 + k0);
      const f2 e = {__builtin_amdgcn_exp2f(z[0]), __builtin_amdgcn_exp2f(z[1])}, dd = e + one;
      const f2 rc = {__builtin_amdgcn_rcpf(dd[0]), __builtin_amdgcn_rcpf(dd[1])};
      aA += (u * rc) * w2;
    }
    {
      const f2 u = hB * w1 + b1, z = u * (u * u * k1 + k0);
      const f2 e = {__builtin_amdgcn_exp2f(z[0]), __builtin_amdgcn_exp2f(z[1])}, dd = e + one;
      const f2 rc = {__builtin_amdgcn_rcpf(dd[0]), __builtin_amdgcn_rcpf(dd[1])};
      aB += (u * rc) * w2;
    }
  }
  float t0 = (lane & 1) ? aA[1] : aA[0], t1 = (lane & 1) ? aA[0] : aA[1];
  t0 += dpp<XOR1>(t1);
  float t2 = (lane & 1) ? aB[1] : aB[0], t3 = (lane & 1) ? aB[0] : aB[1];
  t2 += dpp<XOR1>(t3);
  float s0 = (lane & 2) ? t2 : t0, s1 = (lane & 2) ? t0 : t2;
  s0 += dpp<XOR2>(s1);
  s0 += dpp<ROR4>(s0); s0 += dpp<ROR8>(s0);
  s0 = xrow_sum(s0);
  if (lane < 4) *(LAS float*)(L + L_R2 + (w * 4 + lane) * 4) = s0;
  BAR();
  f4 res = {0.f, 0.f, 0.f, 0.f};
  if (w == 0) {
    const int qq = lane & 1;
    const f4 a = (*(const LAS f4*)(L + L_R2 + (qq * 4 + 0) * 16) + *(const LAS f4*)(L + L_R2 + (qq * 4 + 1) * 16)) +
                 (*(const LAS f4*)(L + L_R2 + (qq * 4 + 2) * 16) + *(const LAS f4*)(L + L_R2 + (qq * 4 + 3) * 16));
    const f4 xr = *(const LAS f4*)(L + L_XL + (8 * j + 4 * qq) * 4);
    res = xr + a + f4{b2, b2, b2, b2};
  }
  return res;
}

__global__ void __launch_bounds__(512, 2) fused_decode_kernel(Params p) {
  __shared__ __attribute__((aligned(1024))) unsigned char smem[L_END];
  LAS unsigned char* L = (LAS unsigned char*)smem;
  const int tid = threadIdx.x, lane = tid & 63, w = __builtin_amdgcn_readfirstlane(tid >> 6);
  const int bid = blockIdx.x;
  v4u dummy = {0u, 0u, 0u, 0u};
  {
    const int piece = (bid >> 3) * 8 + w;
    if (piece < CODE_KB) {
      u64 pc; asm volatile("s_getpc_b64 %0" : "=s"(pc));
      const u64 a = (pc & ~(u64)255) + (u64)(piece * 1024 + lane * 16);
      asm volatile("global_load_dwordx4 %0, %1, off" : "+v"(dummy) : "v"(a) : "memory");
    }
  }
  const int kvh = bid & 7, b = bid >> 6, s = (bid >> 3) & 7;
  const int j = kvh * 8 + s;
  const int g = lane >> 4, c = lane & 15;

  const size_t slab = ((size_t)(b * 8 + kvh) * SK + (SK - WIN) + s * 512) * 256;
  const __amdgpu_buffer_rsrc_t rK = __builtin_amdgcn_make_buffer_rsrc((void*)(p.K + slab), 0, 512 * 256, 0x00020000);
  const __amdgpu_buffer_rsrc_t rV = __builtin_amdgcn_make_buffer_rsrc((void*)(p.V + slab), 0, 512 * 256, 0x00020000);
  const __amdgpu_buffer_rsrc_t rQ = __builtin_amdgcn_make_buffer_rsrc((void*)p.wq, 0, 512 * 4096 * 2, 0x00020000);
  const __amdgpu_buffer_rsrc_t rO = __builtin_amdgcn_make_buffer_rsrc((void*)p.wo, 0, 512 * 4096 * 2, 0x00020000);
  const __amdgpu_buffer_rsrc_t rs = __builtin_amdgcn_make_buffer_rsrc((void*)p.ws, 0, WS_END, 0x00020000);
  const __amdgpu_buffer_rsrc_t rX = __builtin_amdgcn_make_buffer_rsrc((void*)p.x, 0, 4096, 0x00020000);
  const u32 ldsb = (u32)(size_t)L;

  u32 ev, xraw, b2raw, dummy2 = 0;
  asm volatile("s_nop 4\n\tbuffer_load_dword %0, %1, %2, 0 offen sc1" : "=v"(ev) : "v"(WS_EP + b * 256), "s"(rs) : "memory");
  asm volatile("buffer_load_ushort %0, %1, %2, 0 offen" : "=v"(xraw) : "v"((b * 512 + tid) * 2), "s"(rX) : "memory");
  asm volatile("global_load_ushort %0, %1, %2" : "=v"(b2raw) : "v"(0), "s"(p.b2) : "memory");
  const int rot = __builtin_amdgcn_readfirstlane((int)(((u32)(bid * 8 + w) * 2654435761u) >> 28));
  const int cg = s >> 1, kh = s & 1;
  const int voq = ((256 * kh + 32 * w + 8 * g) * 4096 + kvh * 512 + 128 * cg + 8 * c) * 2;
  const int so_ = (s + OROT) & 7;
  const int voo = ((kvh * 512 + (2 * (w >> 2) + (c >> 3)) * 128 + 32 * (w & 3) + 4 * g) * 512 + 64 * so_ + 8 * (c & 7)) * 2;
  int vok[4], vov[2];
#pragma unroll
  for (int i = 0; i < 4; ++i) vok[i] = (64 * w + g) * 256 + ((c ^ (4 * i + g)) * 16);
#pragma unroll
  for (int i = 0; i < 2; ++i) vov[i] = (64 * w + g) * 256 + ((((c >> 1) ^ (g | (i << 2))) * 32) + (c & 1) * 16);
  v4u wqr[8], wor[8], kf[16];
#define LDQ(jj) asm volatile("buffer_load_dwordx4 %0, %1, %2, %3 offen" WPOL : "=v"(wqr[jj]) : "v"(voq), "s"(rQ), "s"((jj) * 8192) : "memory")
#define LDO(jj) asm volatile("buffer_load_dwordx4 %0, %1, %2, %3 offen" WPOL : "=v"(wor[jj]) : "v"(voo), "s"(rO), "s"((16 * ((jj) >> 2) + ((jj) & 3)) * 1024) : "memory")
#define LDK(i) asm volatile("s_mov_b32 m0, %0\n\ts_nop 0\n\tbuffer_load_dwordx4 %1, %2, %3 offen" KVPOL " lds" ::"s"(ldsb + L_KV + w * 16384 + (i) * 1024), "v"(vok[(i) & 3]), "s"(rK), "s"((((i) + rot) & 15) * 1024) : "memory")
#define LDV(i) asm volatile("s_mov_b32 m0, %0\n\ts_nop 0\n\tbuffer_load_dwordx4 %1, %2, %3 offen" KVPOL " lds" ::"s"(ldsb + L_KV + w * 16384 + (i) * 1024), "v"(vov[((i) >> 1) & 1]), "s"(rV), "s"((((i) + rot) & 15) * 1024) : "memory")
#define GW(d) asm volatile("s_waitcnt vmcnt(%0)" ::"n"((d) - 1) : "memory")
#define G() GW(GWIN)
#define GWIN WD
#define EV(n, i) asm volatile("s_waitcnt vmcnt(%0)" ::"n"((n) - (i) - 1) : "memory")
#define KFRAG(t)                                                                                                     \
  _Pragma("unroll") for (int st = 0; st < 4; ++st)                                                                   \
      kf[(t) * 4 + st] = *(const LAS v4u*)(L + L_KV + w * 16384 + (16 * (t) + c) * 256 + (((4 * st + g) ^ c) * 16)); \
  asm volatile("" : "+v"(kf[(t) * 4]), "+v"(kf[(t) * 4 + 1]), "+v"(kf[(t) * 4 + 2]), "+v"(kf[(t) * 4 + 3])::"memory")
  LDQ(0); LDK(0); LDQ(1); LDK(1); LDQ(2); LDK(2);
  if (w == 7) {
    const int me = (b * 8 + kvh) * 8 + s;
    int a_ = lane < 16 ? WS_OB + me * 2048 + lane * 128
           : lane < 32 ? WS_OB + (1 << 19) + me * 2048 + (lane - 16) * 128
           : lane < 34 ? WS_OS + (lane - 32) * 32768 + me * 128
           : lane < 42 ? WS_QB + ((lane - 34) >> 2) * (4 * 8 * 4096) + ((b * 8 + kvh) * 2 + (s & 1)) * 2048 + (s >> 1) * 512 + ((lane - 34) & 3) * 128
           : lane < 46 ? WS_PB + ((lane - 42) >> 1) * 65536 + (b * 8 + kvh) * 2048 + ((s + OROT) & 7) * 256 + ((lane - 42) & 1) * 128
           : lane < 50 ? WS_CS + ((b * 8 + kvh) * 2 + (s & 1)) * 2048 + (s >> 1) * 512 + (lane - 46) * 128
           : WS_XB + (b * 64 + j) * XBS;
    asm volatile("buffer_load_dword %0, %1, %2, 0 offen" : "+v"(dummy2) : "v"(a_), "s"(rs) : "memory");
  }
  G(); LDQ(3); G(); LDK(3);
  asm volatile("s_waitcnt vmcnt(8)" : "+v"(ev), "+v"(xraw), "+v"(b2raw)::"memory");
  const u32 T0 = __builtin_amdgcn_readfirstlane(ev) + 1u;
  const u32 tag = T0 & TMASK;
  float xres = asf(xraw << 16);
  const int xqa = L_XL + (256 * kh + 32 * w + 8 * g) * 4;
  v4u qa;
  {
    *(LAS float*)(L + L_XL + tid * 4) = xres;
    float mean, var;
    ln_stats(xres, L, w, lane, mean, var);
    qa = q_operand(L, xqa, mean, var);
    PIN1(qa);
  }
  G(); LDQ(4); G(); LDK(4); G(); LDQ(5); G(); LDK(5);
  G(); LDQ(6); G(); LDK(6); G(); LDQ(7); G(); LDK(7);
  EV(16, 7);  KFRAG(0);
  G(); LDK(8); G(); LDK(9); G(); LDK(10); G(); LDK(11); G(); LDK(12);
  EV(21, 14);
  asm volatile("" : "+v"(wqr[0]), "+v"(wqr[1]), "+v"(wqr[2]), "+v"(wqr[3]), "+v"(wqr[4]), "+v"(wqr[5]), "+v"(wqr[6]), "+v"(wqr[7]), "+v"(dummy), "+v"(dummy2)::"memory");
  LDK(13); LDK(14); LDK(15);
  {
#pragma unroll
    for (int k = 0; k < 2; ++k) {
      const int piece = k == 0 ? w : 8 + (w % 3);
      int gr = piece * 64 + lane; gr = gr < 672 ? gr : 671;
      const int arr = gr / 224, e = gr - arr * 224;
      const unsigned char* src = (const unsigned char*)(arr == 0 ? p.w1 : arr == 1 ? p.b1 : p.w2) + e * 16;
      asm volatile("s_mov_b32 m0, %0\n\ts_nop 0\n\tglobal_load_lds_dwordx4 %1, off" ::"s"(ldsb + L_PAR + piece * 1024), "v"(src) : "memory");
    }
  }
  LDV(0); LDV(1);
  v4u wqf[8];
  transpose8(wqr, wqf);
  const int qwr = L_RED + w * 576 + (8 * c + 4 * (g & 1)) * 4, qrd = L_RED + (tid & 3) * 576 + (tid >> 2) * 16;
  const int qpub = WS_QB + ((b * 8 + kvh) * 2 + kh) * 2048 + cg * 512 + tid * 4;
  q_gemv(wqf, qa, L, tid, lane, qwr, qrd, rs, qpub, tag);
#undef GWIN
#define GWIN WD2
  EV(28, 15); KFRAG(1);
  G(); LDV(2); G(); LDV(3);
  EV(30, 19); KFRAG(2);
#define WAITN(n) asm volatile("s_waitcnt vmcnt(%0)" ::"n"(n) : "memory")
  G(); LDO(0); G(); LDV(4); G(); LDV(5);
  u32 q0, q1;
  const int qo = WS_QB + (b * 8 + kvh) * 4096 + tid * 4;
  G(); asm volatile("buffer_load_dword %0, %1, %2, 0 offen sc1" : "=v"(q0) : "v"(qo), "s"(rs) : "memory");
  asm volatile("buffer_load_dword %0, %1, %2, 0 offen offset:2048 sc1" : "=v"(q1) : "v"(qo), "s"(rs) : "memory");
  WAITN(11); KFRAG(3);
  G(); LDO(1); G(); LDV(6); G(); LDV(7); G(); LDO(2); G(); LDV(8); G(); LDV(9);
  WAITN(6);
  asm volatile("" : "+v"(q0), "+v"(q1)::"memory");
  LDO(3); LDV(10); LDV(11); LDO(4); LDV(12); LDV(13);
  int budget = 100000;
  if (__builtin_expect(!__all((((q0 ^ tag) | (q1 ^ tag)) & TMASK) == 0), 0)) {
    _Pragma("nounroll") do {
      asm volatile("buffer_load_dword %0, %2, %3, 0 offen sc1\n\tbuffer_load_dword %1, %2, %3, 0 offen offset:2048 sc1\n\ts_waitcnt vmcnt(0)" : "=&v"(q0), "=&v"(q1) : "v"(qo), "s"(rs) : "memory");
    } while (!__all((((q0 ^ tag) | (q1 ^ tag)) & TMASK) == 0) && --budget > 0);
  }
  const int vbase = L_KV + (g >> 1) * 16384 + (8 * (g & 1) + (c >> 2)) * 256 + ((w ^ ((c >> 2) | ((g & 1) << 2))) * 32) + (c & 3) * 8;
  const int a_qwr = L_QL + (tid >> 7) * QSTR + (tid & 127) * 2, a_qrd = L_QL + (c & 3) * QSTR + g * 16;
  const int a_mwr = L_MB + ((lane & 3) * 8 + w) * 4, a_mrd = L_MB + (c & 3) * 32;
  const int a_pwr = L_PL + (c >> 2) * PSTR + ((2 * (w & 1) + (g >> 1)) * 16 + (w >> 1) * 4 + (c & 3)) * 16 + (g & 1) * 8;
  const int a_lwr = L_LB + (w * 16 + g * 4 + (c & 3)) * 4;
  f4 acc[4];
  float M = attn_scores(asf(q0 ^ tag) + asf(q1 ^ tag), kf, L, a_qwr, a_qrd, a_mwr, a_mrd, a_pwr, a_lwr, lane);
#undef GWIN
#define GWIN WD3
  G(); LDO(5); G(); LDO(6); G(); LDO(7); G(); LDV(14); G(); LDV(15);
  WAITN(8);
  BAR();
  const int pub_o = ((b * 8 + kvh) * 8 + s) * 2048 + w * 256 + lane * 4, pub_s = ((b * 8 + kvh) * 8 + s) * 128 + (lane & 7) * 16;
  attn_publish_stats(M, L, w, lane, rs, WS_OS + pub_s, tag, T0);
#pragma unroll
  for (int u = 0; u < 4; ++u) acc[u] = f4{0.f, 0.f, 0.f, 0.f};
  pv_chunk(0, acc, L, vbase, lane);
  pv_chunk(1, acc, L, vbase, lane);
  pv_chunk(2, acc, L, vbase, lane);
  v4u pf3 = *(const LAS v4u*)(L + L_PL + 3 * PSTR + lane * 16);
  PIN1(pf3);
  SB();
  WAITN(0);
  BAR();
  {
    v4u vf3[4];
#pragma unroll
    for (int u = 0; u < 4; ++u) vf3[u] = v_frag(3, u, L, vbase);
    SB();
#pragma unroll
    for (int u = 0; u < 4; ++u) acc[u] = mfma(pf3, vf3[u], acc[u]);
  }
  int po1 = WS_OB + pub_o;
  attn_publish(acc, lane, rs, po1, T0);
  v4u wof[8];
  const float b2 = asf(b2raw << 16);
  int g_oo = (b * 8 + kvh) * 8 * 2048 + (lane >> 3) * 2048 + 2 * (w & 3) * 256 + (w >> 2) * 128 + (lane & 7) * 16;
  int g_so = (b * 8 + kvh) * 8 * 128 + (lane & 7) * 128 + (2 * (w >> 2) + ((lane >> 3) & 1)) * 16;
  int o_xw = L_SC2 + w * 1024 + (lane ^ (lane >> 4)) * 16, o_xr = L_SC2 + w * 1024 + (((c & 7) * 8 + 4 * (c >> 3) + g) ^ ((c & 7) >> 1)) * 16;
  int o_wr = L_RED + (2 * w + (g >> 1)) * 320 + (8 * (c & 7) + 4 * (g & 1)) * 4, o_rd = L_RED + (tid & 3) * 320 + (tid >> 2) * 16;
  int o_pub = WS_PB + (b * 8 + kvh) * 2048 + so_ * 256 + tid * 4;
  g_oo += WS_OB; g_so += WS_OS;
  PIN4(g_oo, g_so, o_wr, o_rd); PIN1(o_pub); PIN2(o_xw, o_xr);
  BAR();

  if (DLY_B) __builtin_amdgcn_s_sleep(DLY_B);
  o_gemv<true>([&]() { q_gemv1(wqf, v4u{0x3f803f80u, 0x3f803f80u, 0x3f803f80u, 0x3f803f80u}, L, lane, L_PL + w * 544 + (8 * c + 4 * (g & 1)) * 4); },
               [&]() { if (w == 2 || w == 3) q_gemv2<544>(L, lane, L_PL + (lane & 3) * 544 + ((w - 2) * 16 + (lane >> 2)) * 16, rs, qpub - WS_QB + WS_CS - 128 * 4, tag); }, wof, wor, L, tid, lane, rs, g_oo, g_so, o_xw, o_xr, o_wr, o_rd, o_pub, tag, T0, budget);
  u32 cs0, cs1;
  const int cso = WS_CS + (b * 8 + kvh) * 4096 + tid * 4;
  float mprev, csum;
  {
    const f4 r = mixer<DLY_CD>([&]() { cs0 = ld32s(rs, cso); cs1 = ld32s(rs, cso + 2048); }, xres, L, tid, w, lane, j, rs, WS_PB + b * 8 * 2048, tag, b2, budget, mprev);
    cs0 ^= tag; cs1 ^= tag;
    if (__builtin_expect(!__all(((cs0 | cs1) & TMASK) == 0), 0)) {
      do { CBAR(); cs0 = ld32s(rs, cso) ^ tag; cs1 = ld32s(rs, cso + 2048) ^ tag; } while (!__all(((cs0 | cs1) & TMASK) == 0) && --budget > 0);
    }
    csum = asf(cs0) + asf(cs1);
    mprev += b2;
    PIN2(csum, mprev);
    if (tid < XBS / 16) { v4u o = {tagf(r[0], tag), tagf(r[1], tag), tagf(r[2], tag), tagf(r[3], tag)}; st128s(o, rs, WS_XB + (b * 64 + j) * XBS + tid * 16); }
  }
  float rstd2, dmean;
  int qo2 = qo + 4 * 8 * 4096, b0 = a_qwr, b1 = a_qrd, b2_ = a_mwr, b3 = a_mrd, b4 = a_pwr, b5 = a_lwr, pr = L_PL + lane * 16, po2 = WS_OB + (1 << 19) + pub_o, ps2 = WS_OS + 32768 + pub_s;
  PIN4(qo2, b0, b1, b2_); PIN4(b3, b4, b5, pr); PIN2(po2, ps2);
  {
    int xo_ = WS_XB + (b * 64 + (tid >> 3)) * XBS + (tid & 7) * 4, ao = WS_XB + (b * 64 + 32 * kh + 4 * w + g) * XBS + (c & 1) * 16, qw = qwr, qr = qrd, qp = qpub + 4 * 8 * 4096;
    PIN4(xo_, ao, qw, qr); PIN1(qp);
    if (DLY_E) __builtin_amdgcn_s_sleep(DLY_E);
    v4u a0 = {tag, tag, tag, tag};
    u32 xv;
#define EBAD() !__all((((a0[0] | a0[1] | a0[2]) | (a0[3] | xv)) & TMASK) == 0)
    CBAR();
    if (c < 2) a0 = ld128s(rs, ao);
    xv = ld32s(rs, xo_) ^ tag;
#pragma unroll
    for (int i = 0; i < 4; ++i) a0[i] ^= tag;
    if (__builtin_expect(EBAD(), 0)) {
      do {
        CBAR();
        a0 = v4u{tag, tag, tag, tag};
        if (c < 2) a0 = ld128s(rs, ao);
        xv = ld32s(rs, xo_) ^ tag;
#pragma unroll
        for (int i = 0; i < 4; ++i) a0[i] ^= tag;
      } while (EBAD() && --budget > 0);
    }
    const u32 p0 = cvtpk(asf(a0[0]) - mprev, asf(a0[1]) - mprev), p1 = cvtpk(asf(a0[2]) - mprev, asf(a0[3]) - mprev);
    const v4u qa2 = {rowbc<0>(p0), rowbc<0>(p1), rowbc<1>(p0), rowbc<1>(p1)};
    q_gemv(wqf, qa2, L, tid, lane, qw, qr, rs, qp, tag);
    if (DLY_A) __builtin_amdgcn_s_sleep(DLY_A);
    CBAR();
    q0 = ld32s(rs, qo2); q1 = ld32s(rs, qo2 + 2048);
    CBAR();
    xres = asf(xv);
    float mean, var;
    ln_stats(xres, L, w, lane, mean, var);
    rstd2 = __builtin_amdgcn_rsqf(var + EPS);
    dmean = (mean - mprev) * csum;
    PIN2(rstd2, dmean);
  }
  {
    v4u vf[16];
#pragma unroll
    for (int t = 0; t < 4; ++t)
#pragma unroll
      for (int u = 0; u < 4; ++u) vf[t * 4 + u] = v_frag(t, u, L, vbase);
    PIN8(vf); { v4u* v2 = vf + 8; PIN8(v2); }
    q0 ^= tag; q1 ^= tag;
    if (__builtin_expect(!__all(((q0 | q1) & TMASK) == 0), 0)) {
      do { CBAR(); q0 = ld32s(rs, qo2) ^ tag; q1 = ld32s(rs, qo2 + 2048) ^ tag; } while (!__all(((q0 | q1) & TMASK) == 0) && --budget > 0);
    }
    M = attn_scores(rstd2 * ((asf(q0) + asf(q1)) - dmean), kf, L, b0, b1, b2_, b3, b4, b5, lane);
    BAR();
    v4u pf[4];
#pragma unroll
    for (int t = 0; t < 4; ++t) pf[t] = *(const LAS v4u*)(L + pr + t * PSTR);
    SB();
#pragma unroll
    for (int t = 0; t < 4; ++t)
#pragma unroll
      for (int u = 0; u < 4; ++u) acc[u] = mfma(pf[t], vf[t * 4 + u], t == 0 ? f4{0.f, 0.f, 0.f, 0.f} : acc[u]);
    attn_publish_stats(M, L, w, lane, rs, ps2, tag, T0);
    attn_publish(acc, lane, rs, po2, T0);
  }
  g_oo += 1 << 19; g_so += 32768; o_pub += 65536;
  PIN2(g_oo, g_so); PIN1(o_pub);
  if (DLY_B2) __builtin_amdgcn_s_sleep(DLY_B2);
  o_gemv<false>([]() {}, []() {}, wof, wor, L, tid, lane, rs, g_oo, g_so, o_xw, o_xr, o_wr, o_rd, o_pub, tag, T0, budget);
  {
    const f4 r = mixer<DLY_CD2>([]() {}, xres, L, tid, w, lane, j, rs, WS_PB + 65536 + b * 8 * 2048, tag, b2, budget, mprev);
    if (w == 0) {
      const u32 lo = cvtpk(r[0], r[1]), hi = cvtpk(r[2], r[3]);
      if (lane < 2) { v2u o = {lo, hi}; *(GAS v2u*)((GAS unsigned char*)p.out + (b * 512 + 8 * j + 4 * lane) * 2) = o; }
      if (lane == 0 && j == 0) __builtin_amdgcn_raw_buffer_store_b32(T0, rs, WS_EP + b * 256, 0, 16);
    }
  }
}

extern "C" void kernel_launch(void* const* d_in, const int* in_sizes, int n_in, void* d_out, int out_size, void* d_ws, size_t ws_size, hipStream_t stream) {
  Params p;
  p.x = (const u16*)d_in[0]; p.K = (const unsigned char*)d_in[1]; p.V = (const unsigned char*)d_in[2];
  p.wq = (const unsigned char*)d_in[3]; p.wo = (const unsigned char*)d_in[4];
  p.w1 = (const u16*)d_in[6]; p.b1 = (const u16*)d_in[7]; p.w2 = (const u16*)d_in[8]; p.b2 = (const u16*)d_in[9];
  p.out = (u16*)d_out; p.ws = (unsigned char*)d_ws;
  hipLaunchKernelGGL(fused_decode_kernel, dim3(256), dim3(512), 0, stream, p);
}
```

```cpp
#include <hip/hip_runtime.h>

typedef unsigned int u32;
typedef unsigned short u16;
typedef unsigned long long u64;
typedef u32 v4u __attribute__((ext_vector_type(4)));
typedef u32 v2u __attribute__((ext_vector_type(2)));
typedef float f4 __attribute__((ext_vector_type(4)));
typedef float f2 __attribute__((ext_vector_type(2)));
typedef short s8v __attribute__((ext_vector_type(8)));
typedef short s4v __attribute__((ext_vector_type(4)));
typedef __bf16 bf2v __attribute__((ext_vector_type(2)));
#define LAS __attribute__((address_space(3)))
#define GAS __attribute__((address_space(1)))
#define DI __device__ __forceinline__

#define DLY_B 10
#define DLY_CD 8
#define DLY_E 16
#define DLY_A 6
#define KVPOL " nt"
#define WPOL " nt"
#define PRETOUCH 1
#define OROT 4
#define XBS 128
#define DLY_B2 13
#define DLY_CD2 7
#define WD2 5
#define WD3 5
#define WD 5
#define CODE_KB 18

constexpr int SK = 16384, WIN = 4096;
constexpr float EPS = 1e-5f;
constexpr float SCL = 0.08838834764831845f * 1.4426950408889634f;
constexpr float GK0 = -2.0f * 0.7978845608028654f * 1.4426950408889634f;
constexpr float GK1 = GK0 * 0.044715f;

constexpr int WS_EP = 0;
constexpr int WS_QB = 4096;
constexpr int WS_CS = WS_QB + 2 * 4 * 8 * 2 * 2048;
constexpr int WS_OB = WS_CS + 4 * 8 * 2 * 2048;
constexpr int WS_OS = WS_OB + 2 * 4 * 8 * 8 * 2048;
constexpr int WS_PB = WS_OS + 2 * 4 * 8 * 8 * 128;
constexpr int WS_XB = WS_PB + 2 * 4 * 8 * 2048;
constexpr int WS_END = WS_XB + 4 * 64 * XBS;

constexpr int L_KV = 0;
constexpr int L_PAR = 131072;
constexpr int PSTR = 1088;
constexpr int L_PL = L_PAR + 11264;
constexpr int QSTR = 288;
constexpr int L_RED = L_PL + 4 * PSTR;
constexpr int L_ST = L_RED + 5120;
constexpr int L_MB = L_ST + 64;
constexpr int L_LB = L_MB + 128;
constexpr int L_R2 = L_LB + 512;
constexpr int L_QL = L_R2 + 128;
constexpr int L_XL = L_QL + 4 * QSTR;
constexpr int L_SC2 = L_QL;
constexpr int L_END = L_SC2 + 8192;
static_assert(L_END <= 163840, "LDS");

struct Params {
  const u16* x; const unsigned char* K; const unsigned char* V; const unsigned char* wq; const unsigned char* wo;
  const u16* w1; const u16* b1; const u16* w2; const u16* b2; u16* out; unsigned char* ws;
};

template <int CTRL> DI float dpp(float x) {
  return __builtin_bit_cast(float, __builtin_amdgcn_update_dpp(0, __builtin_bit_cast(int, x), CTRL, 0xf, 0xf, true));
}
constexpr int XOR1 = 0xB1, XOR2 = 0x4E, HMIR = 0x141, MIR = 0x140, ROR4 = 0x124, ROR8 = 0x128;
DI float asf(u32 x) { return __builtin_bit_cast(float, x); }
DI u32 asu(float x) { return __builtin_bit_cast(u32, x); }
DI u32 cvtpk(float lo, float hi) { f2 f = {lo, hi}; return __builtin_bit_cast(u32, __builtin_convertvector(f, bf2v)); }
DI f4 mfma(v4u a, v4u b, f4 c) {
  return __builtin_amdgcn_mfma_f32_16x16x32_bf16(__builtin_bit_cast(s8v, a), __builtin_bit_cast(s8v, b), c, 0, 0, 0);
}
DI float fmx(float a, float b) { return __builtin_elementwise_maximum(a, b); }
DI float xrow_max(float x) {
  auto s = __builtin_amdgcn_permlane16_swap(asu(x), asu(x), false, false);
  x = fmx(asf(s[0]), asf(s[1]));
  auto t = __builtin_amdgcn_permlane32_swap(asu(x), asu(x), false, false);
  return fmx(asf(t[0]), asf(t[1]));
}
DI float quad_reduce4(f4 r, int lane) {
  float t0 = (lane & 1) ? r[1] : r[0], t1 = (lane & 1) ? r[0] : r[1];
  t0 += dpp<XOR1>(t1);
  float t2 = (lane & 1) ? r[3] : r[2], t3 = (lane & 1) ? r[2] : r[3];
  t2 += dpp<XOR1>(t3);
  float s0 = (lane & 2) ? t2 : t0; const float s1 = (lane & 2) ? t0 : t2;
  s0 += dpp<XOR2>(s1);
  return s0;
}
DI void st32s(u32 v, __amdgpu_buffer_rsrc_t rs, int off) { __builtin_amdgcn_raw_buffer_store_b32(v, rs, off, 0, 16); }
template <int N> DI u32 rowbc(u32 x) { return (u32)__builtin_amdgcn_update_dpp(0, (int)x, 0x150 + N, 0xf, 0xf, false); }
DI u32 xo(u32 a, u32 t, u32 acc) { return __builtin_amdgcn_bitop3_b32(a, t, acc, 0xBE); }
#define PIN1(a) asm volatile("" : "+v"(a))
#define PIN2(a, b) asm volatile("" : "+v"(a), "+v"(b))
#define PIN4(a, b, c, d) asm volatile("" : "+v"(a), "+v"(b), "+v"(c), "+v"(d))
#define PIN8(x) asm volatile("" : "+v"(x[0]), "+v"(x[1]), "+v"(x[2]), "+v"(x[3]), "+v"(x[4]), "+v"(x[5]), "+v"(x[6]), "+v"(x[7]))
DI float xrow_sum(float x) {
  auto s = __builtin_amdgcn_permlane16_swap(asu(x), asu(x), false, false);
  x = asf(s[0]) + asf(s[1]);
  auto t = __builtin_amdgcn_permlane32_swap(asu(x), asu(x), false, false);
  return asf(t[0]) + asf(t[1]);
}
#define BAR() asm volatile("s_waitcnt lgkmcnt(0)\n\ts_barrier" ::: "memory")
#define CBAR() asm volatile("" ::: "memory")
#define SB() __builtin_amdgcn_sched_barrier(0)

constexpr u32 TMASK = 0x3ffu;
DI u32 tagf(float v, u32 tag) { return __builtin_amdgcn_bitop3_b32(asu(v) + 0x200u, TMASK, tag, 0xBA); }
DI float untag(u32 v) { return asf(v & ~TMASK); }

DI u32 ld32s(__amdgpu_buffer_rsrc_t rs, int off) { return __builtin_amdgcn_raw_buffer_load_b32(rs, off, 0, 16); }
DI v4u ld128s(__amdgpu_buffer_rsrc_t rs, int off) { return __builtin_amdgcn_raw_buffer_load_b128(rs, off, 0, 16); }
DI void st128s(v4u v, __amdgpu_buffer_rsrc_t rs, int off) { __builtin_amdgcn_raw_buffer_store_b128(v, rs, off, 0, 16); }

DI void ln_stats(float x, LAS unsigned char* L, int w, int lane, float& mean, float& var) {
  const float c = asf(__builtin_amdgcn_readfirstlane(asu(x)));
  const float d = x - c;
  float s1 = d, s2 = d * d;
  s1 += dpp<XOR1>(s1); s2 += dpp<XOR1>(s2);
  s1 += dpp<XOR2>(s1); s2 += dpp<XOR2>(s2);
  s1 += dpp<HMIR>(s1); s2 += dpp<HMIR>(s2);
  s1 += dpp<MIR>(s1); s2 += dpp<MIR>(s2);
  asm volatile("s_nop 1\n\tv_add_f32_dpp %0, %0, %0 row_bcast:15 row_mask:0xa bank_mask:0xf\n\tv_add_f32_dpp %1, %1, %1 row_bcast:15 row_mask:0xa bank_mask:0xf\n\t"
               "s_nop 0\n\tv_add_f32_dpp %0, %0, %0 row_bcast:31 row_mask:0xc bank_mask:0xf\n\tv_add_f32_dpp %1, %1, %1 row_bcast:31 row_mask:0xc bank_mask:0xf"
               : "+v"(s1), "+v"(s2));
  const f2 pv = {fmaf(s1, 1.0f / 64, c), fmaf(s1 * s1, -1.0f / 64, s2)};
  if (lane == 63) *(LAS f2*)(L + L_ST + w * 8) = pv;
  BAR();
  const f2 v = *(const LAS f2*)(L + L_ST + (lane & 7) * 8);
  float m = v.x;
  m += dpp<XOR1>(m); m += dpp<XOR2>(m); m += dpp<HMIR>(m);
  mean = m * 0.125f;
  const float dv = v.x - mean;
  float t = fmaf(64.0f * dv, dv, v.y);
  t += dpp<XOR1>(t); t += dpp<XOR2>(t); t += dpp<HMIR>(t);
  var = t * (1.0f / 512);
}

DI void transpose8(const v4u (&raw)[8], v4u (&fr)[8]) {
#pragma unroll
  for (int i = 0; i < 8; ++i) {
    const u32 sel = (i & 1) ? 0x07060302u : 0x05040100u;
#pragma unroll
    for (int m = 0; m < 4; ++m) fr[i][m] = __builtin_amdgcn_perm(raw[2 * m + 1][i >> 1], raw[2 * m][i >> 1], sel);
  }
}

DI v4u q_operand(LAS unsigned char* L, int xaddr, float mean, float var) {
  const f4 a = *(const LAS f4*)(L + xaddr), b = *(const LAS f4*)(L + xaddr + 16);
  const float rs_ = __builtin_amdgcn_rsqf(var + EPS), mr = -mean * rs_;
  return v4u{cvtpk(fmaf(a[0], rs_, mr), fmaf(a[1], rs_, mr)), cvtpk(fmaf(a[2], rs_, mr), fmaf(a[3], rs_, mr)),
             cvtpk(fmaf(b[0], rs_, mr), fmaf(b[1], rs_, mr)), cvtpk(fmaf(b[2], rs_, mr), fmaf(b[3], rs_, mr))};
}

DI void q_gemv1(const v4u (&wf)[8], v4u a, LAS unsigned char* L, int lane, int wraddr) {
  f4 d[8];
#pragma unroll
  for (int i = 0; i < 8; ++i) d[i] = mfma(a, wf[i], f4{0.f, 0.f, 0.f, 0.f});
  f4 v;
#pragma unroll
  for (int m = 0; m < 4; ++m) v[m] = (lane & 16) ? d[4 + m][0] : d[m][0];
  if (lane < 32) *(LAS f4*)(L + wraddr) = v;
}
template <int STR>
DI void q_gemv2(LAS unsigned char* L, int lane, int rdaddr, __amdgpu_buffer_rsrc_t rs, int qoff, u32 tag) {
  const f4 r = *(const LAS f4*)(L + rdaddr) + *(const LAS f4*)(L + rdaddr + 4 * STR);
  st32s(tagf(quad_reduce4(r, lane), tag), rs, qoff);
}
DI void q_gemv(const v4u (&wf)[8], v4u a, LAS unsigned char* L, int tid, int lane, int wraddr, int rdaddr, __amdgpu_buffer_rsrc_t rs, int qoff, u32 tag) {
  q_gemv1(wf, a, L, lane, wraddr);
  BAR();
  if (tid < 128) q_gemv2<576>(L, lane, rdaddr, rs, qoff, tag);
}

DI float attn_scores(float qv, const v4u (&kf)[16], LAS unsigned char* L, int qwr, int qrd, int mwr, int mrd, int pwr, int lwr, int lane) {
  *(LAS u16*)(L + qwr) = (u16)(cvtpk(qv, qv) & 0xffffu);
  BAR();
  v4u qf[4];
#pragma unroll
  for (int st = 0; st < 4; ++st) qf[st] = *(const LAS v4u*)(L + qrd + st * 64);
  SB();
  f4 d[4];
#pragma unroll
  for (int st = 0; st < 4; ++st)
#pragma unroll
    for (int t = 0; t < 4; ++t) d[t] = mfma(kf[t * 4 + st], qf[st], st == 0 ? f4{0.f, 0.f, 0.f, 0.f} : d[t]);
  float sc[4];
#pragma unroll
  for (int i = 0; i < 4; ++i) {
    const float a = (lane & 4) ? d[1][i] : d[0][i], bq = (lane & 4) ? d[3][i] : d[2][i];
    sc[i] = (lane & 8) ? bq : a;
  }
  float m = fmx(fmx(sc[0], sc[1]), fmx(sc[2], sc[3]));
  m = fmx(m, dpp<ROR4>(m)); m = fmx(m, dpp<ROR8>(m));
  m = xrow_max(m);
  if (lane < 4) *(LAS float*)(L + mwr) = m;
  BAR();
  const f4 m0 = *(const LAS f4*)(L + mrd), m1 = *(const LAS f4*)(L + mrd + 16);
  float M = fmx(fmx(fmx(m0[0], m0[1]), fmx(m0[2], m0[3])), fmx(fmx(m1[0], m1[1]), fmx(m1[2], m1[3])));
  M = asf(asu(M * SCL) & ~TMASK);
  float p[4];
#pragma unroll
  for (int i = 0; i < 4; ++i) p[i] = __builtin_amdgcn_exp2f(fmaf(sc[i], SCL, -M));
  v2u pk = {cvtpk(p[0], p[1]), cvtpk(p[2], p[3])};
  *(LAS v2u*)(L + pwr) = pk;
  float l = (p[0] + p[1]) + (p[2] + p[3]);
  l += dpp<ROR4>(l); l += dpp<ROR8>(l);
  if ((lane & 12) == 0) *(LAS float*)(L + lwr) = l;
  return M;
}

DI v4u v_frag(int t, int u, LAS unsigned char* L, int vbase) {
  const s4v lo = __builtin_amdgcn_ds_read_tr16_b64_v4i16((LAS s4v*)(L + vbase + u * 32768 + t * 4096));
  const s4v hi = __builtin_amdgcn_ds_read_tr16_b64_v4i16((LAS s4v*)(L + vbase + u * 32768 + t * 4096 + 1024));
  const v2u l2 = __builtin_bit_cast(v2u, lo), h2 = __builtin_bit_cast(v2u, hi);
  return v4u{l2[0], l2[1], h2[0], h2[1]};
}
DI void pv_chunk(int t, f4 (&acc)[4], LAS unsigned char* L, int vbase, int lane) {
  const v4u pf = *(const LAS v4u*)(L + L_PL + t * PSTR + lane * 16);
#pragma unroll
  for (int u = 0; u < 4; ++u) acc[u] = mfma(pf, v_frag(t, u, L, vbase), acc[u]);
}

DI void attn_publish_stats(float M, LAS unsigned char* L, int w, int lane, __amdgpu_buffer_rsrc_t rs, int saddr, u32 tag, u32 T0) {
  if (w == 7) {
    float l = *(const LAS float*)(L + L_LB + lane * 4) + *(const LAS float*)(L + L_LB + (lane + 64) * 4);
    l += dpp<ROR4>(l); l += dpp<ROR8>(l);
    l = xrow_sum(l);
    if (lane < 8) { v4u sv = {asu(M) | tag, tagf(l, tag), T0, T0}; st128s(sv, rs, saddr); }
  }
}
template <int RM> DI float rowput(float old, float x) {
  return __builtin_bit_cast(float, __builtin_amdgcn_update_dpp(__builtin_bit_cast(int, old), __builtin_bit_cast(int, x), 0xE4, RM, 0xf, false));
}
DI void attn_publish(const f4 (&acc)[4], int lane, __amdgpu_buffer_rsrc_t rs, int oaddr, u32 T0) {
  float y[4];
#pragma unroll
  for (int h = 0; h < 4; ++h) y[h] = rowput<0x8>(rowput<0x4>(rowput<0x2>(acc[0][h], acc[1][h]), acc[2][h]), acc[3][h]);
  auto s01 = __builtin_amdgcn_permlane16_swap(asu(y[0]), asu(y[1]), false, false);
  auto s23 = __builtin_amdgcn_permlane16_swap(asu(y[2]), asu(y[3]), false, false);
  const float z01 = asf(s01[0]) + asf(s01[1]), z23 = asf(s23[0]) + asf(s23[1]);
  auto sf = __builtin_amdgcn_permlane32_swap(asu(z01), asu(z23), false, false);
  const float v = asf(sf[0]) + asf(sf[1]);
  st32s(__builtin_amdgcn_perm(cvtpk(v, v), T0, 0x05040100u), rs, oaddr);
}

DI bool o_check(const v4u& x0, const v4u& x1, const v4u& sv, u32 T0) {
  u32 d = xo(x0[0], T0, 0u);
  d = xo(x0[1], T0, d); d = xo(x0[2], T0, d); d = xo(x0[3], T0, d);
  d = xo(x1[0], T0, d); d = xo(x1[1], T0, d); d = xo(x1[2], T0, d); d = xo(x1[3], T0, d);
  d = xo(sv[0], T0, d); d = xo(sv[1], T0, d);
  d = xo(sv[3], T0, xo(sv[2], T0, d & TMASK));
  return __all(d == 0);
}
template <bool FIRST, class Hook, class Hook2>
DI void o_gemv(Hook&& hook, Hook2&& hook2, v4u (&wf)[8], v4u (&wraw)[8], LAS unsigned char* L, int tid, int lane, __amdgpu_buffer_rsrc_t rs, int oo, int so, int xw, int xr, int wraddr, int rdaddr, int poff, u32 tag, u32 T0, int& budget) {
  v4u x0, x1, sv = {T0, T0, T0, T0};
  CBAR();
  x0 = ld128s(rs, oo); x1 = ld128s(rs, oo + 256);
  if (lane < 16) sv = ld128s(rs, so);
  if (FIRST) {
    asm volatile("" : "+v"(wraw[0]), "+v"(wraw[1]), "+v"(wraw[2]), "+v"(wraw[3]), "+v"(wraw[4]), "+v"(wraw[5]), "+v"(wraw[6]), "+v"(wraw[7])::"memory");
    transpose8(wraw, wf);
    PIN8(wf);
    hook();
  }
  if (__builtin_expect(!o_check(x0, x1, sv, T0), 0)) {
    do {
      CBAR();
      x0 = ld128s(rs, oo); x1 = ld128s(rs, oo + 256);
      if (lane < 16) sv = ld128s(rs, so);
    } while (!o_check(x0, x1, sv, T0) && --budget > 0);
  }
  const v4u pk = {__builtin_amdgcn_perm(x0[1], x0[0], 0x07060302u), __builtin_amdgcn_perm(x0[3], x0[2], 0x07060302u),
                  __builtin_amdgcn_perm(x1[1], x1[0], 0x07060302u), __builtin_amdgcn_perm(x1[3], x1[2], 0x07060302u)};
  *(LAS v4u*)(L + xw) = pk;
  CBAR();
  const v4u a = *(const LAS v4u*)(L + xr);
  const float ms = untag(sv[0]), ls = untag(sv[1]);
  float M = ms;
  M = fmx(M, dpp<XOR2>(M)); M = fmx(M, dpp<ROR4>(M)); M = fmx(M, dpp<ROR8>(M));
  const float f = __builtin_amdgcn_exp2f(ms - M);
  float Ls = f * ls;
  Ls += dpp<XOR2>(Ls); Ls += dpp<ROR4>(Ls); Ls += dpp<ROR8>(Ls);
  const float cs = f * __builtin_amdgcn_rcpf(Ls);
  f4 d[8];
#pragma unroll
  for (int i = 0; i < 8; ++i) d[i] = mfma(a, wf[i], f4{0.f, 0.f, 0.f, 0.f});
  const int bp = (8 * ((lane >> 4) & 1) + (lane >> 5)) * 4;
  const float cr0 = asf(__builtin_amdgcn_ds_bpermute(bp, asu(cs))), cr1 = asf(__builtin_amdgcn_ds_bpermute(bp + 8, asu(cs)));
  const float cr2 = asf(__builtin_amdgcn_ds_bpermute(bp + 16, asu(cs))), cr3 = asf(__builtin_amdgcn_ds_bpermute(bp + 24, asu(cs)));
  const f2 c01 = {cr0, cr1}, c23 = {cr2, cr3};
  float e[8];
#pragma unroll
  for (int i = 0; i < 8; ++i) { const f2 t = f2{d[i][2], d[i][3]} * c23 + f2{d[i][0], d[i][1]} * c01; e[i] = t[0] + t[1]; }
  f4 v;
#pragma unroll
  for (int m = 0; m < 4; ++m) { auto t = __builtin_amdgcn_permlane16_swap(asu(e[m]), asu(e[m + 4]), false, false); v[m] = asf(t[0]) + asf(t[1]); }
  auto s02 = __builtin_amdgcn_permlane32_swap(asu(v[0]), asu(v[2]), false, false);
  auto s13 = __builtin_amdgcn_permlane32_swap(asu(v[1]), asu(v[3]), false, false);
  const f2 vv = {asf(s02[0]) + dpp<ROR8>(asf(s02[1])), asf(s13[0]) + dpp<ROR8>(asf(s13[1]))};
  if ((lane & 8) == 0) *(LAS f2*)(L + wraddr) = vv;
  BAR();
  if (tid < 64) {
    const f4 r = *(const LAS f4*)(L + rdaddr) + *(const LAS f4*)(L + rdaddr + 4 * 320);
    st32s(tagf(quad_reduce4(r, lane), tag), rs, poff);
  }
  hook2();
}

template <int DLY, class Hook>
DI f4 mixer(Hook&& hook, float& xres, LAS unsigned char* L, int tid, int w, int lane, int j, __amdgpu_buffer_rsrc_t rs, int pbase, u32 tag, float b2, int& budget, float& mean_out) {
  float pw1[7], pb1[7], pw2[7];
  const int fl = tid & 255, q = tid >> 8;
#pragma unroll
  for (int i = 0; i < 7; ++i) {
    pw1[i] = asf((u32) * (const LAS u16*)(L + L_PAR + (fl + 256 * i) * 2) << 16);
    pb1[i] = asf((u32) * (const LAS u16*)(L + L_PAR + 3584 + (fl + 256 * i) * 2) << 16);
    pw2[i] = asf((u32) * (const LAS u16*)(L + L_PAR + 7168 + (fl + 256 * i) * 2) << 16);
  }
#pragma unroll
  for (int i = 0; i < 7; ++i) asm volatile("" : "+v"(pw1[i]), "+v"(pb1[i]), "+v"(pw2[i]));
  int po = pbase + tid * 4, xa = L_XL + (8 * j + 4 * q) * 4, xw = L_XL + tid * 4;
  asm volatile("" : "+v"(po), "+v"(xa), "+v"(xw));
  if (DLY) __builtin_amdgcn_s_sleep(DLY);
  u32 p[8];
#define PBAD() !__all(((((p[0] | p[1] | p[2]) | (p[3] | p[4] | p[5])) | (p[6] | p[7])) & TMASK) == 0)
  CBAR();
#pragma unroll
  for (int i = 0; i < 8; ++i) p[i] = ld32s(rs, po + i * 2048) ^ tag;
  if (__builtin_expect(PBAD(), 0)) {
    do {
      CBAR();
#pragma unroll
      for (int i = 0; i < 8; ++i) p[i] = ld32s(rs, po + i * 2048) ^ tag;
    } while (PBAD() && --budget > 0);
  }
  hook();
  const float x1 = xres + (((asf(p[0]) + asf(p[1])) + (asf(p[2]) + asf(p[3]))) + ((asf(p[4]) + asf(p[5])) + (asf(p[6]) + asf(p[7]))));
  xres = x1;
  *(LAS float*)(L + xw) = x1;
  float mean, var;
  ln_stats(x1, L, w, lane, mean, var);
  mean_out = mean;
  const f4 xv = *(const LAS f4*)(L + xa);
  const float r1 = __builtin_amdgcn_rsqf(var + EPS);
  const float r2 = __builtin_amdgcn_rsqf(var * r1 * r1 + EPS);
  const float rr = r1 * r2, mr = -mean * rr;
  const f2 hA = {fmaf(xv[0], rr, mr), fmaf(xv[1], rr, mr)}, hB = {fmaf(xv[2], rr, mr), fmaf(xv[3], rr, mr)};
  f2 aA = {0.f, 0.f}, aB = {0.f, 0.f};
#pragma unroll
  for (int i = 0; i < 7; ++i) {
    const f2 w1 = {pw1[i], pw1[i]}, b1 = {pb1[i], pb1[i]}, w2 = {pw2[i], pw2[i]};
    const f2 k0 = {GK0, GK0}, k1 = {GK1, GK1}, one = {1.f, 1.f};
    {
      const f2 u = hA * w1 + b1, z = u * (u * u * k1 + k0);
      const f2 e = {__builtin_amdgcn_exp2f(z[0]), __builtin_amdgcn_exp2f(z[1])}, dd = e + one;
      const f2 rc = {__builtin_amdgcn_rcpf(dd[0]), __builtin_amdgcn_rcpf(dd[1])};
      aA += (u * rc) * w2;
    }
    {
      const f2 u = hB * w1 + b1, z = u * (u * u * k1 + k0);
      const f2 e = {__builtin_amdgcn_exp2f(z[0]), __builtin_amdgcn_exp2f(z[1])}, dd = e + one;
      const f2 rc = {__builtin_amdgcn_rcpf(dd[0]), __builtin_amdgcn_rcpf(dd[1])};
      aB += (u * rc) * w2;
    }
  }
  float t0 = (lane & 1) ? aA[1] : aA[0], t1 = (lane & 1) ? aA[0] : aA[1];
  t0 += dpp<XOR1>(t1);
  float t2 = (lane & 1) ? aB[1] : aB[0], t3 = (lane & 1) ? aB[0] : aB[1];
  t2 += dpp<XOR1>(t3);
  float s0 = (lane & 2) ? t2 : t0, s1 = (lane & 2) ? t0 : t2;
  s0 += dpp<XOR2>(s1);
  s0 += dpp<ROR4>(s0); s0 += dpp<ROR8>(s0);
  s0 = xrow_sum(s0);
  if (lane < 4) *(LAS float*)(L + L_R2 + (w * 4 + lane) * 4) = s0;
  BAR();
  f4 res = {0.f, 0.f, 0.f, 0.f};
  if (w == 0) {
    const int qq = lane & 1;
    const f4 a = (*(const LAS f4*)(L + L_R2 + (qq * 4 + 0) * 16) + *(const LAS f4*)(L + L_R2 + (qq * 4 + 1) * 16)) +
                 (*(const LAS f4*)(L + L_R2 + (qq * 4 + 2) * 16) + *(const LAS f4*)(L + L_R2 + (qq * 4 + 3) * 16));
    const f4 xr = *(const LAS f4*)(L + L_XL + (8 * j + 4 * qq) * 4);
    res = xr + a + f4{b2, b2, b2, b2};
  }
  return res;
}

__global__ void __launch_bounds__(512, 2) fused_decode_kernel(Params p) {
  __shared__ __attribute__((aligned(1024))) unsigned char smem[L_END];
  LAS unsigned char* L = (LAS unsigned char*)smem;
  const int tid = threadIdx.x, lane = tid & 63, w = __builtin_amdgcn_readfirstlane(tid >> 6);
  const int bid = blockIdx.x;
  v4u dummy = {0u, 0u, 0u, 0u};
  {
    const int piece = (bid >> 3) * 8 + w;
    if (piece < CODE_KB) {
      u64 pc; asm volatile("s_getpc_b64 %0" : "=s"(pc));
      const u64 a = (pc & ~(u64)255) + (u64)(piece * 1024 + lane * 16);
      asm volatile("global_load_dwordx4 %0, %1, off" : "+v"(dummy) : "v"(a) : "memory");
    }
  }
  const int kvh = bid & 7, b = bid >> 6, s = (bid >> 3) & 7;
  const int j = kvh * 8 + s;
  const int g = lane >> 4, c = lane & 15;

  const size_t slab = ((size_t)(b * 8 + kvh) * SK + (SK - WIN) + s * 512) * 256;
  const __amdgpu_buffer_rsrc_t rK = __builtin_amdgcn_make_buffer_rsrc((void*)(p.K + slab), 0, 512 * 256, 0x00020000);
  const __amdgpu_buffer_rsrc_t rV = __builtin_amdgcn_make_buffer_rsrc((void*)(p.V + slab), 0, 512 * 256, 0x00020000);
  const __amdgpu_buffer_rsrc_t rQ = __builtin_amdgcn_make_buffer_rsrc((void*)p.wq, 0, 512 * 4096 * 2, 0x00020000);
  const __amdgpu_buffer_rsrc_t rO = __builtin_amdgcn_make_buffer_rsrc((void*)p.wo, 0, 512 * 4096 * 2, 0x00020000);
  const __amdgpu_buffer_rsrc_t rs = __builtin_amdgcn_make_buffer_rsrc((void*)p.ws, 0, WS_END, 0x00020000);
  const __amdgpu_buffer_rsrc_t rX = __builtin_amdgcn_make_buffer_rsrc((void*)p.x, 0, 4096, 0x00020000);
  const u32 ldsb = (u32)(size_t)L;

  u32 ev, xraw, b2raw, dummy2 = 0;
  asm volatile("s_nop 4\n\tbuffer_load_dword %0, %1, %2, 0 offen sc1" : "=v"(ev) : "v"(WS_EP + b * 256), "s"(rs) : "memory");
  asm volatile("buffer_load_ushort %0, %1, %2, 0 offen" : "=v"(xraw) : "v"((b * 512 + tid) * 2), "s"(rX) : "memory");
  asm volatile("global_load_ushort %0, %1, %2" : "=v"(b2raw) : "v"(0), "s"(p.b2) : "memory");
  const int rot = __builtin_amdgcn_readfirstlane((int)(((u32)(bid * 8 + w) * 2654435761u) >> 28));
  const int cg = s >> 1, kh = s & 1;
  const int voq = ((256 * kh + 32 * w + 8 * g) * 4096 + kvh * 512 + 128 * cg + 8 * c) * 2;
  const int so_ = (s + OROT) & 7;
  const int voo = ((kvh * 512 + (2 * (w & 1) + (c >> 3)) * 128 + 32 * (w >> 1) + 4 * g) * 512 + 64 * so_ + 8 * (c & 7)) * 2;
  int vok[4], vov[2];
#pragma unroll
  for (int i = 0; i < 4; ++i) vok[i] = (64 * w + g) * 256 + ((c ^ (4 * i + g)) * 16);
#pragma unroll
  for (int i = 0; i < 2; ++i) vov[i] = (64 * w + g) * 256 + ((((c >> 1) ^ (g | (i << 2))) * 32) + (c & 1) * 16);
  v4u wqr[8], wor[8], kf[16];
#define LDQ(jj) asm volatile("buffer_load_dwordx4 %0, %1, %2, %3 offen" WPOL : "=v"(wqr[jj]) : "v"(voq), "s"(rQ), "s"((jj) * 8192) : "memory")
#define LDO(jj) asm volatile("buffer_load_dwordx4 %0, %1, %2, %3 offen" WPOL : "=v"(wor[jj]) : "v"(voo), "s"(rO), "s"((16 * ((jj) >> 2) + ((jj) & 3)) * 1024) : "memory")
#define LDK(i) asm volatile("s_mov_b32 m0, %0\n\ts_nop 0\n\tbuffer_load_dwordx4 %1, %2, %3 offen" KVPOL " lds" ::"s"(ldsb + L_KV + w * 16384 + (i) * 1024), "v"(vok[(i) & 3]), "s"(rK), "s"((((i) + rot) & 15) * 1024) : "memory")
#define LDV(i) asm volatile("s_mov_b32 m0, %0\n\ts_nop 0\n\tbuffer_load_dwordx4 %1, %2, %3 offen" KVPOL " lds" ::"s"(ldsb + L_KV + w * 16384 + (i) * 1024), "v"(vov[((i) >> 1) & 1]), "s"(rV), "s"((((i) + rot) & 15) * 1024) : "memory")
#define GW(d) asm volatile("s_waitcnt vmcnt(%0)" ::"n"((d) - 1) : "memory")
#define G() GW(GWIN)
#define GWIN WD
#define EV(n, i) asm volatile("s_waitcnt vmcnt(%0)" ::"n"((n) - (i) - 1) : "memory")
#define KFRAG(t)                                                                                                     \
  _Pragma("unroll") for (int st = 0; st < 4; ++st)                                                                   \
      kf[(t) * 4 + st] = *(const LAS v4u*)(L + L_KV + w * 16384 + (16 * (t) + c) * 256 + (((4 * st + g) ^ c) * 16)); \
  asm volatile("" : "+v"(kf[(t) * 4]), "+v"(kf[(t) * 4 + 1]), "+v"(kf[(t) * 4 + 2]), "+v"(kf[(t) * 4 + 3])::"memory")
  LDQ(0); LDK(0); LDQ(1); LDK(1); LDQ(2); LDK(2);
  if (w == 7) {
    const int me = (b * 8 + kvh) * 8 + s;
    int a_ = lane < 16 ? WS_OB + me * 2048 + lane * 128
           : lane < 32 ? WS_OB + (1 << 19) + me * 2048 + (lane - 16) * 128
           : lane < 34 ? WS_OS + (lane - 32) * 32768 + me * 128
           : lane < 42 ? WS_QB + ((lane - 34) >> 2) * (4 * 8 * 4096) + ((b * 8 + kvh) * 2 + (s & 1)) * 2048 + (s >> 1) * 512 + ((lane - 34) & 3) * 128
           : lane < 46 ? WS_PB + ((lane - 42) >> 1) * 65536 + (b * 8 + kvh) * 2048 + ((s + OROT) & 7) * 256 + ((lane - 42) & 1) * 128
           : lane < 50 ? WS_CS + ((b * 8 + kvh) * 2 + (s & 1)) * 2048 + (s >> 1) * 512 + (lane - 46) * 128
           : WS_XB + (b * 64 + j) * XBS;
    asm volatile("buffer_load_dword %0, %1, %2, 0 offen" : "+v"(dummy2) : "v"(a_), "s"(rs) : "memory");
  }
  G(); LDQ(3); G(); LDK(3);
  asm volatile("s_waitcnt vmcnt(8)" : "+v"(ev), "+v"(xraw), "+v"(b2raw)::"memory");
  const u32 T0 = __builtin_amdgcn_readfirstlane(ev) + 1u;
  const u32 tag = T0 & TMASK;
  float xres = asf(xraw << 16);
  const int xqa = L_XL + (256 * kh + 32 * w + 8 * g) * 4;
  v4u qa;
  {
    *(LAS float*)(L + L_XL + tid * 4) = xres;
    float mean, var;
    ln_stats(xres, L, w, lane, mean, var);
    qa = q_operand(L, xqa, mean, var);
    PIN1(qa);
  }
  G(); LDQ(4); G(); LDK(4); G(); LDQ(5); G(); LDK(5);
  G(); LDQ(6); G(); LDK(6); G(); LDQ(7); G(); LDK(7);
  EV(16, 7);  KFRAG(0);
  G(); LDK(8); G(); LDK(9); G(); LDK(10); G(); LDK(11); G(); LDK(12);
  EV(21, 14);
  asm volatile("" : "+v"(wqr[0]), "+v"(wqr[1]), "+v"(wqr[2]), "+v"(wqr[3]), "+v"(wqr[4]), "+v"(wqr[5]), "+v"(wqr[6]), "+v"(wqr[7]), "+v"(dummy), "+v"(dummy2)::"memory");
  LDK(13); LDK(14); LDK(15);
  {
#pragma unroll
    for (int k = 0; k < 2; ++k) {
      const int piece = k == 0 ? w : 8 + (w % 3);
      int gr = piece * 64 + lane; gr = gr < 672 ? gr : 671;
      const int arr = gr / 224, e = gr - arr * 224;
      const unsigned char* src = (const unsigned char*)(arr == 0 ? p.w1 : arr == 1 ? p.b1 : p.w2) + e * 16;
      asm volatile("s_mov_b32 m0, %0\n\ts_nop 0\n\tglobal_load_lds_dwordx4 %1, off" ::"s"(ldsb + L_PAR + piece * 1024), "v"(src) : "memory");
    }
  }
  LDV(0); LDV(1);
  v4u wqf[8];
  transpose8(wqr, wqf);
  const int qwr = L_RED + w * 576 + (8 * c + 4 * (g & 1)) * 4, qrd = L_RED + (tid & 3) * 576 + (tid >> 2) * 16;
  const int qpub = WS_QB + ((b * 8 + kvh) * 2 + kh) * 2048 + cg * 512 + tid * 4;
  q_gemv(wqf, qa, L, tid, lane, qwr, qrd, rs, qpub, tag);
#undef GWIN
#define GWIN WD2
  EV(28, 15); KFRAG(1);
  G(); LDV(2); G(); LDV(3);
  EV(30, 19); KFRAG(2);
#define WAITN(n) asm volatile("s_waitcnt vmcnt(%0)" ::"n"(n) : "memory")
  G(); LDO(0); G(); LDV(4); G(); LDV(5);
  u32 q0, q1;
  const int qo = WS_QB + (b * 8 + kvh) * 4096 + tid * 4;
  G(); asm volatile("buffer_load_dword %0, %1, %2, 0 offen sc1" : "=v"(q0) : "v"(qo), "s"(rs) : "memory");
  asm volatile("buffer_load_dword %0, %1, %2, 0 offen offset:2048 sc1" : "=v"(q1) : "v"(qo), "s"(rs) : "memory");
  WAITN(11); KFRAG(3);
  G(); LDO(1); G(); LDV(6); G(); LDV(7); G(); LDO(2); G(); LDV(8); G(); LDV(9);
  WAITN(6);
  asm volatile("" : "+v"(q0), "+v"(q1)::"memory");
  LDO(3); LDV(10); LDV(11); LDO(4); LDV(12); LDV(13);
  int budget = 100000;
  if (__builtin_expect(!__all((((q0 ^ tag) | (q1 ^ tag)) & TMASK) == 0), 0)) {
    _Pragma("nounroll") do {
      asm volatile("buffer_load_dword %0, %2, %3, 0 offen sc1\n\tbuffer_load_dword %1, %2, %3, 0 offen offset:2048 sc1\n\ts_waitcnt vmcnt(0)" : "=&v"(q0), "=&v"(q1) : "v"(qo), "s"(rs) : "memory");
    } while (!__all((((q0 ^ tag) | (q1 ^ tag)) & TMASK) == 0) && --budget > 0);
  }
  const int vbase = L_KV + (g >> 1) * 16384 + (8 * (g & 1) + (c >> 2)) * 256 + ((w ^ ((c >> 2) | ((g & 1) << 2))) * 32) + (c & 3) * 8;
  const int a_qwr = L_QL + (tid >> 7) * QSTR + (tid & 127) * 2, a_qrd = L_QL + (c & 3) * QSTR + g * 16;
  const int a_mwr = L_MB + ((lane & 3) * 8 + w) * 4, a_mrd = L_MB + (c & 3) * 32;
  const int a_pwr = L_PL + (c >> 2) * PSTR + ((2 * (w & 1) + (g >> 1)) * 16 + (w >> 1) * 4 + (c & 3)) * 16 + (g & 1) * 8;
  const int a_lwr = L_LB + (w * 16 + g * 4 + (c & 3)) * 4;
  f4 acc[4];
  float M = attn_scores(asf(q0 ^ tag) + asf(q1 ^ tag), kf, L, a_qwr, a_qrd, a_mwr, a_mrd, a_pwr, a_lwr, lane);
#undef GWIN
#define GWIN WD3
  G(); LDO(5); G(); LDO(6); G(); LDO(7); G(); LDV(14); G(); LDV(15);
  WAITN(8);
  BAR();
  const int pub_o = ((b * 8 + kvh) * 8 + s) * 2048 + w * 256 + lane * 4, pub_s = ((b * 8 + kvh) * 8 + s) * 128 + (lane & 7) * 16;
  attn_publish_stats(M, L, w, lane, rs, WS_OS + pub_s, tag, T0);
#pragma unroll
  for (int u = 0; u < 4; ++u) acc[u] = f4{0.f, 0.f, 0.f, 0.f};
  pv_chunk(0, acc, L, vbase, lane);
  pv_chunk(1, acc, L, vbase, lane);
  pv_chunk(2, acc, L, vbase, lane);
  v4u pf3 = *(const LAS v4u*)(L + L_PL + 3 * PSTR + lane * 16);
  PIN1(pf3);
  SB();
  WAITN(0);
  BAR();
  {
    v4u vf3[4];
#pragma unroll
    for (int u = 0; u < 4; ++u) vf3[u] = v_frag(3, u, L, vbase);
    SB();
#pragma unroll
    for (int u = 0; u < 4; ++u) acc[u] = mfma(pf3, vf3[u], acc[u]);
  }
  int po1 = WS_OB + pub_o;
  attn_publish(acc, lane, rs, po1, T0);
  v4u wof[8];
  const float b2 = asf(b2raw << 16);
  int g_oo = (b * 8 + kvh) * 8 * 2048 + (lane >> 3) * 2048 + 2 * (w >> 1) * 256 + (w & 1) * 128 + (lane & 7) * 16;
  int g_so = (b * 8 + kvh) * 8 * 128 + ((lane >> 1) & 7) * 128 + (2 * (w & 1) + (lane & 1)) * 16;
  int o_xw = L_SC2 + w * 1024 + (lane ^ (lane >> 4)) * 16, o_xr = L_SC2 + w * 1024 + (((c & 7) * 8 + 4 * (c >> 3) + g) ^ ((c & 7) >> 1)) * 16;
  int o_wr = L_RED + w * 320 + (8 * (c & 7) + 4 * (g & 1) + 2 * (g >> 1)) * 4, o_rd = L_RED + (tid & 3) * 320 + (tid >> 2) * 16;
  int o_pub = WS_PB + (b * 8 + kvh) * 2048 + so_ * 256 + tid * 4;
  g_oo += WS_OB; g_so += WS_OS;
  PIN4(g_oo, g_so, o_wr, o_rd); PIN1(o_pub); PIN2(o_xw, o_xr);
  BAR();

  if (DLY_B) __builtin_amdgcn_s_sleep(DLY_B);
  o_gemv<true>([&]() { q_gemv1(wqf, v4u{0x3f803f80u, 0x3f803f80u, 0x3f803f80u, 0x3f803f80u}, L, lane, L_PL + w * 544 + (8 * c + 4 * (g & 1)) * 4); },
               [&]() { if (w == 2 || w == 3) q_gemv2<544>(L, lane, L_PL + (lane & 3) * 544 + ((w - 2) * 16 + (lane >> 2)) * 16, rs, qpub - WS_QB + WS_CS - 128 * 4, tag); }, wof, wor, L, tid, lane, rs, g_oo, g_so, o_xw, o_xr, o_wr, o_rd, o_pub, tag, T0, budget);
  u32 cs0, cs1;
  const int cso = WS_CS + (b * 8 + kvh) * 4096 + tid * 4;
  float mprev, csum;
  {
    const f4 r = mixer<DLY_CD>([&]() { cs0 = ld32s(rs, cso); cs1 = ld32s(rs, cso + 2048); }, xres, L, tid, w, lane, j, rs, WS_PB + b * 8 * 2048, tag, b2, budget, mprev);
    cs0 ^= tag; cs1 ^= tag;
    if (__builtin_expect(!__all(((cs0 | cs1) & TMASK) == 0), 0)) {
      do { CBAR(); cs0 = ld32s(rs, cso) ^ tag; cs1 = ld32s(rs, cso + 2048) ^ tag; } while (!__all(((cs0 | cs1) & TMASK) == 0) && --budget > 0);
    }
    csum = asf(cs0) + asf(cs1);
    mprev += b2;
    PIN2(csum, mprev);
    if (tid < XBS / 16) { v4u o = {tagf(r[0], tag), tagf(r[1], tag), tagf(r[2], tag), tagf(r[3], tag)}; st128s(o, rs, WS_XB + (b * 64 + j) * XBS + tid * 16); }
  }
  float rstd2, dmean;
  int qo2 = qo + 4 * 8 * 4096, b0 = a_qwr, b1 = a_qrd, b2_ = a_mwr, b3 = a_mrd, b4 = a_pwr, b5 = a_lwr, pr = L_PL + lane * 16, po2 = WS_OB + (1 << 19) + pub_o, ps2 = WS_OS + 32768 + pub_s;
  PIN4(qo2, b0, b1, b2_); PIN4(b3, b4, b5, pr); PIN2(po2, ps2);
  {
    int xo_ = WS_XB + (b * 64 + (tid >> 3)) * XBS + (tid & 7) * 4, ao = WS_XB + (b * 64 + 32 * kh + 4 * w + g) * XBS + (c & 1) * 16, qw = qwr, qr = qrd, qp = qpub + 4 * 8 * 4096;
    PIN4(xo_, ao, qw, qr); PIN1(qp);
    if (DLY_E) __builtin_amdgcn_s_sleep(DLY_E);
    v4u a0 = {tag, tag, tag, tag};
    u32 xv;
#define EBAD() !__all((((a0[0] | a0[1] | a0[2]) | (a0[3] | xv)) & TMASK) == 0)
    CBAR();
    if (c < 2) a0 = ld128s(rs, ao);
    xv = ld32s(rs, xo_) ^ tag;
#pragma unroll
    for (int i = 0; i < 4; ++i) a0[i] ^= tag;
    if (__builtin_expect(EBAD(), 0)) {
      do {
        CBAR();
        a0 = v4u{tag, tag, tag, tag};
        if (c < 2) a0 = ld128s(rs, ao);
        xv = ld32s(rs, xo_) ^ tag;
#pragma unroll
        for (int i = 0; i < 4; ++i) a0[i] ^= tag;
      } while (EBAD() && --budget > 0);
    }
    const u32 p0 = cvtpk(asf(a0[0]) - mprev, asf(a0[1]) - mprev), p1 = cvtpk(asf(a0[2]) - mprev, asf(a0[3]) - mprev);
    const v4u qa2 = {rowbc<0>(p0), rowbc<0>(p1), rowbc<1>(p0), rowbc<1>(p1)};
    q_gemv(wqf, qa2, L, tid, lane, qw, qr, rs, qp, tag);
    if (DLY_A) __builtin_amdgcn_s_sleep(DLY_A);
    CBAR();
    q0 = ld32s(rs, qo2); q1 = ld32s(rs, qo2 + 2048);
    CBAR();
    xres = asf(xv);
    float mean, var;
    ln_stats(xres, L, w, lane, mean, var);
    rstd2 = __builtin_amdgcn_rsqf(var + EPS);
    dmean = (mean - mprev) * csum;
    PIN2(rstd2, dmean);
  }
  {
    v4u vf[16];
#pragma unroll
    for (int t = 0; t < 4; ++t)
#pragma unroll
      for (int u = 0; u < 4; ++u) vf[t * 4 + u] = v_frag(t, u, L, vbase);
    PIN8(vf); { v4u* v2 = vf + 8; PIN8(v2); }
    q0 ^= tag; q1 ^= tag;
    if (__builtin_expect(!__all(((q0 | q1) & TMASK) == 0), 0)) {
      do { CBAR(); q0 = ld32s(rs, qo2) ^ tag; q1 = ld32s(rs, qo2 + 2048) ^ tag; } while (!__all(((q0 | q1) & TMASK) == 0) && --budget > 0);
    }
    M = attn_scores(rstd2 * ((asf(q0) + asf(q1)) - dmean), kf, L, b0, b1, b2_, b3, b4, b5, lane);
    BAR();
    v4u pf[4];
#pragma unroll
    for (int t = 0; t < 4; ++t) pf[t] = *(const LAS v4u*)(L + pr + t * PSTR);
    SB();
#pragma unroll
    for (int t = 0; t < 4; ++t)
#pragma unroll
      for (int u = 0; u < 4; ++u) acc[u] = mfma(pf[t], vf[t * 4 + u], t == 0 ? f4{0.f, 0.f, 0.f, 0.f} : acc[u]);
    attn_publish_stats(M, L, w, lane, rs, ps2, tag, T0);
    attn_publish(acc, lane, rs, po2, T0);
  }
  g_oo += 1 << 19; g_so += 32768; o_pub += 65536;
  PIN2(g_oo, g_so); PIN1(o_pub);
  if (DLY_B2) __builtin_amdgcn_s_sleep(DLY_B2);
  o_gemv<false>([]() {}, []() {}, wof, wor, L, tid, lane, rs, g_oo, g_so, o_xw, o_xr, o_wr, o_rd, o_pub, tag, T0, budget);
  {
    const f4 r = mixer<DLY_CD2>([]() {}, xres, L, tid, w, lane, j, rs, WS_PB + 65536 + b * 8 * 2048, tag, b2, budget, mprev);
    if (w == 0) {
      const u32 lo = cvtpk(r[0], r[1]), hi = cvtpk(r[2], r[3]);
      if (lane < 2) { v2u o = {lo, hi}; *(GAS v2u*)((GAS unsigned char*)p.out + (b * 512 + 8 * j + 4 * lane) * 2) = o; }
      if (lane == 0 && j == 0) __builtin_amdgcn_raw_buffer_store_b32(T0, rs, WS_EP + b * 256, 0, 16);
    }
  }
}

extern "C" void kernel_launch(void* const* d_in, const int* in_sizes, int n_in, void* d_out, int out_size, void* d_ws, size_t ws_size, hipStream_t stream) {
  Params p;
  p.x = (const u16*)d_in[0]; p.K = (const unsigned char*)d_in[1]; p.V = (const unsigned char*)d_in[2];
  p.wq = (const unsigned char*)d_in[3]; p.wo = (const unsigned char*)d_in[4];
  p.w1 = (const u16*)d_in[6]; p.b1 = (const u16*)d_in[7]; p.w2 = (const u16*)d_in[8]; p.b2 = (const u16*)d_in[9];
  p.out = (u16*)d_out; p.ws = (unsigned char*)d_ws;
  hipLaunchKernelGGL(fused_decode_kernel, dim3(256), dim3(512), 0, stream, p);
}
```
